```python
import torch

def _softmax(x, axis=-1):
    return torch.softmax(x, dim=axis)
def _gelu(x):
    return torch.nn.functional.gelu(x, approximate="tanh")
def _silu(x):
    return torch.nn.functional.silu(x)
def _sigmoid(x):
    return torch.sigmoid(x)
def _layernorm(x, axis=-1, eps=1e-5):
    m = x.mean(dim=axis, keepdim=True)
    v = x.var(dim=axis, keepdim=True, unbiased=False)
    return (x - m) / torch.sqrt(v + eps)
def _scatter_add(tgt, idx, src, axis=0):
    return tgt.clone().index_add_(axis, idx, src)
def _segment_sum(src, seg, n):
    out = torch.zeros((n,) + src.shape[1:], dtype=src.dtype)
    return out.index_add_(0, seg, src)
def _pad(x, pw):
    flat = tuple(v for pair in reversed(pw) for v in pair)
    return torch.nn.functional.pad(x, flat)

kernel_name = 'gated_mix__linear_attn_block__sliding_local_dilated__prompt'

def setup_inputs():

    _w = (lambda a: (lambda t: t.to(torch.bfloat16) if t.is_floating_point() else t)(torch.as_tensor(a)))
    def _A_setup():
        B, S, D = 1, 2048, 640
        return {
            "x": _w((lambda *s: torch.randn(*s, dtype=torch.bfloat16) * 0.05)(B, S, D)),
            "wqkv": _w((lambda *s: torch.randn(*s, dtype=torch.bfloat16) * 1.6)(D, 3 * D)),
            "wo": _w((lambda *s: torch.randn(*s, dtype=torch.bfloat16) * 0.8)(D, D)),
        }
    _A_in = _A_setup()

    def _B_setup():
        B, S, D = 1, 2048, 640
        return {
            "x": _w((lambda *s: torch.randn(*s, dtype=torch.bfloat16) * 0.05)(B, S, D)),
            "wqkv": _w((lambda *s: torch.randn(*s, dtype=torch.bfloat16) * 0.8)(D, 3 * D)),
            "wo": _w((lambda *s: torch.randn(*s, dtype=torch.bfloat16) * 0.05)(D, D)),
        }
    _B_in = _B_setup()

    return {
        "A_x": _A_in["x"],
        "A_wqkv": _A_in["wqkv"],
        "A_wo": _A_in["wo"],
        "B_x": _B_in["x"],
        "B_wqkv": _B_in["wqkv"],
        "B_wo": _B_in["wo"],
        "gate_w": _w((lambda *s: torch.randn(*s, dtype=torch.bfloat16) * 0.05)(640) * 16.0),
    }

def reference(A_x, A_wqkv, A_wo, B_x, B_wqkv, B_wo, gate_w):
    def _A_ref(x, wqkv, wo):
        B, S, D = x.shape
        H = 32; Dh = D // H
        qkv = (lambda x, *d: x.permute(*d))((x @ wqkv).reshape(B, S, 3, H, Dh), 2, 0, 3, 1, 4)
        q, k, v = qkv[0], qkv[1], qkv[2]
        phi = lambda t: torch.where(t > 0, t + 1, torch.exp(t)) + 1e-6
        pq, pk = phi(q), phi(k)
        kv = torch.einsum("bhsd,bhse->bhde", pk, v)
        z = torch.einsum("bhsd->bhd", pk)
        num = torch.einsum("bhqd,bhde->bhqe", pq, kv)
        den = torch.einsum("bhqd,bhd->bhq", pq, z)[..., None] + 1e-6
        o = num / den
        return (lambda x, *d: x.permute(*d))(o, 0, 2, 1, 3).reshape(B, S, D) @ wo

    def _B_ref(x, wqkv, wo):
        B, S, D = x.shape
        H = 32; Dh = D // H
        W, dil = 8, 2
        qkv = (lambda x, *d: x.permute(*d))((x @ wqkv).reshape(B, S, 3, H, Dh), 2, 0, 3, 1, 4)
        q, k, v = qkv[0], qkv[1], qkv[2]
        scale = Dh ** -0.5
        pos = torch.arange(S)
        off = torch.arange(W) * dil
        idx = pos[:, None] - off[None, :]
        mask = idx >= 0
        idx_c = torch.where(mask, idx, 0).reshape(-1)
        kg = k[:, :, idx_c, :].reshape(B, H, S, W, Dh)
        vg = v[:, :, idx_c, :].reshape(B, H, S, W, Dh)
        logits = torch.einsum("bhsd,bhswd->bhsw", q, kg) * scale
        logits = torch.where(mask[None, None, :, :], logits, -1e9)
        attn = _softmax(logits, axis=-1)
        o = torch.einsum("bhsw,bhswd->bhsd", attn, vg)
        return (lambda x, *d: x.permute(*d))(o, 0, 2, 1, 3).reshape(B, S, D) @ wo

    x = A_x
    g = _sigmoid((x * gate_w).sum(-1, keepdims=True))
    a = _A_ref(x, A_wqkv, A_wo)
    b = _B_ref(x, B_wqkv, B_wo)
    return g * a + (1 - g) * b
```

```cpp
#include <hip/hip_runtime.h>
#include <hip/hip_bf16.h>
typedef __hip_bfloat16 bf16;
constexpr int S=2048, D=640, H=32, DH=20;
__device__ inline float b2f(bf16 x){return __bfloat162float(x);}
__device__ inline float rb(float x){return __bfloat162float(__float2bfloat16(x));}
__global__ void k_qkv(const bf16* x, const bf16* wa, const bf16* wb, float* qkv){
  int n = blockIdx.x*256+threadIdx.x;
  int s = blockIdx.y;
  const bf16* w = n<1920? wa+n : wb+(n-1920);
  float acc=0;
  for(int k=0;k<D;k++) acc += b2f(x[s*D+k])*b2f(w[k*1920]);
  qkv[s*3840+n]=rb(acc);
}
__device__ inline float phi(float t){ return (t>0? t+1.f : __expf(t)) + 1e-6f; }
__global__ void k_kv(const float* qkv, float* kv){
  int h=blockIdx.x; int t=threadIdx.x; if(t>=420) return;
  int d=t/21, e=t%21;
  float acc=0;
  for(int s=0;s<S;s++){
    float pk=phi(qkv[s*3840+640+h*20+d]);
    float v = e<20? qkv[s*3840+1280+h*20+e] : 1.f;
    acc+=pk*v;
  }
  kv[h*420+t]=acc;
}
__global__ void k_o(const float* qkv, const float* kv, const bf16* x, const bf16* gw, float* o){
  int s=blockIdx.x; int t=threadIdx.x;
  float g=0; for(int k=0;k<D;k++) g+= b2f(x[s*D+k])*b2f(gw[k]);
  g = 1.f/(1.f+__expf(-g));
  if(t<32){
    int h=t; float pq[20]; for(int d=0;d<20;d++) pq[d]=phi(qkv[s*3840+h*20+d]);
    float den=1e-6f; for(int d=0;d<20;d++) den+=pq[d]*kv[h*420+d*21+20];
    for(int e=0;e<20;e++){ float num=0; for(int d=0;d<20;d++) num+=pq[d]*kv[h*420+d*21+e]; o[s*1280+h*20+e]=g*num/den; }
  } else {
    int h=t-32; float l[8]; float m=-1e30f;
    for(int w=0;w<8;w++){ int j=s-2*w; float a=-1e9f; if(j>=0){ a=0; for(int d=0;d<20;d++) a+=qkv[s*3840+1920+h*20+d]*qkv[j*3840+1920+640+h*20+d]; a*=0.22360679775f;} l[w]=a; m=fmaxf(m,a);}
    float sum=0; for(int w=0;w<8;w++){ l[w]=__expf(l[w]-m); sum+=l[w]; }
    for(int e=0;e<20;e++){ float a=0; for(int w=0;w<8;w++){ int j=s-2*w; if(j>=0) a+=l[w]*qkv[j*3840+1920+1280+h*20+e]; } o[s*1280+640+h*20+e]=(1.f-g)*a/sum; }
  }
}
__global__ void k_out(const float* o, const bf16* woa, const bf16* wob, bf16* out){
  int n=blockIdx.x*128+threadIdx.x; int s=blockIdx.y;
  float acc=0;
  for(int k=0;k<D;k++) acc+=o[s*1280+k]*b2f(woa[k*D+n]);
  for(int k=0;k<D;k++) acc+=o[s*1280+640+k]*b2f(wob[k*D+n]);
  out[s*D+n]=__float2bfloat16(acc);
}
extern "C" void kernel_launch(void* const* d_in, const int* in_sizes, int n_in, void* d_out, int out_size, void* d_ws, size_t ws_size, hipStream_t stream){
  const bf16* x=(const bf16*)d_in[0]; const bf16* wa=(const bf16*)d_in[1]; const bf16* woa=(const bf16*)d_in[2];
  const bf16* wb=(const bf16*)d_in[4]; const bf16* wob=(const bf16*)d_in[5]; const bf16* gw=(const bf16*)d_in[6];
  float* qkv=(float*)d_ws; float* kv=qkv+S*3840; float* o=kv+32*420+64;
  k_qkv<<<dim3(15,S),256,0,stream>>>(x,wa,wb,qkv);
  k_kv<<<32,448,0,stream>>>(qkv,kv);
  k_o<<<S,64,0,stream>>>(qkv,kv,x,gw,o);
  k_out<<<dim3(5,S),128,0,stream>>>(o,woa,wob,(bf16*)d_out);
}
```

```cpp
#include <hip/hip_runtime.h>
#include <hip/hip_bf16.h>

typedef unsigned long long u64;
typedef unsigned u32;
typedef u32 u32x2 __attribute__((ext_vector_type(2)));
typedef u32 u32x4 __attribute__((ext_vector_type(4)));
typedef float f32x2 __attribute__((ext_vector_type(2)));
typedef float f32x4 __attribute__((ext_vector_type(4)));
typedef short s16x4 __attribute__((ext_vector_type(4)));
typedef __bf16 bf16x2 __attribute__((ext_vector_type(2)));
typedef __bf16 bf16x8 __attribute__((ext_vector_type(8)));
#define LAS __attribute__((address_space(3)))
#define GAS __attribute__((address_space(1)))
#define DEV __device__ __forceinline__
#define SB() __builtin_amdgcn_sched_barrier(0)
#define RLX_AGENT __ATOMIC_RELAXED, __HIP_MEMORY_SCOPE_AGENT

#define STAMP_DECL
#define STAMP(i)
#define STAMP_OUT

constexpr u32 WS_EPOCH = 0;
constexpr u32 WS_KVFLAG = 4096;
constexpr u32 WS_BFLAG = WS_KVFLAG + 512 * 8;
constexpr u32 WS_AFLAG = WS_BFLAG + 512 * 8;
constexpr u32 WS_GATE = 32768;
constexpr u32 WS_KVP = WS_GATE + 8 * 128 * 64;
constexpr u32 KVP_STRIDE = 1792;
constexpr u32 WS_O = WS_KVP + 512 * KVP_STRIDE;
constexpr u32 WS_END = WS_O + 128 * 40960;
constexpr u64 TAG_STEP = 0x9E3779B97F4A7C15ull;
constexpr u32 SPIN_CAP = 1u << 18;

constexpr u32 L_RING = 40960;
constexpr u32 SEG = 5136;
constexpr u32 BX_SLOT = 18432, BW_SLOT = 15424, L_BX = L_RING, L_BW = L_BX + 3 * BX_SLOT;
constexpr u32 AX_SLOT = 16384, AW_SLOT = 10272, L_AX = L_RING, L_AW = L_AX + 3 * AX_SLOT;
constexpr u32 L_MISC = L_BW + 4 * BW_SLOT;
constexpr u32 L_HALOK = L_MISC, L_HALOV = L_MISC + 2560;
constexpr u32 L_GATEW = L_MISC, L_TAB = L_MISC + 1280, TAB_STRIDE = 848;
constexpr u32 L_CTL = L_MISC + 5120;
constexpr u32 LDS_BYTES = 163840;
static_assert(L_TAB + 4 * TAB_STRIDE <= L_CTL && L_CTL + 256 <= LDS_BYTES, "lds map");

template <int N> DEV void waitv() { asm volatile("s_waitcnt vmcnt(%0)" ::"i"(N) : "memory"); }
DEV void waitl0() { asm volatile("s_waitcnt lgkmcnt(0)" ::: "memory"); }
DEV void bar() { asm volatile("s_barrier" ::: "memory"); }
DEV void glds(const char* sbase, u32 voff, u32 lds) {
  asm volatile("s_mov_b32 m0, %2\n\ts_nop 0\n\tglobal_load_lds_dwordx4 %0, %1" ::"v"(voff), "s"(sbase), "s"(lds));
}
DEV void glds4(const char* sbase, u32 v0, u32 v1, u32 v2, u32 v3, u32 lds) {
  asm volatile("s_mov_b32 m0, %5\n\ts_nop 0\n\tglobal_load_lds_dwordx4 %0, %4\n\tglobal_load_lds_dwordx4 %1, %4 offset:1024\n\t"
               "global_load_lds_dwordx4 %2, %4 offset:2048\n\tglobal_load_lds_dwordx4 %3, %4 offset:3072"
               ::"v"(v0), "v"(v1), "v"(v2), "v"(v3), "s"(sbase), "s"(lds));
}
DEV void glds5(const char* sbase, const u32 (&v)[5], u32 lds) { glds4(sbase, v[0], v[1], v[2], v[3], lds); glds(sbase, v[4], lds + 4096); }
DEV u32x4 ld128(u32 a) { return *(LAS u32x4*)(size_t)a; }
DEV u32x2 ld64(u32 a) { return *(LAS u32x2*)(size_t)a; }
DEV void st64(u32 a, u32x2 v) { *(LAS u32x2*)(size_t)a = v; }
DEV u32x2 ldtr(u32 a) { return __builtin_bit_cast(u32x2, __builtin_amdgcn_ds_read_tr16_b64_v4i16((LAS s16x4*)(size_t)a)); }
DEV u32x4 cat(u32x2 a, u32x2 b) { return __builtin_shufflevector(a, b, 0, 1, 2, 3); }
DEV u32x4 ldB(u32 a0, u32 a1) { return cat(ldtr(a0), ldtr(a1)); }
DEV f32x4 mma(u32x4 a, u32x4 b, f32x4 c) {
  return __builtin_amdgcn_mfma_f32_16x16x32_bf16(__builtin_bit_cast(bf16x8, a), __builtin_bit_cast(bf16x8, b), c, 0, 0, 0);
}
DEV u32 pk2(float a, float b) { f32x2 v = {a, b}; return __builtin_bit_cast(u32, __builtin_convertvector(v, bf16x2)); }
DEV u32x2 pk4(f32x4 v) { return u32x2{pk2(v[0], v[1]), pk2(v[2], v[3])}; }
DEV float phi1(float t) {
  float e = __builtin_amdgcn_fmed3f(__builtin_amdgcn_exp2f(t * 1.44269504089f), 0.f, 1.f);
  return __builtin_fmaxf(t, 0.f) + (e + 1e-6f);
}
DEV f32x4 phi4(f32x4 v) { return f32x4{phi1(v[0]), phi1(v[1]), phi1(v[2]), phi1(v[3])}; }
DEV float xmax(float v) {
  u32 u = __builtin_bit_cast(u32, v);
  auto r = __builtin_amdgcn_permlane16_swap(u, u, false, false);
  v = __builtin_fmaxf(__builtin_bit_cast(float, (u32)r[0]), __builtin_bit_cast(float, (u32)r[1]));
  u = __builtin_bit_cast(u32, v);
  auto s = __builtin_amdgcn_permlane32_swap(u, u, false, false);
  return __builtin_fmaxf(__builtin_bit_cast(float, (u32)s[0]), __builtin_bit_cast(float, (u32)s[1]));
}
DEV float xsum(float v) {
  u32 u = __builtin_bit_cast(u32, v);
  auto r = __builtin_amdgcn_permlane16_swap(u, u, false, false);
  v = __builtin_bit_cast(float, (u32)r[0]) + __builtin_bit_cast(float, (u32)r[1]);
  u = __builtin_bit_cast(u32, v);
  auto s = __builtin_amdgcn_permlane32_swap(u, u, false, false);
  return __builtin_bit_cast(float, (u32)s[0]) + __builtin_bit_cast(float, (u32)s[1]);
}
DEV u32 odd_rows(u32 x) { auto r = __builtin_amdgcn_permlane16_swap(x, x, false, false); return r[1]; }

typedef __amdgpu_buffer_rsrc_t rsrc_t;
DEV u32x4 gld128(rsrc_t r, u32 off) { return __builtin_amdgcn_raw_buffer_load_b128(r, off, 0, 16); }
DEV u32 gld32(rsrc_t r, u32 off) { return __builtin_amdgcn_raw_buffer_load_b32(r, off, 0, 16); }
DEV void gst128(rsrc_t r, u32 off, u32x4 v) { __builtin_amdgcn_raw_buffer_store_b128(v, r, off, 0, 16); }
DEV void gst64(rsrc_t r, u32 off, u32x2 v) { __builtin_amdgcn_raw_buffer_store_b64(v, r, off, 0, 16); }
DEV void gst32(rsrc_t r, u32 off, u32 v) { __builtin_amdgcn_raw_buffer_store_b32(v, r, off, 0, 16); }
DEV void publish(char* ws, u32 off, u64 tag, int lane) {
  waitv<0>();
  if (lane == 0) __hip_atomic_store((GAS u64*)(ws + off), tag, RLX_AGENT);
}
DEV void await(char* ws, u32 off, u64 tag) {
  for (u32 spins = 0; spins < SPIN_CAP; ++spins) {
    u64 v = __hip_atomic_load((GAS u64*)(ws + off), RLX_AGENT);
    if (__all(v == tag)) break;
    __builtin_amdgcn_s_sleep(1);
  }
  asm volatile("" ::: "memory");
}
DEV void lds_arrive(u32 a, int lane) {
  if (lane == 0) __hip_atomic_fetch_add((LAS u32*)(size_t)a, 1u, __ATOMIC_RELAXED, __HIP_MEMORY_SCOPE_WORKGROUP);
}
DEV void lds_await(u32 a, u32 want) {
  asm volatile("" ::: "memory");
  for (u32 spins = 0; spins < (SPIN_CAP << 4); ++spins) {
    u32 v = __hip_atomic_load((LAS u32*)(size_t)a, __ATOMIC_RELAXED, __HIP_MEMORY_SCOPE_WORKGROUP);
    if (__builtin_amdgcn_readfirstlane(v) >= want) break;
  }
  asm volatile("" ::: "memory");
}

DEV void wo_offsets(int lane, u32 (&om)[5], u32 (&os)[5]) {
#pragma unroll
  for (int i = 0; i < 5; ++i) {
    u32 c = i * 64 + lane, pos = (c * 6554u) >> 16, o = c - pos * 10;
    om[i] = (pos + 4 * (pos >> 4)) * 1280 + o * 16 - (i & 3) * 1024 * (i < 4);
    os[i] = (20 * (2 * ((pos >> 2) & 3) + (pos >> 4)) + 16 + (pos & 3)) * 1280 + o * 16 - (i & 3) * 1024 * (i < 4);
  }
}
DEV void wo_issue(const char* wo, int ks, u32 lds, const u32 (&om)[5], const u32 (&os)[5]) {
  int sg = (ks * 52) >> 8, n = ks - sg * 5;
  if (n < 4) glds5(wo + (sg * 160 + n * 40) * 1280, om, lds);
  else glds5(wo + sg * 160 * 1280, os, lds);
}

__global__ __launch_bounds__(512, 2) void fused_kernel(const char* __restrict__ X, const char* __restrict__ WA, const char* __restrict__ WoA,
                                                       const char* __restrict__ WB, const char* __restrict__ WoB, const char* __restrict__ GW,
                                                       char* out, char* ws) {
  extern __shared__ __attribute__((aligned(16))) char smem[];
  const u32 lds0 = (u32)(size_t)(LAS char*)smem;
  const int b = blockIdx.x, xcd = b & 7, jj = b >> 3;
  const int rt = (xcd & 1) * 8 + (jj & 7), ct = (xcd >> 1) * 4 + (jj >> 3);
  const int r0t = ((jj & 7) * 5 + (jj >> 3) * 10) >> 2, r0 = r0t >= 10 ? r0t - 10 : r0t;
  const bool isB = ct >= 8;
  const int hg = ct & 7;
  const int wave = __builtin_amdgcn_readfirstlane(threadIdx.x >> 6), lane = threadIdx.x & 63;
  const int g = lane >> 4, i16 = lane & 15, q4 = (lane >> 2) & 3, p4 = lane & 3;
  const rsrc_t wsr = __builtin_amdgcn_make_buffer_rsrc(ws, 0, WS_END, 0x00020000);
  const u32 aA0 = i16 * 128 + ((g ^ ((i16 >> 1) & 7)) << 4);
  const u32 rB0 = (8 * g + (q4 ^ (4 * (g & 1)))) * 160, rB1 = (8 * g + ((4 + q4) ^ (4 * (g & 1)))) * 160;
  const int rg = xcd * 4 + (jj >> 3), ctile = jj & 7;
  u64 tag;
  STAMP_DECL
  STAMP(0);

  if (wave >= 4) {
    const int lw = wave - 4;
    u32 ow[5];
#pragma unroll
    for (int i = 0; i < 5; ++i) {
      u32 c = i * 64 + lane, pos = (c * 6554u) >> 16, o = c - pos * 10;
      ow[i] = (pos ^ (((pos >> 3) & 1) << 2)) * 3840 + o * 16 - (i & 3) * 1024 * (i < 4);
    }
    const u32 ox0 = (lane >> 3) * 2560 + (((lane & 7) ^ (lane >> 4)) << 4), ox1 = (ox0 ^ 64) + 20480 - 1024;
    const u32 ox2 = ox0 + 2 * (20480 - 1024), ox3 = ox1 + 2 * (20480 - 1024);
    const char* Wt = (isB ? WB : WA) + hg * 160;
    auto ksof = [&](int s) { int k = s + r0; return k >= 10 ? k - 10 : k; };
    auto xquad = [&](int s, int qd, u32 slotbase) {
      glds4(X + (128 * rt + 64 * (qd & 1) + (qd >> 1)) * 1280 + ksof(s) * 128, ox0, ox1, ox2, ox3, slotbase + qd * 4096);
    };
    auto wseg = [&](int u, int seg, u32 dst) { glds5(Wt + (ksof(u >> 1) * 64 + (u & 1) * 32) * 3840 + seg * 1280, ow, dst); };
    if (isB) {
      auto xhalf = [&](int n) {
        const int s = n >> 1; const u32 sbs = lds0 + L_BX + (s % 3) * BX_SLOT;
        if (lw == 3) { xquad(s, (n & 1) * 2, sbs); xquad(s, (n & 1) * 2 + 1, sbs); }
        if (lw == 0) glds(X + (rt ? 128 * rt - 16 + (n & 1) : 0) * 1280 + ksof(s) * 128, ox0 ^ ((n & 1) << 6), sbs + 16384 + (n & 1) * 1024);
      };
      auto wait_unit = [&]() { if (lw == 0) waitv<6>(); else if (lw == 3) waitv<8>(); else waitv<5>(); };
      auto unit = [&](int u) {
        if (lw < 3) wseg(u, lw, lds0 + L_BW + (u & 3) * BW_SLOT + lw * SEG);
        if (u < 19) xhalf(u + 1);
      };
      xhalf(0); unit(0); unit(1);
      const u32 cH = (1 + (lw >> 1)) * SEG + (lw & 1) * 96 + p4 * 8;
      f32x4 h[3] = {};
      wait_unit(); bar();
#pragma nounroll
      for (int u = 0; u < 20; ++u) {
        const u32 xa = lds0 + L_BX + ((u >> 1) % 3) * BX_SLOT + 8 * 2048 + (aA0 ^ ((u & 1) << 6));
        const u32 wb = lds0 + L_BW + (u & 3) * BW_SLOT + cH;
        u32x4 ah = ld128(xa), f0 = ldB(wb + rB0, wb + rB1), f1 = ldB(wb + rB0 + 32, wb + rB1 + 32), f2 = ldB(wb + rB0 + 64, wb + rB1 + 64);
        SB();
        if (u + 2 < 20) unit(u + 2);
        SB();
        if (lw < 2) { h[0] = mma(f0, ah, h[0]); h[1] = mma(f1, ah, h[1]); if (!(lw & 1)) h[2] = mma(f2, ah, h[2]); }
        else { h[0] = mma(ah, f0, h[0]); h[1] = mma(ah, f1, h[1]); if (!(lw & 1)) h[2] = mma(ah, f2, h[2]); }
        SB();
        waitl0();
        if (u + 2 < 19) wait_unit(); else if (u + 2 == 19 && lw < 3) waitv<5>(); else waitv<0>();
        bar();
      }
      const int f0i = (lw & 1) * 3;
#pragma unroll
      for (int f = 0; f < 3; ++f) {
        if (f < 2 || !(lw & 1)) {
          u32 a = lw < 2 ? L_HALOK + (((f0i + f) * 4 + g) * 16 + i16) * 8 : L_HALOV + (((f0i + f) * 16 + i16) * 4 + g) * 8;
          st64(lds0 + a, pk4(h[f]));
        }
      }
      waitl0(); bar();
      tag = *(LAS u64*)(size_t)(lds0 + L_CTL);
    } else {
      auto xhalf = [&](int n) { xquad(n >> 1, (n & 1) * 2 + (lw & 1), lds0 + L_AX + ((n >> 1) % 3) * AX_SLOT); };
      auto unit = [&](int u) {
        if (lw < 2) wseg(u, 1 + lw, lds0 + L_AW + (u & 3) * AW_SLOT + lw * SEG);
        else if (u < 19) xhalf(u + 1);
      };
      if (lw == 1) { glds(GW, lane * 16, lds0 + L_GATEW); if (lane < 16) glds(GW, 1024 + lane * 16, lds0 + L_GATEW + 1024); }
      if (lw >= 2) xhalf(0);
      unit(0); unit(1);
      u32x4 xr[20][2];
      f32x4 gl[2] = {};
      if (lw < 2) waitv<5>(); else waitv<4>();
      bar();
#pragma unroll
      for (int u = 0; u < 20; ++u) {
        const u32 xa = lds0 + L_AX + ((u >> 1) % 3) * AX_SLOT + lw * 4096 + (aA0 ^ ((u & 1) << 6));
        xr[u][0] = ld128(xa); xr[u][1] = ld128(xa + 2048);
        u32x4 gw = ld128(lds0 + L_GATEW + ksof(u >> 1) * 128 + (u & 1) * 64 + g * 16);
        SB();
        if (u + 2 < 20) unit(u + 2); else wseg((u - 18) * 4 + lw, 0, lds0 + ((u - 18) * 4 + lw) * 5120);
        SB();
        gl[0] = mma(gw, xr[u][0], gl[0]); gl[1] = mma(gw, xr[u][1], gl[1]);
        SB();
        waitl0();
        if (u + 2 < 19) { if (lw < 2) waitv<5>(); else waitv<4>(); }
        else if (u + 2 == 19) { if (lw < 2) waitv<5>(); else waitv<0>(); }
        else waitv<5>();
        bar();
      }
      STAMP(1);
#pragma unroll
      for (int r = 2; r < 5; ++r) wseg(r * 4 + lw, 0, lds0 + (r * 4 + lw) * 5120);
      f32x4 qa[2][5] = {};
      const u32 cQ = p4 * 8, cQ5 = 32 + p4 * 40;
#pragma unroll
      for (int r = 0; r < 5; ++r) {
        if (r > 0) {
          if (r == 1) waitv<15>(); else if (r == 2) waitv<10>(); else if (r == 3) waitv<5>(); else waitv<0>();
          lds_arrive(lds0 + L_CTL + 16 + r * 4, lane);
          lds_await(lds0 + L_CTL + 16 + r * 4, 4);
        }
#pragma unroll
        for (int n = 0; n < 4; ++n) {
          const int u = r * 4 + n; const u32 wb = lds0 + u * 5120;
#pragma unroll
          for (int f = 0; f < 5; ++f) {
            const u32 c = f < 4 ? cQ + f * 40 : cQ5;
            u32x4 w = ldB(wb + rB0 + c, wb + rB1 + c);
            qa[0][f] = mma(w, xr[u][0], qa[0][f]); qa[1][f] = mma(w, xr[u][1], qa[1][f]);
          }
        }
      }
      STAMP(2);
      u32x2 pq[2][5];
#pragma unroll
      for (int r = 0; r < 2; ++r)
#pragma unroll
        for (int f = 0; f < 5; ++f) pq[r][f] = pk4(phi4(qa[r][f]));
      float gate[2];
#pragma unroll
      for (int r = 0; r < 2; ++r) gate[r] = __builtin_amdgcn_rcpf(1.f + __builtin_amdgcn_exp2f(gl[r][0] * -1.44269504089f));
      waitl0(); bar();
      STAMP(3);
      tag = *(LAS u64*)(size_t)(lds0 + L_CTL);
      f32x4 n5[2] = {};
      u32x2 ob[2][4];
      float sc5[2] = {0.f, 0.f};
#pragma unroll
      for (int h = 0; h < 4; ++h) {
        const u32 tb = lds0 + L_TAB + h * TAB_STRIDE;
        const bool gh = g == h, eh = q4 == (h ^ 1);
        u32x4 a0 = cat(ld64(tb + lane * 8), ld64(tb + (gh ? 64 + i16 : 105) * 8));
        u32x4 a5 = cat(ld64(tb + (eh ? 80 + p4 + 4 * g : 105) * 8), ld64(tb + (eh && gh ? 100 + p4 : 105) * 8));
        u32x4 az = cat(ld64(tb + (96 + g) * 8), ld64(tb + (gh ? 104 : 105) * 8));
#pragma unroll
        for (int r = 0; r < 2; ++r) {
          u32x4 bo = cat(pq[r][h], pq[r][4]);
          f32x4 num = mma(a0, bo, f32x4{0.f, 0.f, 0.f, 0.f});
          f32x4 den = mma(az, bo, f32x4{1e-6f, 1e-6f, 1e-6f, 1e-6f});
          n5[r] = mma(a5, bo, n5[r]);
          float sc = gate[r] * __builtin_amdgcn_rcpf(den[0]);
          ob[r][h] = pk4(num * sc);
          sc5[r] = (g ^ 1) == h ? sc : sc5[r];
        }
      }
#pragma unroll
      for (int r = 0; r < 2; ++r) {
        const u32 blk = rt * 8 + lw * 2 + r;
        const u32 base = WS_O + blk * 40960 + 20480 + (hg >> 1) * 5120;
        gst128(wsr, base + (hg & 1) * 2048 + lane * 16, cat(ob[r][0], ob[r][1]));
        gst128(wsr, base + (hg & 1) * 2048 + 1024 + lane * 16, cat(ob[r][2], ob[r][3]));
        gst64(wsr, base + 4096 + ((2 * (hg & 1) + (g >> 1)) * 16 + i16) * 16 + ((g ^ 1) & 1) * 8, pk4(n5[r] * sc5[r]));
        if (lane < 16) gst32(wsr, WS_GATE + (hg * 128 + blk) * 64 + lane * 4, __builtin_bit_cast(u32, gate[r]));
      }
      publish(ws, WS_AFLAG + ((rt * 4 + lw) * 8 + hg) * 8, tag, lane);
      STAMP(4);
    }
    u32 om[5], os[5];
    wo_offsets(lane, om, os);
    const char* wob = WoB + ctile * 160; const char* woa = WoA + ctile * 160;
#pragma nounroll
    for (int s = lw; s < 30; s += 4) {
      if (s < 20) wo_issue(wob, s, lds0 + s * 5120, om, os);
      else wo_issue(woa, s < 25 ? s - 20 : s - 15, lds0 + s * 5120, om, os);
    }
    STAMP(7);
    waitv<0>(); STAMP(8); bar();
    bar();
    STAMP(9);
#pragma nounroll
    for (int s = lw; s < 10; s += 4) wo_issue(woa, s < 5 ? s + 5 : s + 10, lds0 + s * 5120, om, os);
    waitv<0>(); STAMP(10); bar();
    bar();
    if (b == 0 && lw == 0) {
#pragma unroll
      for (int r = 0; r < 2; ++r) {
        const int n = r * 64 + lane;
        await(ws, WS_BFLAG + (((n >> 3) * 2 * 8 + (n & 7)) * 2) * 8, tag);
        await(ws, WS_AFLAG + (((n >> 3) * 4) * 8 + (n & 7)) * 8, tag);
      }
      if (lane == 0) __hip_atomic_store((GAS u64*)(ws + WS_EPOCH), tag, RLX_AGENT);
    }
    STAMP(13); STAMP_OUT;
    return;
  }

  if (wave == 0) {
    u64 e = __hip_atomic_load((GAS u64*)(ws + WS_EPOCH), RLX_AGENT);
    if (lane == 0) *(LAS u64*)(size_t)(lds0 + L_CTL) = e + TAG_STEP;
    if (lane < 8) *(LAS u32*)(size_t)(lds0 + L_CTL + 16 + lane * 4) = 0;
    waitl0();
  }
  if (isB) {
    const int par = wave & 1, hp = wave >> 1;
    const u32 cM = hp * 80 + p4 * 8, cX = hp * 80 + 32 + (p4 & 1) * SEG + (p4 >> 1) * 40, cY = 2 * SEG + hp * 80 + 32 + (p4 & 1) * 40;
    f32x4 acc[4][8];
#pragma unroll
    for (int r = 0; r < 4; ++r)
#pragma unroll
      for (int f = 0; f < 8; ++f) acc[r][f] = f32x4{0.f, 0.f, 0.f, 0.f};
    struct Fr { u32x4 a[4], w[8]; };
    auto rd = [&](Fr& F, u32 xs, int u, int kk) {
      const u32 aa = xs + (aA0 ^ (kk << 6)) + par * 8192;
      const u32 b0 = lds0 + L_BW + (u & 3) * BW_SLOT + rB0, b1 = lds0 + L_BW + (u & 3) * BW_SLOT + rB1;
#pragma unroll
      for (int r = 0; r < 4; ++r) F.a[r] = ld128(aa + r * 2048);
      F.w[0] = ldB(b0 + cM, b1 + cM); F.w[1] = ldB(b0 + cM + 40, b1 + cM + 40);
      F.w[2] = ldB(b0 + cM + SEG, b1 + cM + SEG); F.w[3] = ldB(b0 + cM + SEG + 40, b1 + cM + SEG + 40);
      F.w[4] = ldB(b0 + cX, b1 + cX);
      F.w[5] = ldB(b0 + cM + 2 * SEG, b1 + cM + 2 * SEG); F.w[6] = ldB(b0 + cM + 2 * SEG + 40, b1 + cM + 2 * SEG + 40);
      F.w[7] = ldB(b0 + cY, b1 + cY);
    };
    auto mm = [&](Fr& F, int f0, int f1) {
#pragma unroll
      for (int f = f0; f < f1; ++f)
#pragma unroll
        for (int r = 0; r < 4; ++r) acc[r][f] = f < 5 ? mma(F.w[f], F.a[r], acc[r][f]) : mma(F.a[r], F.w[f], acc[r][f]);
    };
    auto ilv = [&]() {
#pragma unroll
      for (int n = 0; n < 20; ++n) { __builtin_amdgcn_sched_group_barrier(0x8, 1, 0); __builtin_amdgcn_sched_group_barrier(0x100, 1, 0); }
      __builtin_amdgcn_sched_group_barrier(0x8, 4, 0);
    };
    Fr F0, F1;
    SB(); bar();
    STAMP(1);
    u32 xs = lds0 + L_BX;
    rd(F0, xs, 0, 0);
#pragma nounroll
    for (int s = 0; s < 10; ++s) {
      SB(); waitl0(); SB();
      mm(F0, 0, 2); SB();
      bar(); SB();
      rd(F1, xs, 2 * s + 1, 1); mm(F0, 2, 8); ilv(); SB();
      waitl0(); SB();
      mm(F1, 0, 2); SB();
      bar(); SB();
      xs = xs == lds0 + L_BX + 2 * BX_SLOT ? lds0 + L_BX : xs + BX_SLOT;
      rd(F0, xs, 2 * s + 2, 0); mm(F1, 2, 8); ilv();
      SB();
    }
    STAMP(2);
    u32x2 P[4][8];
#pragma unroll
    for (int r = 0; r < 4; ++r)
#pragma unroll
      for (int f = 0; f < 8; ++f) P[r][f] = pk4(acc[r][f]);
    bar();
    tag = *(LAS u64*)(size_t)(lds0 + L_CTL);
    u32x2 pKa, pKb, pK4, pVa, pVb, pY;
    {
      const u32 kt = lds0 + L_HALOK + ((i16 & 7) + 8 * par) * 8, vt = lds0 + L_HALOV + ((g & 1) + 2 * par) * 8;
      pKa = ld64(kt + (10 * hp + g) * 128); pKb = ld64(kt + (10 * hp + 5 + g) * 128); pK4 = ld64(kt + (10 * hp + 4 + 5 * (g >> 1)) * 128);
      pVa = ld64(vt + (40 * hp + i16) * 32); pVb = ld64(vt + (40 * hp + 20 + i16) * 32);
      pY = ld64(vt + (40 * hp + 16 + 20 * (q4 & 1) + p4) * 32);
    }
    f32x4 bc, bp;
    u32x2 mp;
#pragma unroll
    for (int j = 0; j < 4; ++j) {
      const int p = 4 * g + j;
      bc[j] = (p <= i16 && p >= i16 - 7) ? 0.f : -1e30f;
      bp[j] = p >= i16 + 9 ? 0.f : -1e30f;
    }
    mp[0] = (4 * g >= i16 + 9 ? 0xFFFFu : 0u) | (4 * g + 1 >= i16 + 9 ? 0xFFFF0000u : 0u);
    mp[1] = (4 * g + 2 >= i16 + 9 ? 0xFFFFu : 0u) | (4 * g + 3 >= i16 + 9 ? 0xFFFF0000u : 0u);
    const float cs = 0.22360679775f * 1.44269504089f;
    const u32x2 zero2 = {0u, 0u};
#pragma unroll
    for (int r = 0; r < 4; ++r) {
      const u32x2 K4 = {odd_rows(P[r][4][0]), odd_rows(P[r][4][1])};
      f32x4 bpr = bp;
      if (r == 0 && rt == 0) bpr = f32x4{-1e30f, -1e30f, -1e30f, -1e30f};
      u32x4 pb[2];
#pragma unroll
      for (int h = 0; h < 2; ++h) {
        const u32x4 qo = cat(P[r][h], g == 2 * h ? P[r][4] : zero2);
        f32x4 sc = mma(cat(P[r][2 + h], K4), qo, bc);
        f32x4 sp = mma(cat(h ? pKb : pKa, pK4), qo, bpr);
        f32x4 m;
#pragma unroll
        for (int j = 0; j < 4; ++j) m[j] = __builtin_fmaxf(sc[j], sp[j]);
        const float mx = xmax(__builtin_fmaxf(__builtin_fmaxf(m[0], m[1]), __builtin_fmaxf(m[2], m[3]))) * cs;
#pragma unroll
        for (int j = 0; j < 4; ++j) m[j] = __builtin_amdgcn_exp2f(__builtin_fmaf(m[j], cs, -mx));
        const float rs = __builtin_amdgcn_rcpf(xsum((m[0] + m[1]) + (m[2] + m[3])));
        const u32x2 pp = pk4(m * rs);
        pb[h] = cat(pp & mp, pp & ~mp);
      }
      const f32x4 z4 = {0.f, 0.f, 0.f, 0.f};
      const u32x4 yo = cat(pY, P[r][7]);
      f32x4 oa = mma(cat(pVa, P[r][5]), pb[0], z4), obb = mma(cat(pVb, P[r][6]), pb[1], z4);
      f32x4 ya = mma(yo, pb[0], z4), yb = mma(yo, pb[1], z4);
      const u32 base = WS_O + (rt * 8 + par * 4 + r) * 40960 + (hg >> 1) * 5120;
      const int pi = 2 * (hg & 1) + hp;
      gst128(wsr, base + pi * 1024 + lane * 16, cat(pk4(oa), pk4(obb)));
      if (g < 2) gst64(wsr, base + 4096 + (pi * 16 + i16) * 16 + g * 8, pk4(g ? yb : ya));
      pKa = P[r][2]; pKb = P[r][3]; pK4 = K4; pVa = P[r][5]; pVb = P[r][6]; pY = P[r][7];
    }
    STAMP(3);
    publish(ws, WS_BFLAG + (((rt * 2 + par) * 8 + hg) * 2 + hp) * 8, tag, lane);
    STAMP(4);
  } else {
    const int h = wave;
    const u32 cK = h * 40 + p4 * 8, cZ = h * 40 + 32 + (p4 == (h ^ 1) ? SEG : 0);
    f32x4 acc[8][3];
#pragma unroll
    for (int r = 0; r < 8; ++r)
#pragma unroll
      for (int f = 0; f < 3; ++f) acc[r][f] = f32x4{0.f, 0.f, 0.f, 0.f};
    struct Fr { u32x4 a[8], w[3]; };
    auto rd = [&](Fr& F, u32 xs, int u, int kk) {
      const u32 aa = xs + (aA0 ^ (kk << 6));
      const u32 b0 = lds0 + L_AW + (u & 3) * AW_SLOT + rB0, b1 = lds0 + L_AW + (u & 3) * AW_SLOT + rB1;
      F.w[0] = ldB(b0 + cK, b1 + cK); F.w[1] = ldB(b0 + cK + SEG, b1 + cK + SEG); F.w[2] = ldB(b0 + cZ, b1 + cZ);
#pragma unroll
      for (int r = 0; r < 8; ++r) F.a[r] = ld128(aa + r * 2048);
    };
    auto mm = [&](Fr& F, int r0_, int r1_) {
#pragma unroll
      for (int r = r0_; r < r1_; ++r)
#pragma unroll
        for (int f = 0; f < 3; ++f) acc[r][f] = mma(F.a[r], F.w[f], acc[r][f]);
    };
    auto ilv = [&]() {
#pragma unroll
      for (int n = 0; n < 14; ++n) { __builtin_amdgcn_sched_group_barrier(0x8, 1, 0); __builtin_amdgcn_sched_group_barrier(0x100, 1, 0); }
      __builtin_amdgcn_sched_group_barrier(0x8, 4, 0);
    };
    Fr F0, F1;
    SB(); bar();
    STAMP(1);
    u32 xs = lds0 + L_AX;
    rd(F0, xs, 0, 0);
#pragma nounroll
    for (int s = 0; s < 10; ++s) {
      SB(); waitl0(); SB();
      mm(F0, 0, 2); SB();
      bar(); SB();
      rd(F1, xs, 2 * s + 1, 1); mm(F0, 2, 8); ilv(); SB();
      waitl0(); SB();
      mm(F1, 0, 2); SB();
      bar(); SB();
      xs = xs == lds0 + L_AX + 2 * AX_SLOT ? lds0 + L_AX : xs + AX_SLOT;
      rd(F0, xs, 2 * s + 2, 0); mm(F1, 2, 8); ilv();
      SB();
    }
    STAMP(2);
    tag = *(LAS u64*)(size_t)(lds0 + L_CTL);
    const bool eh = q4 == (h ^ 1), gh = g == h, zc = i16 == 4 * h;
    const u32x2 ones = {0x3F803F80u, 0x3F803F80u};
    f32x4 t00 = {}, t01 = {}, t10 = {}, t11 = {};
#pragma unroll
    for (int k = 0; k < 4; ++k) {
      const u32x4 pk = cat(pk4(phi4(acc[2 * k][0])), pk4(phi4(acc[2 * k + 1][0])));
      const u32x4 pz = cat(pk4(phi4(acc[2 * k][2])), pk4(phi4(acc[2 * k + 1][2])));
      const u32x4 vv = cat(pk4(acc[2 * k][1]), pk4(acc[2 * k + 1][1]));
      const u32x2 z0 = pk4(acc[2 * k][2]), z1 = pk4(acc[2 * k + 1][2]);
      const u32x4 zb = cat(eh ? z0 : ones, eh ? z1 : ones);
      t00 = mma(pk, vv, t00); t01 = mma(pk, zb, t01); t10 = mma(pz, vv, t10); t11 = mma(pz, zb, t11);
    }
    const u32 pb = WS_KVP + (hg * 4 + h) * 16 * KVP_STRIDE;
    {
      const u32 o = pb + rt * KVP_STRIDE;
      gst128(wsr, o + lane * 16, __builtin_bit_cast(u32x4, t00));
      if (gh) gst128(wsr, o + (64 + i16) * 16, __builtin_bit_cast(u32x4, t10));
      if (eh || zc) gst128(wsr, o + (eh ? 80 + p4 + 4 * g : 96 + g) * 16, __builtin_bit_cast(u32x4, t01));
      if (gh && (eh || zc)) gst128(wsr, o + (eh ? 100 + p4 : 104) * 16, __builtin_bit_cast(u32x4, t11));
    }
    STAMP(3);
    publish(ws, WS_KVFLAG + ((hg * 4 + h) * 16 + rt) * 8, tag, lane);
    STAMP(4);
    await(ws, WS_KVFLAG + ((hg * 4 + h) * 16 + i16) * 8, tag);
    STAMP(5);
    f32x4 sm = {}, st = {};
    {
      u32x4 vm[16], vt[16];
      const u32 tl = lane < 41 ? lane : 40;
#pragma unroll
      for (int r = 0; r < 16; ++r) { vm[r] = gld128(wsr, pb + r * KVP_STRIDE + lane * 16); vt[r] = gld128(wsr, pb + r * KVP_STRIDE + 1024 + tl * 16); }
#pragma unroll
      for (int r = 0; r < 16; ++r) { sm += __builtin_bit_cast(f32x4, vm[r]); st += __builtin_bit_cast(f32x4, vt[r]); }
    }
    const u32 tb = lds0 + L_TAB + h * TAB_STRIDE;
    st64(tb + lane * 8, pk4(sm));
    if (lane < 42) st64(tb + (64 + lane) * 8, lane < 41 ? pk4(st) : u32x2{0u, 0u});
    waitl0(); bar();
    STAMP(6);
  }

  {
    const int rp = wave & 1, kh = wave >> 1;
    const int rt2 = rg >> 1, par2 = rg & 1;
    const u32 blk0 = rg * 4 + rp * 2;
    const u32 wr = lds0 + (4 * g + q4) * 160 + p4 * 8;
    f32x4 acc[2][2][5];
#pragma unroll
    for (int hf = 0; hf < 2; ++hf)
#pragma unroll
      for (int r = 0; r < 2; ++r)
#pragma unroll
        for (int c = 0; c < 5; ++c) acc[hf][r][c] = f32x4{0.f, 0.f, 0.f, 0.f};
    u32x4 of[10][2];
    auto step = [&](int hf, int n, u32 slot) {
#pragma unroll
      for (int c = 0; c < 5; ++c) {
        const u32x4 w = ldB(wr + slot * 5120 + c * 32, wr + slot * 5120 + 2560 + c * 32);
        acc[hf][0][c] = mma(w, of[n][0], acc[hf][0][c]); acc[hf][1][c] = mma(w, of[n][1], acc[hf][1][c]);
      }
    };
    await(ws, WS_BFLAG + ((rt2 * 2 + par2) * 16 + kh * 8 + (lane & 7)) * 8, tag);
    STAMP(7);
#pragma unroll
    for (int n = 0; n < 10; ++n)
#pragma unroll
      for (int r = 0; r < 2; ++r) of[n][r] = gld128(wsr, WS_O + (blk0 + r) * 40960 + (kh * 10 + n) * 1024 + lane * 16);
    bar();
    STAMP(8);
#pragma unroll
    for (int n = 0; n < 10; ++n) step(0, n, kh * 10 + n);
    waitl0(); bar();
    STAMP(9);
    await(ws, WS_AFLAG + ((rt2 * 4 + par2 * 2 + rp) * 8 + kh * 4 + (lane & 3)) * 8, tag);
    STAMP(10);
#pragma unroll
    for (int n = 0; n < 10; ++n)
#pragma unroll
      for (int r = 0; r < 2; ++r) of[n][r] = gld128(wsr, WS_O + (blk0 + r) * 40960 + 20480 + (kh * 10 + n) * 1024 + lane * 16);
    float gt[2];
#pragma unroll
    for (int r = 0; r < 2; ++r) gt[r] = 1.f - __builtin_bit_cast(float, gld32(wsr, WS_GATE + (kh * 4 * 128 + blk0 + r) * 64 + i16 * 4));
#pragma unroll
    for (int n = 0; n < 5; ++n) step(1, n, 20 + kh * 5 + n);
    bar();
#pragma unroll
    for (int n = 5; n < 10; ++n) step(1, n, kh * 5 + n - 5);
    STAMP(11);
    const u32 xb = lds0 + 10 * 5120;
#pragma unroll
    for (int c = 0; c < 5; ++c) {
      const f32x4 v = kh ? acc[1][0][c] + acc[0][0][c] * gt[0] : acc[1][1][c] + acc[0][1][c] * gt[1];
      *(LAS f32x4*)(size_t)(xb + wave * 5120 + c * 1024 + lane * 16) = v;
    }
    waitl0(); bar();
    const u32 blk = blk0 + kh;
    const int tok = (blk >> 3) * 128 + (blk & 3) * 32 + ((blk >> 2) & 1) + 2 * i16;
    char* orow = out + tok * 1280 + ctile * 160 + g * 8;
#pragma unroll
    for (int c = 0; c < 5; ++c) {
      f32x4 v = kh ? acc[1][1][c] + acc[0][1][c] * gt[1] : acc[1][0][c] + acc[0][0][c] * gt[0];
      v += *(LAS f32x4*)(size_t)(xb + (wave ^ 2) * 5120 + c * 1024 + lane * 16);
      *(GAS u32x2*)(orow + c * 32) = pk4(v);
    }
    STAMP(12);
    waitv<0>(); STAMP(13); STAMP_OUT;
  }
}

extern "C" void kernel_launch(void* const* d_in, const int* in_sizes, int n_in, void* d_out, int out_size, void* d_ws, size_t ws_size,
                              hipStream_t stream) {
  (void)hipFuncSetAttribute((const void*)fused_kernel, hipFuncAttributeMaxDynamicSharedMemorySize, LDS_BYTES);
  fused_kernel<<<256, 512, LDS_BYTES, stream>>>((const char*)d_in[0], (const char*)d_in[1], (const char*)d_in[2], (const char*)d_in[4],
                                                (const char*)d_in[5], (const char*)d_in[6], (char*)d_out, (char*)d_ws);
}
```

```cpp
#define PF_CODE
#include <hip/hip_runtime.h>
#include <hip/hip_bf16.h>

typedef unsigned long long u64;
typedef unsigned u32;
typedef u32 u32x2 __attribute__((ext_vector_type(2)));
typedef u32 u32x4 __attribute__((ext_vector_type(4)));
typedef float f32x2 __attribute__((ext_vector_type(2)));
typedef float f32x4 __attribute__((ext_vector_type(4)));
typedef short s16x4 __attribute__((ext_vector_type(4)));
typedef __bf16 bf16x2 __attribute__((ext_vector_type(2)));
typedef __bf16 bf16x8 __attribute__((ext_vector_type(8)));
#define LAS __attribute__((address_space(3)))
#define GAS __attribute__((address_space(1)))
#define DEV __device__ __forceinline__
#define SB() __builtin_amdgcn_sched_barrier(0)
#define RLX_AGENT __ATOMIC_RELAXED, __HIP_MEMORY_SCOPE_AGENT

#define STAMP_DECL
#define CSTAMP(i)
#define STAMP(i)
#define STAMP_OUT

constexpr u32 WS_EPOCH = 0;
constexpr u32 WS_KVFLAG = 4096;
constexpr u32 WS_BFLAG = WS_KVFLAG + 512 * 8;
constexpr u32 WS_AFLAG = WS_BFLAG + 512 * 8;
constexpr u32 WS_GATE = 32768;
constexpr u32 WS_KVP = WS_GATE + 8 * 128 * 64;
constexpr u32 KVP_STRIDE = 1792;
constexpr u32 WS_O = WS_KVP + 512 * KVP_STRIDE;
constexpr u32 WS_END = WS_O + 128 * 40960;
constexpr u64 TAG_STEP = 0x9E3779B97F4A7C15ull;
constexpr u32 SPIN_CAP = 1u << 18;

constexpr u32 L_RING = 40960;
#define XSEG 5136
constexpr u32 SEG = XSEG;
constexpr u32 BX_SLOT = 18432, BW_SLOT = (XSEG==5136?15424:15360), L_BX = L_RING, L_BW = L_BX + 3 * BX_SLOT;
constexpr u32 AX_SLOT = 16384, AW_SLOT = 2*XSEG, L_AX = L_RING, L_AW = L_AX + 3 * AX_SLOT;
constexpr u32 L_MISC = 157952;
constexpr u32 L_HALOK = L_MISC, L_HALOV = L_MISC + 2560;
constexpr u32 L_GATEW = L_MISC, L_TAB = L_MISC + 1280, TAB_STRIDE = 848;
constexpr u32 L_CTL = L_MISC + 5120;
constexpr u32 L_DONE = L_CTL + 16, L_CNT = L_CTL + 64;
constexpr u32 LDS_BYTES = 163840;
static_assert(L_TAB + 4 * TAB_STRIDE <= L_CTL && L_CTL + 256 <= LDS_BYTES, "lds map");

template <int N> DEV void waitv() { asm volatile("s_waitcnt vmcnt(%0)" ::"i"(N) : "memory"); }
DEV void waitl0() { asm volatile("s_waitcnt lgkmcnt(0)" ::: "memory"); }
DEV void bar() { asm volatile("s_barrier" ::: "memory"); }
DEV void glds(const char* sbase, u32 voff, u32 lds) {
  asm volatile("s_mov_b32 m0, %2\n\ts_nop 0\n\tglobal_load_lds_dwordx4 %0, %1" ::"v"(voff), "s"(sbase), "s"(lds));
}
DEV void glds4(const char* sbase, u32 v0, u32 v1, u32 v2, u32 v3, u32 lds) {
  asm volatile("s_mov_b32 m0, %5\n\ts_nop 0\n\tglobal_load_lds_dwordx4 %0, %4\n\tglobal_load_lds_dwordx4 %1, %4 offset:1024\n\t"
               "global_load_lds_dwordx4 %2, %4 offset:2048\n\tglobal_load_lds_dwordx4 %3, %4 offset:3072"
               ::"v"(v0), "v"(v1), "v"(v2), "v"(v3), "s"(sbase), "s"(lds));
}
DEV void glds5(const char* sbase, const u32 (&v)[5], u32 lds) { glds4(sbase, v[0], v[1], v[2], v[3], lds); glds(sbase, v[4], lds + 4096); }
DEV u32x4 ld128(u32 a) { return *(LAS u32x4*)(size_t)a; }
DEV u32x2 ld64(u32 a) { return *(LAS u32x2*)(size_t)a; }
DEV void st64(u32 a, u32x2 v) { *(LAS u32x2*)(size_t)a = v; }
DEV u32x2 ldtr(u32 a) { return __builtin_bit_cast(u32x2, __builtin_amdgcn_ds_read_tr16_b64_v4i16((LAS s16x4*)(size_t)a)); }
DEV u32x4 cat(u32x2 a, u32x2 b) { return __builtin_shufflevector(a, b, 0, 1, 2, 3); }
DEV u32x4 ldB(u32 a0, u32 a1) { return cat(ldtr(a0), ldtr(a1)); }
DEV f32x4 mma(u32x4 a, u32x4 b, f32x4 c) {
  return __builtin_amdgcn_mfma_f32_16x16x32_bf16(__builtin_bit_cast(bf16x8, a), __builtin_bit_cast(bf16x8, b), c, 0, 0, 0);
}
DEV u32 pk2(float a, float b) { f32x2 v = {a, b}; return __builtin_bit_cast(u32, __builtin_convertvector(v, bf16x2)); }
DEV u32x2 pk4(f32x4 v) { return u32x2{pk2(v[0], v[1]), pk2(v[2], v[3])}; }
DEV float phi1(float t) {
  float e = __builtin_amdgcn_fmed3f(__builtin_amdgcn_exp2f(t * 1.44269504089f), 0.f, 1.f);
  return __builtin_fmaxf(t, 0.f) + (e + 1e-6f);
}
DEV f32x4 phi4(f32x4 v) { return f32x4{phi1(v[0]), phi1(v[1]), phi1(v[2]), phi1(v[3])}; }
DEV float xmax(float v) {
  u32 u = __builtin_bit_cast(u32, v);
  auto r = __builtin_amdgcn_permlane16_swap(u, u, false, false);
  v = __builtin_fmaxf(__builtin_bit_cast(float, (u32)r[0]), __builtin_bit_cast(float, (u32)r[1]));
  u = __builtin_bit_cast(u32, v);
  auto s = __builtin_amdgcn_permlane32_swap(u, u, false, false);
  return __builtin_fmaxf(__builtin_bit_cast(float, (u32)s[0]), __builtin_bit_cast(float, (u32)s[1]));
}
DEV float xsum(float v) {
  u32 u = __builtin_bit_cast(u32, v);
  auto r = __builtin_amdgcn_permlane16_swap(u, u, false, false);
  v = __builtin_bit_cast(float, (u32)r[0]) + __builtin_bit_cast(float, (u32)r[1]);
  u = __builtin_bit_cast(u32, v);
  auto s = __builtin_amdgcn_permlane32_swap(u, u, false, false);
  return __builtin_bit_cast(float, (u32)s[0]) + __builtin_bit_cast(float, (u32)s[1]);
}
DEV u32 odd_rows(u32 x) { auto r = __builtin_amdgcn_permlane16_swap(x, x, false, false); return r[1]; }

typedef __amdgpu_buffer_rsrc_t rsrc_t;
DEV u32x4 gld128(rsrc_t r, u32 off) { return __builtin_amdgcn_raw_buffer_load_b128(r, off, 0, 16); }
DEV u32 gld32(rsrc_t r, u32 off) { return __builtin_amdgcn_raw_buffer_load_b32(r, off, 0, 16); }
DEV void gst128(rsrc_t r, u32 off, u32x4 v) { __builtin_amdgcn_raw_buffer_store_b128(v, r, off, 0, 16); }
DEV void gst64(rsrc_t r, u32 off, u32x2 v) { __builtin_amdgcn_raw_buffer_store_b64(v, r, off, 0, 16); }
DEV void gst32(rsrc_t r, u32 off, u32 v) { __builtin_amdgcn_raw_buffer_store_b32(v, r, off, 0, 16); }
DEV void publish(char* ws, u32 off, u64 tag, int lane) {
  waitv<0>();
  if (lane == 0) __hip_atomic_store((GAS u64*)(ws + off), tag, RLX_AGENT);
}
DEV void await(char* ws, u32 off, u64 tag) {
  for (u32 spins = 0; spins < SPIN_CAP; ++spins) {
    u64 v = __hip_atomic_load((GAS u64*)(ws + off), RLX_AGENT);
    if (__all(v == tag)) break;
    __builtin_amdgcn_s_sleep(1);
  }
  asm volatile("" ::: "memory");
}
DEV u32 ldw(u32 a) { return __hip_atomic_load((LAS u32*)(size_t)a, __ATOMIC_RELAXED, __HIP_MEMORY_SCOPE_WORKGROUP); }
DEV void stw(u32 a, u32 v) { __hip_atomic_store((LAS u32*)(size_t)a, v, __ATOMIC_RELAXED, __HIP_MEMORY_SCOPE_WORKGROUP); }
DEV void fence_c() { asm volatile("" ::: "memory"); }
DEV void lds_arrive(u32 a, int lane) {
  if (lane == 0) __hip_atomic_fetch_add((LAS u32*)(size_t)a, 1u, __ATOMIC_RELAXED, __HIP_MEMORY_SCOPE_WORKGROUP);
}
template <int SLEEP> DEV void lds_await(u32 v, u32 a, u32 want) {
  if (__builtin_expect(!__all(v >= want), 0)) {
    for (u32 spins = 0; spins < (SPIN_CAP << 4); ++spins) {
      if (__all(ldw(a) >= want)) break;
      if (SLEEP) __builtin_amdgcn_s_sleep(SLEEP);
    }
  }
  fence_c();
}

DEV void wo_offsets(int lane, u32 (&om)[5], u32 (&os)[5]) {
#pragma unroll
  for (int i = 0; i < 5; ++i) {
    u32 c = i * 64 + lane, pos = (c * 6554u) >> 16, o = c - pos * 10;
    om[i] = (pos + 4 * (pos >> 4)) * 1280 + o * 16 - (i & 3) * 1024 * (i < 4);
    os[i] = (20 * (2 * ((pos >> 2) & 3) + (pos >> 4)) + 16 + (pos & 3)) * 1280 + o * 16 - (i & 3) * 1024 * (i < 4);
  }
}
DEV void wo_issue(const char* wo, int ks, u32 lds, const u32 (&om)[5], const u32 (&os)[5]) {
  int sg = (ks * 52) >> 8, n = ks - sg * 5;
  if (n < 4) glds5(wo + (sg * 160 + n * 40) * 1280, om, lds);
  else glds5(wo + sg * 160 * 1280, os, lds);
}

__global__ __launch_bounds__(512, 2) void fused_kernel(const char* __restrict__ X, const char* __restrict__ WA, const char* __restrict__ WoA,
                                                       const char* __restrict__ WB, const char* __restrict__ WoB, const char* __restrict__ GW,
                                                       char* out, char* ws) {
  extern __shared__ __attribute__((aligned(16))) char smem[];
  const u32 lds0 = (u32)(size_t)(LAS char*)smem;
  const int b = blockIdx.x, xcd = b & 7, jj = b >> 3;
  const int rt = (xcd & 1) * 8 + (jj & 7), ct = (xcd >> 1) * 4 + (jj >> 3);
  const int r0t = ((jj & 7) * 5 + (jj >> 3) * 10) >> 2, r0 = r0t >= 10 ? r0t - 10 : r0t;
  const bool isB = ct >= 8;
  const int hg = ct & 7;
  const int wave = __builtin_amdgcn_readfirstlane(threadIdx.x >> 6), lane = threadIdx.x & 63;
  const int g = lane >> 4, i16 = lane & 15, q4 = (lane >> 2) & 3, p4 = lane & 3;
  const rsrc_t wsr = __builtin_amdgcn_make_buffer_rsrc(ws, 0, WS_END, 0x00020000);
  const u32 aA0 = i16 * 128 + ((g ^ ((i16 >> 1) & 7)) << 4);
  const u32 rB0 = (8 * g + (q4 ^ (4 * (g & 1)))) * 160, rB1 = (8 * g + ((4 + q4) ^ (4 * (g & 1)))) * 160;
  const int rg = xcd * 4 + (jj >> 3), ctile = jj & 7;
  u64 tag;
  STAMP_DECL
  STAMP(0);

  if (wave >= 4) {
    const int lw = wave - 4;
    __builtin_amdgcn_s_setprio(3);
    u32 ow[5];
#pragma unroll
    for (int i = 0; i < 5; ++i) {
      u32 c = i * 64 + lane, pos = (c * 6554u) >> 16, o = c - pos * 10;
      ow[i] = (pos ^ (((pos >> 3) & 1) << 2)) * 3840 + o * 16 - (i & 3) * 1024 * (i < 4);
    }
    const u32 ox0 = (lane >> 3) * 2560 + (((lane & 7) ^ (lane >> 4)) << 4), ox1 = (ox0 ^ 64) + 20480 - 1024;
    const u32 ox2 = ox0 + 2 * (20480 - 1024), ox3 = ox1 + 2 * (20480 - 1024);
    const char* Wt = (isB ? WB : WA) + hg * 160;
    auto ksof = [&](int s) { int k = s + r0; return k >= 10 ? k - 10 : k; };
    auto xquad = [&](int s, int qd, u32 slotbase) {
      glds4(X + (128 * rt + 64 * (qd & 1) + (qd >> 1)) * 1280 + ksof(s) * 128, ox0, ox1, ox2, ox3, slotbase + qd * 4096);
    };
    auto wseg = [&](int u, int seg, u32 dst) { glds5(Wt + (ksof(u >> 1) * 64 + (u & 1) * 32) * 3840 + seg * 1280, ow, dst); };
    if (isB) {
      if (lw == 0) {
        u32 wk = r0 * (64 * 3840), xk = r0 * 128, xs = lds0 + L_BX;
        const char* Xt = X + rt * (128 * 1280); const char* Xh = rt ? Xt - 16 * 1280 : Xt;
        auto issue_x = [&](int par) {
          const char* p = Xt + par * 1280 + xk;
          glds4(p, ox0, ox1, ox2, ox3, xs + par * 8192); glds4(p + 64 * 1280, ox0, ox1, ox2, ox3, xs + par * 8192 + 4096);
          glds(Xh + par * 1280 + xk, ox0 ^ (par << 6), xs + 16384 + par * 1024);
          if (par) { xk = xk == 9 * 128 ? 0 : xk + 128; xs = xs == lds0 + L_BX + 2 * BX_SLOT ? lds0 + L_BX : xs + BX_SLOT; }
        };
        const u32 da = lds0 + L_DONE + ((lane & 7) == 4 ? 0 : (lane & 7)) * 4;
        issue_x(0);
        u32 dv = 0;
#pragma nounroll
        for (int u = 0; u < 20; ++u) {
          if (u >= 4) lds_await<0>(dv, da, u - 3);
          const char* p = Wt + wk; const u32 d = lds0 + L_BW + (u & 3) * BW_SLOT;
          glds5(p, ow, d); glds5(p + 1280, ow, d + SEG);
          if (u == 0) bar();
          else { waitv<10>(); stw(lds0 + L_CNT + (u - 1) * 4, 4); }
          glds5(p + 2560, ow, d + 2 * SEG);
          wk = wk == 608 * 3840 ? 0 : wk + 32 * 3840;
          dv = ldw(da);
          if (u < 19) issue_x((u + 1) & 1);
        }
        waitv<0>(); stw(lds0 + L_CNT + 19 * 4, 4);
      } else {
        const int fb = lw == 1 ? 0 : lw * 3 - 2;
        u32 ch[4];
#pragma unroll
        for (int f = 0; f < 4; ++f) { const int ph = fb + f; ch[f] = (ph >= 5 ? 2 * SEG + (ph - 5) * 32 : SEG + ph * 32) + p4 * 8; }
        f32x4 h[4] = {};
        u32 xs = lds0 + L_BX + 8 * 2048;
        bar();
#pragma nounroll
        for (int u = 0; u < 20; ++u) {
          lds_await<1>(0, lds0 + L_CNT + u * 4, 4);
          const u32 wb = lds0 + L_BW + (u & 3) * BW_SLOT;
          const u32x4 ah = ld128(xs + (aA0 ^ ((u & 1) << 6)));
          u32x4 w[4];
#pragma unroll
          for (int f = 0; f < 3; ++f) w[f] = ldB(wb + rB0 + ch[f], wb + rB1 + ch[f]);
          if (lw == 1) w[3] = ldB(wb + rB0 + ch[3], wb + rB1 + ch[3]);
          fence_c(); stw(lds0 + L_DONE + wave * 4, u + 1);
#pragma unroll
          for (int f = 0; f < 3; ++f) h[f] = mma(w[f], ah, h[f]);
          if (lw == 1) h[3] = mma(w[3], ah, h[3]);
          if (u & 1) xs = xs == lds0 + L_BX + 8 * 2048 + 2 * BX_SLOT ? lds0 + L_BX + 8 * 2048 : xs + BX_SLOT;
        }
#pragma unroll
        for (int f = 0; f < 4; ++f) {
          if (f < 3 || lw == 1) {
            const int ph = fb + f;
            const u32x2 v = pk4(h[f]);
            if (ph < 5) st64(lds0 + L_HALOK + ((ph * 4 + g) * 16 + i16) * 8, v);
            else {
              LAS unsigned short* d = (LAS unsigned short*)(size_t)(lds0 + L_HALOV + ((ph - 5) * 16 + 4 * g) * 32 + i16 * 2);
              d[0] = (unsigned short)v[0]; d[16] = (unsigned short)(v[0] >> 16); d[32] = (unsigned short)v[1]; d[48] = (unsigned short)(v[1] >> 16);
            }
          }
        }
      }
      waitl0(); bar();
      tag = *(LAS u64*)(size_t)(lds0 + L_CTL);
    } else {
      auto xhalf = [&](int n) { xquad(n >> 1, (n & 1) * 2 + (lw & 1), lds0 + L_AX + ((n >> 1) % 3) * AX_SLOT); };
      auto unit = [&](int u) {
        if (lw < 2) wseg(u, 1 + lw, lds0 + L_AW + (u & 3) * AW_SLOT + lw * SEG);
        else if (u < 19) xhalf(u + 1);
      };
      if (lw == 1) { glds(GW, lane * 16, lds0 + L_GATEW); if (lane < 16) glds(GW, 1024 + lane * 16, lds0 + L_GATEW + 1024); }
      if (lw >= 2) xhalf(0);
      unit(0); unit(1);
      u32x4 xr[20][2];
      f32x4 gl[2] = {};
      u32x4 gw;
      const u32 da = lds0 + L_DONE + (lane & 7) * 4;
      bar();
      if (lw < 2) waitv<5>(); else waitv<4>();
      lds_arrive(lds0 + L_CNT, lane);
      u32 dv = 0;
#pragma unroll
      for (int u = 0; u < 20; ++u) {
        if (u + 2 < 20) {
          if (u >= 2) lds_await<0>(dv, da, u - 1);
          unit(u + 2);
        } else wseg((u - 18) * 4 + lw, 0, lds0 + ((u - 18) * 4 + lw) * 5120);
        if (u + 2 < 19) { if (lw < 2) waitv<5>(); else waitv<4>(); }
        else if (u + 2 == 19) { if (lw < 2) waitv<5>(); else waitv<0>(); }
        else waitv<5>();
        lds_arrive(lds0 + L_CNT + (u + 1) * 4, lane);
        if (u > 0) { gl[0] = mma(gw, xr[u - 1][0], gl[0]); gl[1] = mma(gw, xr[u - 1][1], gl[1]); }
        lds_await<0>(0, lds0 + L_CNT + u * 4, 4);
        const u32 xa = lds0 + L_AX + ((u >> 1) % 3) * AX_SLOT + lw * 4096 + (aA0 ^ ((u & 1) << 6));
        xr[u][0] = ld128(xa); xr[u][1] = ld128(xa + 2048);
        gw = ld128(lds0 + L_GATEW + ksof(u >> 1) * 128 + (u & 1) * 64 + g * 16);
        fence_c(); stw(lds0 + L_DONE + wave * 4, u + 1);
        dv = ldw(da);
        SB();
      }
      gl[0] = mma(gw, xr[19][0], gl[0]); gl[1] = mma(gw, xr[19][1], gl[1]);
      __builtin_amdgcn_s_setprio(0);
      STAMP(1);
      lds_await<0>(dv, da, 20);
      f32x4 qa[2][5] = {};
      const u32 cQ = p4 * 8, cQ5 = 32 + p4 * 40;
#pragma unroll
      for (int r = 0; r < 5; ++r) {
        if (r < 3) { wseg((r + 2) * 4 + lw, 0, lds0 + ((r + 2) * 4 + lw) * 5120); waitv<5>(); } else waitv<0>();
        if (r < 4) lds_arrive(lds0 + L_CNT + (21 + r) * 4, lane);
        lds_await<0>(0, lds0 + L_CNT + (20 + r) * 4, 4);
#pragma unroll
        for (int n = 0; n < 4; ++n) {
          const int u = r * 4 + n; const u32 wb = lds0 + u * 5120;
#pragma unroll
          for (int f = 0; f < 5; ++f) {
            const u32 c = f < 4 ? cQ + f * 40 : cQ5;
            u32x4 w = ldB(wb + rB0 + c, wb + rB1 + c);
            qa[0][f] = mma(w, xr[u][0], qa[0][f]); qa[1][f] = mma(w, xr[u][1], qa[1][f]);
          }
        }
      }
      STAMP(2);
      u32x2 pq[2][5];
#pragma unroll
      for (int r = 0; r < 2; ++r)
#pragma unroll
        for (int f = 0; f < 5; ++f) pq[r][f] = pk4(phi4(qa[r][f]));
      float gate[2];
#pragma unroll
      for (int r = 0; r < 2; ++r) gate[r] = __builtin_amdgcn_rcpf(1.f + __builtin_amdgcn_exp2f(gl[r][0] * -1.44269504089f));
      waitl0(); bar();
      STAMP(3);
      tag = *(LAS u64*)(size_t)(lds0 + L_CTL);
      f32x4 n5[2] = {};
      u32x2 ob[2][4];
      float sc5[2] = {0.f, 0.f};
#pragma unroll
      for (int h = 0; h < 4; ++h) {
        const u32 tb = lds0 + L_TAB + h * TAB_STRIDE;
        const bool gh = g == h, eh = q4 == (h ^ 1);
        u32x4 a0 = cat(ld64(tb + lane * 8), ld64(tb + (gh ? 64 + i16 : 105) * 8));
        u32x4 a5 = cat(ld64(tb + (eh ? 80 + p4 + 4 * g : 105) * 8), ld64(tb + (eh && gh ? 100 + p4 : 105) * 8));
        u32x4 az = cat(ld64(tb + (96 + g) * 8), ld64(tb + (gh ? 104 : 105) * 8));
#pragma unroll
        for (int r = 0; r < 2; ++r) {
          u32x4 bo = cat(pq[r][h], pq[r][4]);
          f32x4 num = mma(a0, bo, f32x4{0.f, 0.f, 0.f, 0.f});
          f32x4 den = mma(az, bo, f32x4{1e-6f, 1e-6f, 1e-6f, 1e-6f});
          n5[r] = mma(a5, bo, n5[r]);
          float sc = gate[r] * __builtin_amdgcn_rcpf(den[0]);
          ob[r][h] = pk4(num * sc);
          sc5[r] = (g ^ 1) == h ? sc : sc5[r];
        }
      }
      STAMP(5);
#pragma unroll
      for (int r = 0; r < 2; ++r) {
        const u32 blk = rt * 8 + lw * 2 + r;
        const u32 base = WS_O + blk * 40960 + 20480 + (hg >> 1) * 5120;
        gst128(wsr, base + (hg & 1) * 2048 + lane * 16, cat(ob[r][0], ob[r][1]));
        gst128(wsr, base + (hg & 1) * 2048 + 1024 + lane * 16, cat(ob[r][2], ob[r][3]));
        gst64(wsr, base + 4096 + ((2 * (hg & 1) + (g >> 1)) * 16 + i16) * 16 + ((g ^ 1) & 1) * 8, pk4(n5[r] * sc5[r]));
        if (lane < 16) gst32(wsr, WS_GATE + (hg * 128 + blk) * 64 + lane * 4, __builtin_bit_cast(u32, gate[r]));
      }
      STAMP(6);
      publish(ws, WS_AFLAG + ((rt * 4 + lw) * 8 + hg) * 8, tag, lane);
      STAMP(4);
    }
    u32 om[5], os[5];
    wo_offsets(lane, om, os);
    const char* wob = WoB + ctile * 160; const char* woa = WoA + ctile * 160;
#pragma nounroll
    for (int s = lw; s < 30; s += 4) {
      if (s < 20) wo_issue(wob, s, lds0 + s * 5120, om, os);
      else wo_issue(woa, s < 25 ? s - 20 : s - 15, lds0 + s * 5120, om, os);
    }
    STAMP(7);
    waitv<0>(); STAMP(8); bar();
    bar();
    STAMP(9);
#pragma nounroll
    for (int s = lw; s < 10; s += 4) wo_issue(woa, s < 5 ? s + 5 : s + 10, lds0 + s * 5120, om, os);
    waitv<0>(); STAMP(10); bar();
    bar();
    if (b == 0 && lw == 0) {
#pragma unroll
      for (int r = 0; r < 2; ++r) {
        const int n = r * 64 + lane;
        await(ws, WS_BFLAG + (((n >> 3) * 2 * 8 + (n & 7)) * 2) * 8, tag);
        await(ws, WS_AFLAG + (((n >> 3) * 4) * 8 + (n & 7)) * 8, tag);
      }
      if (lane == 0) __hip_atomic_store((GAS u64*)(ws + WS_EPOCH), tag, RLX_AGENT);
    }
    STAMP(13); STAMP_OUT;
    asm volatile(".Lpf_end:");
    return;
  }

  if (wave == 0 && lane < 44) stw(lds0 + L_DONE + lane * 4, 0);
  if (wave == 1) {
    u64 pc; u32 len;
    asm volatile("s_getpc_b64 %0\n.Lpf_here:\n\ts_mov_b32 %1, .Lpf_end-.Lpf_here" : "=s"(pc), "=s"(len));
    const u32 o = (lane * 32 + jj) * 128;
    if (o < len) { u32 v = *(GAS const volatile u32*)(pc + o); asm volatile("" :: "v"(v)); }
  }
  {
    const u64 e = __hip_atomic_load((GAS u64*)(ws + WS_EPOCH), RLX_AGENT);
    waitl0(); bar();
    tag = (((u64)__builtin_amdgcn_readfirstlane((u32)(e >> 32)) << 32) | __builtin_amdgcn_readfirstlane((u32)e)) + TAG_STEP;
    if (wave == 0 && lane == 0) *(LAS u64*)(size_t)(lds0 + L_CTL) = tag;
  }
  if (isB) {
    const int par = wave & 1, hp = wave >> 1;
    const u32 cM = hp * 80 + p4 * 8, cX = hp * 80 + 32 + (p4 & 1) * SEG + (p4 >> 1) * 40, cY = 2 * SEG + hp * 80 + 32 + (p4 & 1) * 40;
    f32x4 acc[4][8];
#pragma unroll
    for (int r = 0; r < 4; ++r)
#pragma unroll
      for (int f = 0; f < 8; ++f) acc[r][f] = f32x4{0.f, 0.f, 0.f, 0.f};
    struct Fr { u32x4 a[4], w[8]; };
    auto rd = [&](Fr& F, u32 xs, int u, int kk) {
      const u32 aa = xs + (aA0 ^ (kk << 6)) + par * 8192;
      const u32 b0 = lds0 + L_BW + (u & 3) * BW_SLOT + rB0, b1 = lds0 + L_BW + (u & 3) * BW_SLOT + rB1;
#pragma unroll
      for (int r = 0; r < 4; ++r) F.a[r] = ld128(aa + r * 2048);
      F.w[0] = ldB(b0 + cM, b1 + cM); F.w[1] = ldB(b0 + cM + 40, b1 + cM + 40);
      F.w[2] = ldB(b0 + cM + SEG, b1 + cM + SEG); F.w[3] = ldB(b0 + cM + SEG + 40, b1 + cM + SEG + 40);
      F.w[4] = ldB(b0 + cX, b1 + cX);
      F.w[5] = ldB(b0 + cM + 2 * SEG, b1 + cM + 2 * SEG); F.w[6] = ldB(b0 + cM + 2 * SEG + 40, b1 + cM + 2 * SEG + 40);
      F.w[7] = ldB(b0 + cY, b1 + cY);
    };
    auto mm = [&](Fr& F, int f0, int f1) {
#pragma unroll
      for (int f = f0; f < f1; ++f)
#pragma unroll
        for (int r = 0; r < 4; ++r) acc[r][f] = f < 5 ? mma(F.w[f], F.a[r], acc[r][f]) : mma(F.a[r], F.w[f], acc[r][f]);
    };
    auto ilv = [&]() {
#pragma unroll
      for (int n = 0; n < 22; ++n) { __builtin_amdgcn_sched_group_barrier(0x8, 1, 0); __builtin_amdgcn_sched_group_barrier(0x80, 1, 0); }
      __builtin_amdgcn_sched_group_barrier(0x8, 6, 0);
    };
    Fr F0, F1;
    u32 xs = lds0 + L_BX, ca = lds0 + L_CNT;
    const u32 dn = lds0 + L_DONE + wave * 4;
#pragma unroll
    for (int r = 0; r < 4; ++r)
#pragma unroll
      for (int f = 0; f < 8; ++f) asm volatile("" : "+v"(acc[r][f]));
    lds_await<1>(0, ca, 4);
    STAMP(1); CSTAMP(14);
    rd(F0, xs, 0, 0); fence_c(); stw(dn, 1);
    u32 cv = ldw(ca + 4);
#pragma nounroll
    for (int s = 0; s < 10; ++s) {
      SB();
      mm(F0, 0, 1); SB();
      lds_await<0>(cv, ca + 4, 4); SB();
      rd(F1, xs, 2 * s + 1, 1); fence_c(); stw(dn, 2 * s + 2); cv = ldw(ca + 8); mm(F0, 1, 8); ilv(); SB();
      mm(F1, 0, 1); SB();
      if (s < 9) lds_await<0>(cv, ca + 8, 4);
      SB();
      xs = xs == lds0 + L_BX + 2 * BX_SLOT ? lds0 + L_BX : xs + BX_SLOT;
      rd(F0, xs, 2 * s + 2, 0); fence_c(); stw(dn, 2 * s + 3); cv = ldw(ca + 12); mm(F1, 1, 8); ilv();
      ca += 8;
      SB();
    }
    STAMP(2); CSTAMP(15);
    u32x2 P[4][8];
#pragma unroll
    for (int r = 0; r < 4; ++r)
#pragma unroll
      for (int f = 0; f < 8; ++f) P[r][f] = pk4(acc[r][f]);
    STAMP(5);
    waitl0(); bar();
    STAMP(6);
    u32x2 pKa, pKb, pK4, pVa, pVb, pY;
    {
      const u32 kt = lds0 + L_HALOK + ((i16 & 7) + 8 * par) * 8, vt = lds0 + L_HALOV + ((g & 1) + 2 * par) * 8;
      pKa = ld64(kt + (10 * hp + g) * 128); pKb = ld64(kt + (10 * hp + 5 + g) * 128); pK4 = ld64(kt + (10 * hp + 4 + 5 * (g >> 1)) * 128);
      pVa = ld64(vt + (40 * hp + i16) * 32); pVb = ld64(vt + (40 * hp + 20 + i16) * 32);
      pY = ld64(vt + (40 * hp + 16 + 20 * (q4 & 1) + p4) * 32);
    }
    f32x4 bc, bp;
    u32x2 mp;
#pragma unroll
    for (int j = 0; j < 4; ++j) {
      const int p = 4 * g + j;
      bc[j] = (p <= i16 && p >= i16 - 7) ? 0.f : -1e30f;
      bp[j] = p >= i16 + 9 ? 0.f : -1e30f;
    }
    mp[0] = (4 * g >= i16 + 9 ? 0xFFFFu : 0u) | (4 * g + 1 >= i16 + 9 ? 0xFFFF0000u : 0u);
    mp[1] = (4 * g + 2 >= i16 + 9 ? 0xFFFFu : 0u) | (4 * g + 3 >= i16 + 9 ? 0xFFFF0000u : 0u);
    const float cs = 0.22360679775f * 1.44269504089f;
    const u32x2 zero2 = {0u, 0u};
#pragma unroll
    for (int r = 0; r < 4; ++r) {
      const u32x2 K4 = {odd_rows(P[r][4][0]), odd_rows(P[r][4][1])};
      f32x4 bpr = bp;
      if (r == 0 && rt == 0) bpr = f32x4{-1e30f, -1e30f, -1e30f, -1e30f};
      u32x4 pb[2];
#pragma unroll
      for (int h = 0; h < 2; ++h) {
        const u32x4 qo = cat(P[r][h], g == 2 * h ? P[r][4] : zero2);
        f32x4 sc = mma(cat(P[r][2 + h], K4), qo, bc);
        f32x4 sp = mma(cat(h ? pKb : pKa, pK4), qo, bpr);
        f32x4 m;
#pragma unroll
        for (int j = 0; j < 4; ++j) m[j] = __builtin_fmaxf(sc[j], sp[j]);
        const float mx = xmax(__builtin_fmaxf(__builtin_fmaxf(m[0], m[1]), __builtin_fmaxf(m[2], m[3]))) * cs;
#pragma unroll
        for (int j = 0; j < 4; ++j) m[j] = __builtin_amdgcn_exp2f(__builtin_fmaf(m[j], cs, -mx));
        const float rs = __builtin_amdgcn_rcpf(xsum((m[0] + m[1]) + (m[2] + m[3])));
        const u32x2 pp = pk4(m * rs);
        pb[h] = cat(pp & mp, pp & ~mp);
      }
      const f32x4 z4 = {0.f, 0.f, 0.f, 0.f};
      const u32x4 yo = cat(pY, P[r][7]);
      f32x4 oa = mma(cat(pVa, P[r][5]), pb[0], z4), obb = mma(cat(pVb, P[r][6]), pb[1], z4);
      f32x4 ya = mma(yo, pb[0], z4), yb = mma(yo, pb[1], z4);
      const u32 base = WS_O + (rt * 8 + par * 4 + r) * 40960 + (hg >> 1) * 5120;
      const int pi = 2 * (hg & 1) + hp;
      gst128(wsr, base + pi * 1024 + lane * 16, cat(pk4(oa), pk4(obb)));
      if (g < 2) gst64(wsr, base + 4096 + (pi * 16 + i16) * 16 + g * 8, pk4(g ? yb : ya));
      pKa = P[r][2]; pKb = P[r][3]; pK4 = K4; pVa = P[r][5]; pVb = P[r][6]; pY = P[r][7];
    }
    STAMP(3);
    publish(ws, WS_BFLAG + (((rt * 2 + par) * 8 + hg) * 2 + hp) * 8, tag, lane);
    STAMP(4);
  } else {
    const int h = wave;
    const u32 cK = h * 40 + p4 * 8, cZ = h * 40 + 32 + (p4 == (h ^ 1) ? SEG : 0);
    f32x4 acc[8][3];
#pragma unroll
    for (int r = 0; r < 8; ++r)
#pragma unroll
      for (int f = 0; f < 3; ++f) acc[r][f] = f32x4{0.f, 0.f, 0.f, 0.f};
    struct Fr { u32x4 a[8], w[3]; };
    auto rd = [&](Fr& F, u32 xs, int u, int kk) {
      const u32 aa = xs + (aA0 ^ (kk << 6));
      const u32 b0 = lds0 + L_AW + (u & 3) * AW_SLOT + rB0, b1 = lds0 + L_AW + (u & 3) * AW_SLOT + rB1;
      F.w[0] = ldB(b0 + cK, b1 + cK); F.w[1] = ldB(b0 + cK + SEG, b1 + cK + SEG); F.w[2] = ldB(b0 + cZ, b1 + cZ);
#pragma unroll
      for (int r = 0; r < 8; ++r) F.a[r] = ld128(aa + r * 2048);
    };
    auto mm = [&](Fr& F, int r0_, int r1_) {
#pragma unroll
      for (int r = r0_; r < r1_; ++r)
#pragma unroll
        for (int f = 0; f < 3; ++f) acc[r][f] = mma(F.a[r], F.w[f], acc[r][f]);
    };
    auto ilv = [&]() {
#pragma unroll
      for (int n = 0; n < 16; ++n) { __builtin_amdgcn_sched_group_barrier(0x8, 1, 0); __builtin_amdgcn_sched_group_barrier(0x80, 1, 0); }
      __builtin_amdgcn_sched_group_barrier(0x8, 5, 0);
    };
    Fr F0, F1;
    u32 xs = lds0 + L_AX, ca = lds0 + L_CNT;
    const u32 dn = lds0 + L_DONE + wave * 4;
#pragma unroll
    for (int r = 0; r < 8; ++r)
#pragma unroll
      for (int f = 0; f < 3; ++f) asm volatile("" : "+v"(acc[r][f]));
    lds_await<1>(0, ca, 4);
    STAMP(1); CSTAMP(14);
    rd(F0, xs, 0, 0); fence_c(); stw(dn, 1);
    u32 cv = ldw(ca + 4);
#pragma nounroll
    for (int s = 0; s < 10; ++s) {
      SB();
      mm(F0, 0, 1); SB();
      lds_await<0>(cv, ca + 4, 4); SB();
      rd(F1, xs, 2 * s + 1, 1); fence_c(); stw(dn, 2 * s + 2); cv = ldw(ca + 8); mm(F0, 1, 8); ilv(); SB();
      mm(F1, 0, 1); SB();
      if (s < 9) lds_await<0>(cv, ca + 8, 4);
      SB();
      xs = xs == lds0 + L_AX + 2 * AX_SLOT ? lds0 + L_AX : xs + AX_SLOT;
      rd(F0, xs, 2 * s + 2, 0); fence_c(); stw(dn, 2 * s + 3); cv = ldw(ca + 12); mm(F1, 1, 8); ilv();
      ca += 8;
      SB();
    }
    STAMP(2); CSTAMP(15);
    __builtin_amdgcn_s_setprio(2);
    const bool eh = q4 == (h ^ 1), gh = g == h, zc = i16 == 4 * h;
    const u32x2 ones = {0x3F803F80u, 0x3F803F80u};
    f32x4 t00 = {}, t01 = {}, t10 = {}, t11 = {};
#pragma unroll
    for (int k = 0; k < 4; ++k) {
      const u32x4 pk = cat(pk4(phi4(acc[2 * k][0])), pk4(phi4(acc[2 * k + 1][0])));
      const u32x4 pz = cat(pk4(phi4(acc[2 * k][2])), pk4(phi4(acc[2 * k + 1][2])));
      const u32x4 vv = cat(pk4(acc[2 * k][1]), pk4(acc[2 * k + 1][1]));
      const u32x2 z0 = pk4(acc[2 * k][2]), z1 = pk4(acc[2 * k + 1][2]);
      const u32x4 zb = cat(eh ? z0 : ones, eh ? z1 : ones);
      t00 = mma(pk, vv, t00); t01 = mma(pk, zb, t01); t10 = mma(pz, vv, t10); t11 = mma(pz, zb, t11);
    }
    const u32 pb = WS_KVP + (hg * 4 + h) * 16 * KVP_STRIDE;
    {
      const u32 o = pb + rt * KVP_STRIDE;
      gst128(wsr, o + lane * 16, __builtin_bit_cast(u32x4, t00));
      if (gh) gst128(wsr, o + (64 + i16) * 16, __builtin_bit_cast(u32x4, t10));
      if (eh || zc) gst128(wsr, o + (eh ? 80 + p4 + 4 * g : 96 + g) * 16, __builtin_bit_cast(u32x4, t01));
      if (gh && (eh || zc)) gst128(wsr, o + (eh ? 100 + p4 : 104) * 16, __builtin_bit_cast(u32x4, t11));
    }
    STAMP(3);
    publish(ws, WS_KVFLAG + ((hg * 4 + h) * 16 + rt) * 8, tag, lane);
    STAMP(4);
    await(ws, WS_KVFLAG + ((hg * 4 + h) * 16 + i16) * 8, tag);
    STAMP(5);
    f32x4 sm = {}, st = {};
    {
      u32x4 vm[16], vt[16];
      const u32 tl = lane < 41 ? lane : 40;
#pragma unroll
      for (int r = 0; r < 16; ++r) { vm[r] = gld128(wsr, pb + r * KVP_STRIDE + lane * 16); vt[r] = gld128(wsr, pb + r * KVP_STRIDE + 1024 + tl * 16); }
#pragma unroll
      for (int r = 0; r < 16; ++r) { sm += __builtin_bit_cast(f32x4, vm[r]); st += __builtin_bit_cast(f32x4, vt[r]); }
    }
    const u32 tb = lds0 + L_TAB + h * TAB_STRIDE;
    st64(tb + lane * 8, pk4(sm));
    if (lane < 42) st64(tb + (64 + lane) * 8, lane < 41 ? pk4(st) : u32x2{0u, 0u});
    waitl0(); bar();
    STAMP(6);
  }

  {
    const int rp = wave & 1, kh = wave >> 1;
    const int rt2 = rg >> 1, par2 = rg & 1;
    const u32 blk0 = rg * 4 + rp * 2;
    const u32 wr = lds0 + (4 * g + q4) * 160 + p4 * 8;
    f32x4 acc[2][2][5];
#pragma unroll
    for (int hf = 0; hf < 2; ++hf)
#pragma unroll
      for (int r = 0; r < 2; ++r)
#pragma unroll
        for (int c = 0; c < 5; ++c) acc[hf][r][c] = f32x4{0.f, 0.f, 0.f, 0.f};
    u32x4 of[10][2];
    auto step = [&](int hf, int n, u32 slot) {
#pragma unroll
      for (int c = 0; c < 5; ++c) {
        const u32x4 w = ldB(wr + slot * 5120 + c * 32, wr + slot * 5120 + 2560 + c * 32);
        acc[hf][0][c] = mma(w, of[n][0], acc[hf][0][c]); acc[hf][1][c] = mma(w, of[n][1], acc[hf][1][c]);
      }
    };
    await(ws, WS_BFLAG + ((rt2 * 2 + par2) * 16 + kh * 8 + (lane & 7)) * 8, tag);
    STAMP(7);
#pragma unroll
    for (int n = 0; n < 10; ++n)
#pragma unroll
      for (int r = 0; r < 2; ++r) of[n][r] = gld128(wsr, WS_O + (blk0 + r) * 40960 + (kh * 10 + n) * 1024 + lane * 16);
    bar();
    STAMP(8);
#pragma unroll
    for (int n = 0; n < 10; ++n) step(0, n, kh * 10 + n);
    waitl0(); bar();
    STAMP(9);
    await(ws, WS_AFLAG + ((rt2 * 4 + par2 * 2 + rp) * 8 + kh * 4 + (lane & 3)) * 8, tag);
    STAMP(10);
#pragma unroll
    for (int n = 0; n < 10; ++n)
#pragma unroll
      for (int r = 0; r < 2; ++r) of[n][r] = gld128(wsr, WS_O + (blk0 + r) * 40960 + 20480 + (kh * 10 + n) * 1024 + lane * 16);
    float gt[2];
#pragma unroll
    for (int r = 0; r < 2; ++r) gt[r] = 1.f - __builtin_bit_cast(float, gld32(wsr, WS_GATE + (kh * 4 * 128 + blk0 + r) * 64 + i16 * 4));
#pragma unroll
    for (int n = 0; n < 5; ++n) step(1, n, 20 + kh * 5 + n);
    bar();
#pragma unroll
    for (int n = 5; n < 10; ++n) step(1, n, kh * 5 + n - 5);
    STAMP(11);
    const u32 xb = lds0 + 10 * 5120;
#pragma unroll
    for (int c = 0; c < 5; ++c) {
      const f32x4 v = kh ? acc[1][0][c] + acc[0][0][c] * gt[0] : acc[1][1][c] + acc[0][1][c] * gt[1];
      *(LAS f32x4*)(size_t)(xb + wave * 5120 + c * 1024 + lane * 16) = v;
    }
    waitl0(); bar();
    const u32 blk = blk0 + kh;
    const int tok = (blk >> 3) * 128 + (blk & 3) * 32 + ((blk >> 2) & 1) + 2 * i16;
    char* orow = out + tok * 1280 + ctile * 160 + g * 8;
#pragma unroll
    for (int c = 0; c < 5; ++c) {
      f32x4 v = kh ? acc[1][1][c] + acc[0][1][c] * gt[1] : acc[1][0][c] + acc[0][0][c] * gt[0];
      v += *(LAS f32x4*)(size_t)(xb + (wave ^ 2) * 5120 + c * 1024 + lane * 16);
      *(GAS u32x2*)(orow + c * 32) = pk4(v);
    }
    STAMP(12);
    waitv<0>(); STAMP(13); STAMP_OUT;
  }
}

extern "C" void kernel_launch(void* const* d_in, const int* in_sizes, int n_in, void* d_out, int out_size, void* d_ws, size_t ws_size,
                              hipStream_t stream) {
  (void)hipFuncSetAttribute((const void*)fused_kernel, hipFuncAttributeMaxDynamicSharedMemorySize, LDS_BYTES);
  fused_kernel<<<256, 512, LDS_BYTES, stream>>>((const char*)d_in[0], (const char*)d_in[1], (const char*)d_in[2], (const char*)d_in[4],
                                                (const char*)d_in[5], (const char*)d_in[6], (char*)d_out, (char*)d_ws);
}
```

```cpp
#define DO_STAMP
#define DIAG_WG 0
#define DIAG_I 0
#include <hip/hip_runtime.h>
#include <hip/hip_bf16.h>

typedef unsigned long long u64;
typedef unsigned u32;
typedef u32 u32x2 __attribute__((ext_vector_type(2)));
typedef u32 u32x4 __attribute__((ext_vector_type(4)));
typedef float f32x2 __attribute__((ext_vector_type(2)));
typedef float f32x4 __attribute__((ext_vector_type(4)));
typedef short s16x4 __attribute__((ext_vector_type(4)));
typedef __bf16 bf16x2 __attribute__((ext_vector_type(2)));
typedef __bf16 bf16x8 __attribute__((ext_vector_type(8)));
#define LAS __attribute__((address_space(3)))
#define GAS __attribute__((address_space(1)))
#define DEV __device__ __forceinline__
#define SB() __builtin_amdgcn_sched_barrier(0)
#define RLX_AGENT __ATOMIC_RELAXED, __HIP_MEMORY_SCOPE_AGENT

#define STAMP_DECL u64 stv[16] = {};
#define CSTAMP(i) do { SB(); asm volatile("s_memtime %0\n\ts_waitcnt lgkmcnt(0)" : "=s"(stv[i]) :: "memory"); SB(); } while (0)
#define STAMP(i) do { SB(); asm volatile("s_memrealtime %0\n\ts_waitcnt lgkmcnt(0)" : "=s"(stv[i]) :: "memory"); SB(); } while (0)
#define STAMP_OUT

constexpr u32 WS_EPOCH = 0;
constexpr u32 WS_KVFLAG = 4096;
constexpr u32 WS_BFLAG = WS_KVFLAG + 512 * 8;
constexpr u32 WS_AFLAG = WS_BFLAG + 512 * 8;
constexpr u32 WS_GATE = 32768;
constexpr u32 WS_KVP = WS_GATE + 8 * 128 * 64;
constexpr u32 KVP_STRIDE = 1792;
constexpr u32 WS_O = WS_KVP + 512 * KVP_STRIDE;
constexpr u32 WS_END = WS_O + 128 * 40960;
constexpr u64 TAG_STEP = 0x9E3779B97F4A7C15ull;
constexpr u32 SPIN_CAP = 1u << 18;

constexpr u32 L_RING = 40960;
#define XSEG 5136
constexpr u32 SEG = XSEG;
constexpr u32 BX_SLOT = 18432, BW_SLOT = (XSEG==5136?15424:15360), L_BX = L_RING, L_BW = L_BX + 3 * BX_SLOT;
constexpr u32 AX_SLOT = 16384, AW_SLOT = 2*XSEG, L_AX = L_RING, L_AW = L_AX + 3 * AX_SLOT;
constexpr u32 L_MISC = 157952;
constexpr u32 L_HALOK = L_MISC, L_HALOV = L_MISC + 2560;
constexpr u32 L_GATEW = L_MISC, L_TAB = L_MISC + 1280, TAB_STRIDE = 848;
constexpr u32 L_CTL = L_MISC + 5120;
constexpr u32 L_DONE = L_CTL + 16, L_CNT = L_CTL + 64;
constexpr u32 LDS_BYTES = 163840;
static_assert(L_TAB + 4 * TAB_STRIDE <= L_CTL && L_CTL + 256 <= LDS_BYTES, "lds map");

template <int N> DEV void waitv() { asm volatile("s_waitcnt vmcnt(%0)" ::"i"(N) : "memory"); }
DEV void waitl0() { asm volatile("s_waitcnt lgkmcnt(0)" ::: "memory"); }
DEV void bar() { asm volatile("s_barrier" ::: "memory"); }
DEV void glds(const char* sbase, u32 voff, u32 lds) {
  asm volatile("s_mov_b32 m0, %2\n\ts_nop 0\n\tglobal_load_lds_dwordx4 %0, %1" ::"v"(voff), "s"(sbase), "s"(lds));
}
DEV void glds4(const char* sbase, u32 v0, u32 v1, u32 v2, u32 v3, u32 lds) {
  asm volatile("s_mov_b32 m0, %5\n\ts_nop 0\n\tglobal_load_lds_dwordx4 %0, %4\n\tglobal_load_lds_dwordx4 %1, %4 offset:1024\n\t"
               "global_load_lds_dwordx4 %2, %4 offset:2048\n\tglobal_load_lds_dwordx4 %3, %4 offset:3072"
               ::"v"(v0), "v"(v1), "v"(v2), "v"(v3), "s"(sbase), "s"(lds));
}
DEV void glds5(const char* sbase, const u32 (&v)[5], u32 lds) { glds4(sbase, v[0], v[1], v[2], v[3], lds); glds(sbase, v[4], lds + 4096); }
DEV u32x4 ld128(u32 a) { return *(LAS u32x4*)(size_t)a; }
DEV u32x2 ld64(u32 a) { return *(LAS u32x2*)(size_t)a; }
DEV void st64(u32 a, u32x2 v) { *(LAS u32x2*)(size_t)a = v; }
DEV u32x2 ldtr(u32 a) { return __builtin_bit_cast(u32x2, __builtin_amdgcn_ds_read_tr16_b64_v4i16((LAS s16x4*)(size_t)a)); }
DEV u32x4 cat(u32x2 a, u32x2 b) { return __builtin_shufflevector(a, b, 0, 1, 2, 3); }
DEV u32x4 ldB(u32 a0, u32 a1) { return cat(ldtr(a0), ldtr(a1)); }
DEV f32x4 mma(u32x4 a, u32x4 b, f32x4 c) {
  return __builtin_amdgcn_mfma_f32_16x16x32_bf16(__builtin_bit_cast(bf16x8, a), __builtin_bit_cast(bf16x8, b), c, 0, 0, 0);
}
DEV u32 pk2(float a, float b) { f32x2 v = {a, b}; return __builtin_bit_cast(u32, __builtin_convertvector(v, bf16x2)); }
DEV u32x2 pk4(f32x4 v) { return u32x2{pk2(v[0], v[1]), pk2(v[2], v[3])}; }
DEV float phi1(float t) {
  float e = __builtin_amdgcn_fmed3f(__builtin_amdgcn_exp2f(t * 1.44269504089f), 0.f, 1.f);
  return __builtin_fmaxf(t, 0.f) + (e + 1e-6f);
}
DEV f32x4 phi4(f32x4 v) { return f32x4{phi1(v[0]), phi1(v[1]), phi1(v[2]), phi1(v[3])}; }
DEV float xmax(float v) {
  u32 u = __builtin_bit_cast(u32, v);
  auto r = __builtin_amdgcn_permlane16_swap(u, u, false, false);
  v = __builtin_fmaxf(__builtin_bit_cast(float, (u32)r[0]), __builtin_bit_cast(float, (u32)r[1]));
  u = __builtin_bit_cast(u32, v);
  auto s = __builtin_amdgcn_permlane32_swap(u, u, false, false);
  return __builtin_fmaxf(__builtin_bit_cast(float, (u32)s[0]), __builtin_bit_cast(float, (u32)s[1]));
}
DEV float xsum(float v) {
  u32 u = __builtin_bit_cast(u32, v);
  auto r = __builtin_amdgcn_permlane16_swap(u, u, false, false);
  v = __builtin_bit_cast(float, (u32)r[0]) + __builtin_bit_cast(float, (u32)r[1]);
  u = __builtin_bit_cast(u32, v);
  auto s = __builtin_amdgcn_permlane32_swap(u, u, false, false);
  return __builtin_bit_cast(float, (u32)s[0]) + __builtin_bit_cast(float, (u32)s[1]);
}
DEV u32 odd_rows(u32 x) { auto r = __builtin_amdgcn_permlane16_swap(x, x, false, false); return r[1]; }

typedef __amdgpu_buffer_rsrc_t rsrc_t;
DEV u32x4 gld128(rsrc_t r, u32 off) { return __builtin_amdgcn_raw_buffer_load_b128(r, off, 0, 16); }
DEV u32 gld32(rsrc_t r, u32 off) { return __builtin_amdgcn_raw_buffer_load_b32(r, off, 0, 16); }
DEV void gst128(rsrc_t r, u32 off, u32x4 v) { __builtin_amdgcn_raw_buffer_store_b128(v, r, off, 0, 16); }
DEV void gst64(rsrc_t r, u32 off, u32x2 v) { __builtin_amdgcn_raw_buffer_store_b64(v, r, off, 0, 16); }
DEV void gst32(rsrc_t r, u32 off, u32 v) { __builtin_amdgcn_raw_buffer_store_b32(v, r, off, 0, 16); }
DEV void publish(char* ws, u32 off, u64 tag, int lane) {
  waitv<0>();
  if (lane == 0) __hip_atomic_store((GAS u64*)(ws + off), tag, RLX_AGENT);
}
DEV void await(char* ws, u32 off, u64 tag) {
  for (u32 spins = 0; spins < SPIN_CAP; ++spins) {
    u64 v = __hip_atomic_load((GAS u64*)(ws + off), RLX_AGENT);
    if (__all(v == tag)) break;
    __builtin_amdgcn_s_sleep(1);
  }
  asm volatile("" ::: "memory");
}
DEV u32 ldw(u32 a) { return __hip_atomic_load((LAS u32*)(size_t)a, __ATOMIC_RELAXED, __HIP_MEMORY_SCOPE_WORKGROUP); }
DEV void stw(u32 a, u32 v) { __hip_atomic_store((LAS u32*)(size_t)a, v, __ATOMIC_RELAXED, __HIP_MEMORY_SCOPE_WORKGROUP); }
DEV void fence_c() { asm volatile("" ::: "memory"); }
DEV void lds_arrive(u32 a, int lane) {
  if (lane == 0) __hip_atomic_fetch_add((LAS u32*)(size_t)a, 1u, __ATOMIC_RELAXED, __HIP_MEMORY_SCOPE_WORKGROUP);
}
template <int SLEEP> DEV void lds_await(u32 v, u32 a, u32 want) {
  if (__builtin_expect(!__all(v >= want), 0)) {
    for (u32 spins = 0; spins < (SPIN_CAP << 4); ++spins) {
      if (__all(ldw(a) >= want)) break;
      if (SLEEP) __builtin_amdgcn_s_sleep(SLEEP);
    }
  }
  fence_c();
}

DEV void wo_offsets(int lane, u32 (&om)[5], u32 (&os)[5]) {
#pragma unroll
  for (int i = 0; i < 5; ++i) {
    u32 c = i * 64 + lane, pos = (c * 6554u) >> 16, o = c - pos * 10;
    om[i] = (pos + 4 * (pos >> 4)) * 1280 + o * 16 - (i & 3) * 1024 * (i < 4);
    os[i] = (20 * (2 * ((pos >> 2) & 3) + (pos >> 4)) + 16 + (pos & 3)) * 1280 + o * 16 - (i & 3) * 1024 * (i < 4);
  }
}
DEV void wo_issue(const char* wo, int ks, u32 lds, const u32 (&om)[5], const u32 (&os)[5]) {
  int sg = (ks * 52) >> 8, n = ks - sg * 5;
  if (n < 4) glds5(wo + (sg * 160 + n * 40) * 1280, om, lds);
  else glds5(wo + sg * 160 * 1280, os, lds);
}

__global__ __launch_bounds__(512, 2) void fused_kernel(const char* __restrict__ X, const char* __restrict__ WA, const char* __restrict__ WoA,
                                                       const char* __restrict__ WB, const char* __restrict__ WoB, const char* __restrict__ GW,
                                                       char* out, char* ws) {
  extern __shared__ __attribute__((aligned(16))) char smem[];
  const u32 lds0 = (u32)(size_t)(LAS char*)smem;
  const int b = blockIdx.x, xcd = b & 7, jj = b >> 3;
  const int rt = (xcd & 1) * 8 + (jj & 7), ct = (xcd >> 1) * 4 + (jj >> 3);
  const int r0t = ((jj & 7) * 5 + (jj >> 3) * 10) >> 2, r0 = r0t >= 10 ? r0t - 10 : r0t;
  const bool isB = ct >= 8;
  const int hg = ct & 7;
  const int wave = __builtin_amdgcn_readfirstlane(threadIdx.x >> 6), lane = threadIdx.x & 63;
  const int g = lane >> 4, i16 = lane & 15, q4 = (lane >> 2) & 3, p4 = lane & 3;
  const rsrc_t wsr = __builtin_amdgcn_make_buffer_rsrc(ws, 0, WS_END, 0x00020000);
  const u32 aA0 = i16 * 128 + ((g ^ ((i16 >> 1) & 7)) << 4);
  const u32 rB0 = (8 * g + (q4 ^ (4 * (g & 1)))) * 160, rB1 = (8 * g + ((4 + q4) ^ (4 * (g & 1)))) * 160;
  const int rg = xcd * 4 + (jj >> 3), ctile = jj & 7;
  u64 tag;
  STAMP_DECL
  STAMP(0);

  if (wave >= 4) {
    const int lw = wave - 4;
    __builtin_amdgcn_s_setprio(3);
    u32 ow[5];
#pragma unroll
    for (int i = 0; i < 5; ++i) {
      u32 c = i * 64 + lane, pos = (c * 6554u) >> 16, o = c - pos * 10;
      ow[i] = (pos ^ (((pos >> 3) & 1) << 2)) * 3840 + o * 16 - (i & 3) * 1024 * (i < 4);
    }
    const u32 ox0 = (lane >> 3) * 2560 + (((lane & 7) ^ (lane >> 4)) << 4), ox1 = (ox0 ^ 64) + 20480 - 1024;
    const u32 ox2 = ox0 + 2 * (20480 - 1024), ox3 = ox1 + 2 * (20480 - 1024);
    const char* Wt = (isB ? WB : WA) + hg * 160;
    auto ksof = [&](int s) { int k = s + r0; return k >= 10 ? k - 10 : k; };
    auto xquad = [&](int s, int qd, u32 slotbase) {
      glds4(X + (128 * rt + 64 * (qd & 1) + (qd >> 1)) * 1280 + ksof(s) * 128, ox0, ox1, ox2, ox3, slotbase + qd * 4096);
    };
    auto wseg = [&](int u, int seg, u32 dst) { glds5(Wt + (ksof(u >> 1) * 64 + (u & 1) * 32) * 3840 + seg * 1280, ow, dst); };
    if (isB) {
      if (lw == 0) {
        u32 wk = r0 * (64 * 3840), xk = r0 * 128, xs = lds0 + L_BX;
        const char* Xt = X + rt * (128 * 1280); const char* Xh = rt ? Xt - 16 * 1280 : Xt;
        auto issue_x = [&](int par) {
          const char* p = Xt + par * 1280 + xk;
          glds4(p, ox0, ox1, ox2, ox3, xs + par * 8192); glds4(p + 64 * 1280, ox0, ox1, ox2, ox3, xs + par * 8192 + 4096);
          glds(Xh + par * 1280 + xk, ox0 ^ (par << 6), xs + 16384 + par * 1024);
          if (par) { xk = xk == 9 * 128 ? 0 : xk + 128; xs = xs == lds0 + L_BX + 2 * BX_SLOT ? lds0 + L_BX : xs + BX_SLOT; }
        };
        const u32 da = lds0 + L_DONE + ((lane & 7) == 4 ? 0 : (lane & 7)) * 4;
        issue_x(0);
        u32 dv = 0;
#pragma nounroll
        for (int u = 0; u < 20; ++u) {
          if (u >= 4) lds_await<0>(dv, da, u - 3);
          const char* p = Wt + wk; const u32 d = lds0 + L_BW + (u & 3) * BW_SLOT;
          glds5(p, ow, d); glds5(p + 1280, ow, d + SEG);
          if (u == 0) bar();
          else { waitv<10>(); stw(lds0 + L_CNT + (u - 1) * 4, 4); }
          glds5(p + 2560, ow, d + 2 * SEG);
          wk = wk == 608 * 3840 ? 0 : wk + 32 * 3840;
          dv = ldw(da);
          if (u < 19) issue_x((u + 1) & 1);
        }
        waitv<0>(); stw(lds0 + L_CNT + 19 * 4, 4);
      } else {
        const int fb = lw == 1 ? 0 : lw * 3 - 2;
        u32 ch[4];
#pragma unroll
        for (int f = 0; f < 4; ++f) { const int ph = fb + f; ch[f] = (ph >= 5 ? 2 * SEG + (ph - 5) * 32 : SEG + ph * 32) + p4 * 8; }
        f32x4 h[4] = {};
        u32 xs = lds0 + L_BX + 8 * 2048;
        bar();
#pragma nounroll
        for (int u = 0; u < 20; ++u) {
          lds_await<1>(0, lds0 + L_CNT + u * 4, 4);
          const u32 wb = lds0 + L_BW + (u & 3) * BW_SLOT;
          const u32x4 ah = ld128(xs + (aA0 ^ ((u & 1) << 6)));
          u32x4 w[4];
#pragma unroll
          for (int f = 0; f < 3; ++f) w[f] = ldB(wb + rB0 + ch[f], wb + rB1 + ch[f]);
          if (lw == 1) w[3] = ldB(wb + rB0 + ch[3], wb + rB1 + ch[3]);
          fence_c(); stw(lds0 + L_DONE + wave * 4, u + 1);
#pragma unroll
          for (int f = 0; f < 3; ++f) h[f] = mma(w[f], ah, h[f]);
          if (lw == 1) h[3] = mma(w[3], ah, h[3]);
          if (u & 1) xs = xs == lds0 + L_BX + 8 * 2048 + 2 * BX_SLOT ? lds0 + L_BX + 8 * 2048 : xs + BX_SLOT;
        }
#pragma unroll
        for (int f = 0; f < 4; ++f) {
          if (f < 3 || lw == 1) {
            const int ph = fb + f;
            const u32x2 v = pk4(h[f]);
            if (ph < 5) st64(lds0 + L_HALOK + ((ph * 4 + g) * 16 + i16) * 8, v);
            else {
              LAS unsigned short* d = (LAS unsigned short*)(size_t)(lds0 + L_HALOV + ((ph - 5) * 16 + 4 * g) * 32 + i16 * 2);
              d[0] = (unsigned short)v[0]; d[16] = (unsigned short)(v[0] >> 16); d[32] = (unsigned short)v[1]; d[48] = (unsigned short)(v[1] >> 16);
            }
          }
        }
      }
      waitl0(); bar();
      tag = *(LAS u64*)(size_t)(lds0 + L_CTL);
    } else {
      auto xhalf = [&](int n) { xquad(n >> 1, (n & 1) * 2 + (lw & 1), lds0 + L_AX + ((n >> 1) % 3) * AX_SLOT); };
      auto unit = [&](int u) {
        if (lw < 2) wseg(u, 1 + lw, lds0 + L_AW + (u & 3) * AW_SLOT + lw * SEG);
        else if (u < 19) xhalf(u + 1);
      };
      if (lw == 1) { glds(GW, lane * 16, lds0 + L_GATEW); if (lane < 16) glds(GW, 1024 + lane * 16, lds0 + L_GATEW + 1024); }
      if (lw >= 2) xhalf(0);
      unit(0); unit(1);
      u32x4 xr[20][2];
      f32x4 gl[2] = {};
      u32x4 gw;
      const u32 da = lds0 + L_DONE + (lane & 7) * 4;
      bar();
      if (lw < 2) waitv<5>(); else waitv<4>();
      lds_arrive(lds0 + L_CNT, lane);
      u32 dv = 0;
#pragma unroll
      for (int u = 0; u < 20; ++u) {
        if (u + 2 < 20) {
          if (u >= 2) lds_await<0>(dv, da, u - 1);
          unit(u + 2);
        } else wseg((u - 18) * 4 + lw, 0, lds0 + ((u - 18) * 4 + lw) * 5120);
        if (u + 2 < 19) { if (lw < 2) waitv<5>(); else waitv<4>(); }
        else if (u + 2 == 19) { if (lw < 2) waitv<5>(); else waitv<0>(); }
        else waitv<5>();
        lds_arrive(lds0 + L_CNT + (u + 1) * 4, lane);
        if (u > 0) { gl[0] = mma(gw, xr[u - 1][0], gl[0]); gl[1] = mma(gw, xr[u - 1][1], gl[1]); }
        lds_await<0>(0, lds0 + L_CNT + u * 4, 4);
        const u32 xa = lds0 + L_AX + ((u >> 1) % 3) * AX_SLOT + lw * 4096 + (aA0 ^ ((u & 1) << 6));
        xr[u][0] = ld128(xa); xr[u][1] = ld128(xa + 2048);
        gw = ld128(lds0 + L_GATEW + ksof(u >> 1) * 128 + (u & 1) * 64 + g * 16);
        fence_c(); stw(lds0 + L_DONE + wave * 4, u + 1);
        dv = ldw(da);
        SB();
      }
      gl[0] = mma(gw, xr[19][0], gl[0]); gl[1] = mma(gw, xr[19][1], gl[1]);
      __builtin_amdgcn_s_setprio(0);
      STAMP(1);
      lds_await<0>(dv, da, 20);
      f32x4 qa[2][5] = {};
      const u32 cQ = p4 * 8, cQ5 = 32 + p4 * 40;
#pragma unroll
      for (int r = 0; r < 5; ++r) {
        if (r < 3) { wseg((r + 2) * 4 + lw, 0, lds0 + ((r + 2) * 4 + lw) * 5120); waitv<5>(); } else waitv<0>();
        if (r < 4) lds_arrive(lds0 + L_CNT + (21 + r) * 4, lane);
        lds_await<0>(0, lds0 + L_CNT + (20 + r) * 4, 4);
#pragma unroll
        for (int n = 0; n < 4; ++n) {
          const int u = r * 4 + n; const u32 wb = lds0 + u * 5120;
#pragma unroll
          for (int f = 0; f < 5; ++f) {
            const u32 c = f < 4 ? cQ + f * 40 : cQ5;
            u32x4 w = ldB(wb + rB0 + c, wb + rB1 + c);
            qa[0][f] = mma(w, xr[u][0], qa[0][f]); qa[1][f] = mma(w, xr[u][1], qa[1][f]);
          }
        }
      }
      STAMP(2);
      u32x2 pq[2][5];
#pragma unroll
      for (int r = 0; r < 2; ++r)
#pragma unroll
        for (int f = 0; f < 5; ++f) pq[r][f] = pk4(phi4(qa[r][f]));
      float gate[2];
#pragma unroll
      for (int r = 0; r < 2; ++r) gate[r] = __builtin_amdgcn_rcpf(1.f + __builtin_amdgcn_exp2f(gl[r][0] * -1.44269504089f));
      waitl0(); bar();
      STAMP(3);
      tag = *(LAS u64*)(size_t)(lds0 + L_CTL);
      f32x4 n5[2] = {};
      u32x2 ob[2][4];
      float sc5[2] = {0.f, 0.f};
#pragma unroll
      for (int h = 0; h < 4; ++h) {
        const u32 tb = lds0 + L_TAB + h * TAB_STRIDE;
        const bool gh = g == h, eh = q4 == (h ^ 1);
        u32x4 a0 = cat(ld64(tb + lane * 8), ld64(tb + (gh ? 64 + i16 : 105) * 8));
        u32x4 a5 = cat(ld64(tb + (eh ? 80 + p4 + 4 * g : 105) * 8), ld64(tb + (eh && gh ? 100 + p4 : 105) * 8));
        u32x4 az = cat(ld64(tb + (96 + g) * 8), ld64(tb + (gh ? 104 : 105) * 8));
#pragma unroll
        for (int r = 0; r < 2; ++r) {
          u32x4 bo = cat(pq[r][h], pq[r][4]);
          f32x4 num = mma(a0, bo, f32x4{0.f, 0.f, 0.f, 0.f});
          f32x4 den = mma(az, bo, f32x4{1e-6f, 1e-6f, 1e-6f, 1e-6f});
          n5[r] = mma(a5, bo, n5[r]);
          float sc = gate[r] * __builtin_amdgcn_rcpf(den[0]);
          ob[r][h] = pk4(num * sc);
          sc5[r] = (g ^ 1) == h ? sc : sc5[r];
        }
      }
      STAMP(5);
#pragma unroll
      for (int r = 0; r < 2; ++r) {
        const u32 blk = rt * 8 + lw * 2 + r;
        const u32 base = WS_O + blk * 40960 + 20480 + (hg >> 1) * 5120;
        gst128(wsr, base + (hg & 1) * 2048 + lane * 16, cat(ob[r][0], ob[r][1]));
        gst128(wsr, base + (hg & 1) * 2048 + 1024 + lane * 16, cat(ob[r][2], ob[r][3]));
        gst64(wsr, base + 4096 + ((2 * (hg & 1) + (g >> 1)) * 16 + i16) * 16 + ((g ^ 1) & 1) * 8, pk4(n5[r] * sc5[r]));
        if (lane < 16) gst32(wsr, WS_GATE + (hg * 128 + blk) * 64 + lane * 4, __builtin_bit_cast(u32, gate[r]));
      }
      STAMP(6);
      publish(ws, WS_AFLAG + ((rt * 4 + lw) * 8 + hg) * 8, tag, lane);
      STAMP(4);
    }
    u32 om[5], os[5];
    wo_offsets(lane, om, os);
    const char* wob = WoB + ctile * 160; const char* woa = WoA + ctile * 160;
#pragma nounroll
    for (int s = lw; s < 30; s += 4) {
      if (s < 20) wo_issue(wob, s, lds0 + s * 5120, om, os);
      else wo_issue(woa, s < 25 ? s - 20 : s - 15, lds0 + s * 5120, om, os);
    }
    STAMP(7);
    waitv<0>(); STAMP(8); bar();
    bar();
    STAMP(9);
#pragma nounroll
    for (int s = lw; s < 10; s += 4) wo_issue(woa, s < 5 ? s + 5 : s + 10, lds0 + s * 5120, om, os);
    waitv<0>(); STAMP(10); bar();
    bar();
    if (b == 0 && lw == 0) {
#pragma unroll
      for (int r = 0; r < 2; ++r) {
        const int n = r * 64 + lane;
        await(ws, WS_BFLAG + (((n >> 3) * 2 * 8 + (n & 7)) * 2) * 8, tag);
        await(ws, WS_AFLAG + (((n >> 3) * 4) * 8 + (n & 7)) * 8, tag);
      }
      if (lane == 0) __hip_atomic_store((GAS u64*)(ws + WS_EPOCH), tag, RLX_AGENT);
    }
    STAMP(13); STAMP_OUT;
    return;
  }

  if (wave == 0 && lane < 44) stw(lds0 + L_DONE + lane * 4, 0);
  {
    const u64 e = __hip_atomic_load((GAS u64*)(ws + WS_EPOCH), RLX_AGENT);
    waitl0(); bar();
    tag = (((u64)__builtin_amdgcn_readfirstlane((u32)(e >> 32)) << 32) | __builtin_amdgcn_readfirstlane((u32)e)) + TAG_STEP;
    if (wave == 0 && lane == 0) *(LAS u64*)(size_t)(lds0 + L_CTL) = tag;
  }
  if (isB) {
    const int par = wave & 1, hp = wave >> 1;
    const u32 cM = hp * 80 + p4 * 8, cX = hp * 80 + 32 + (p4 & 1) * SEG + (p4 >> 1) * 40, cY = 2 * SEG + hp * 80 + 32 + (p4 & 1) * 40;
    f32x4 acc[4][8];
#pragma unroll
    for (int r = 0; r < 4; ++r)
#pragma unroll
      for (int f = 0; f < 8; ++f) acc[r][f] = f32x4{0.f, 0.f, 0.f, 0.f};
    struct Fr { u32x4 a[4], w[8]; };
    auto rd = [&](Fr& F, u32 xs, int u, int kk) {
      const u32 aa = xs + (aA0 ^ (kk << 6)) + par * 8192;
      const u32 b0 = lds0 + L_BW + (u & 3) * BW_SLOT + rB0, b1 = lds0 + L_BW + (u & 3) * BW_SLOT + rB1;
#pragma unroll
      for (int r = 0; r < 4; ++r) F.a[r] = ld128(aa + r * 2048);
      F.w[0] = ldB(b0 + cM, b1 + cM); F.w[1] = ldB(b0 + cM + 40, b1 + cM + 40);
      F.w[2] = ldB(b0 + cM + SEG, b1 + cM + SEG); F.w[3] = ldB(b0 + cM + SEG + 40, b1 + cM + SEG + 40);
      F.w[4] = ldB(b0 + cX, b1 + cX);
      F.w[5] = ldB(b0 + cM + 2 * SEG, b1 + cM + 2 * SEG); F.w[6] = ldB(b0 + cM + 2 * SEG + 40, b1 + cM + 2 * SEG + 40);
      F.w[7] = ldB(b0 + cY, b1 + cY);
    };
    auto mm = [&](Fr& F, int f0, int f1) {
#pragma unroll
      for (int f = f0; f < f1; ++f)
#pragma unroll
        for (int r = 0; r < 4; ++r) acc[r][f] = f < 5 ? mma(F.w[f], F.a[r], acc[r][f]) : mma(F.a[r], F.w[f], acc[r][f]);
    };
    auto ilv = [&]() {
#pragma unroll
      for (int n = 0; n < 22; ++n) { __builtin_amdgcn_sched_group_barrier(0x8, 1, 0); __builtin_amdgcn_sched_group_barrier(0x80, 1, 0); }
      __builtin_amdgcn_sched_group_barrier(0x8, 6, 0);
    };
    Fr F0, F1;
    u32 xs = lds0 + L_BX, ca = lds0 + L_CNT;
    const u32 dn = lds0 + L_DONE + wave * 4;
#pragma unroll
    for (int r = 0; r < 4; ++r)
#pragma unroll
      for (int f = 0; f < 8; ++f) asm volatile("" : "+v"(acc[r][f]));
    lds_await<1>(0, ca, 4);
    STAMP(1); CSTAMP(14);
    rd(F0, xs, 0, 0); fence_c(); stw(dn, 1);
    u32 cv = ldw(ca + 4);
#pragma nounroll
    for (int s = 0; s < 10; ++s) {
      SB();
      mm(F0, 0, 1); SB();
      lds_await<0>(cv, ca + 4, 4); SB();
      rd(F1, xs, 2 * s + 1, 1); fence_c(); stw(dn, 2 * s + 2); cv = ldw(ca + 8); mm(F0, 1, 8); ilv(); SB();
      mm(F1, 0, 1); SB();
      if (s < 9) lds_await<0>(cv, ca + 8, 4);
      SB();
      xs = xs == lds0 + L_BX + 2 * BX_SLOT ? lds0 + L_BX : xs + BX_SLOT;
      rd(F0, xs, 2 * s + 2, 0); fence_c(); stw(dn, 2 * s + 3); cv = ldw(ca + 12); mm(F1, 1, 8); ilv();
      ca += 8;
      SB();
    }
    STAMP(2); CSTAMP(15);
    u32x2 P[4][8];
#pragma unroll
    for (int r = 0; r < 4; ++r)
#pragma unroll
      for (int f = 0; f < 8; ++f) P[r][f] = pk4(acc[r][f]);
    STAMP(5);
    waitl0(); bar();
    STAMP(6);
    u32x2 pKa, pKb, pK4, pVa, pVb, pY;
    {
      const u32 kt = lds0 + L_HALOK + ((i16 & 7) + 8 * par) * 8, vt = lds0 + L_HALOV + ((g & 1) + 2 * par) * 8;
      pKa = ld64(kt + (10 * hp + g) * 128); pKb = ld64(kt + (10 * hp + 5 + g) * 128); pK4 = ld64(kt + (10 * hp + 4 + 5 * (g >> 1)) * 128);
      pVa = ld64(vt + (40 * hp + i16) * 32); pVb = ld64(vt + (40 * hp + 20 + i16) * 32);
      pY = ld64(vt + (40 * hp + 16 + 20 * (q4 & 1) + p4) * 32);
    }
    f32x4 bc, bp;
    u32x2 mp;
#pragma unroll
    for (int j = 0; j < 4; ++j) {
      const int p = 4 * g + j;
      bc[j] = (p <= i16 && p >= i16 - 7) ? 0.f : -1e30f;
      bp[j] = p >= i16 + 9 ? 0.f : -1e30f;
    }
    mp[0] = (4 * g >= i16 + 9 ? 0xFFFFu : 0u) | (4 * g + 1 >= i16 + 9 ? 0xFFFF0000u : 0u);
    mp[1] = (4 * g + 2 >= i16 + 9 ? 0xFFFFu : 0u) | (4 * g + 3 >= i16 + 9 ? 0xFFFF0000u : 0u);
    const float cs = 0.22360679775f * 1.44269504089f;
    const u32x2 zero2 = {0u, 0u};
#pragma unroll
    for (int r = 0; r < 4; ++r) {
      const u32x2 K4 = {odd_rows(P[r][4][0]), odd_rows(P[r][4][1])};
      f32x4 bpr = bp;
      if (r == 0 && rt == 0) bpr = f32x4{-1e30f, -1e30f, -1e30f, -1e30f};
      u32x4 pb[2];
#pragma unroll
      for (int h = 0; h < 2; ++h) {
        const u32x4 qo = cat(P[r][h], g == 2 * h ? P[r][4] : zero2);
        f32x4 sc = mma(cat(P[r][2 + h], K4), qo, bc);
        f32x4 sp = mma(cat(h ? pKb : pKa, pK4), qo, bpr);
        f32x4 m;
#pragma unroll
        for (int j = 0; j < 4; ++j) m[j] = __builtin_fmaxf(sc[j], sp[j]);
        const float mx = xmax(__builtin_fmaxf(__builtin_fmaxf(m[0], m[1]), __builtin_fmaxf(m[2], m[3]))) * cs;
#pragma unroll
        for (int j = 0; j < 4; ++j) m[j] = __builtin_amdgcn_exp2f(__builtin_fmaf(m[j], cs, -mx));
        const float rs = __builtin_amdgcn_rcpf(xsum((m[0] + m[1]) + (m[2] + m[3])));
        const u32x2 pp = pk4(m * rs);
        pb[h] = cat(pp & mp, pp & ~mp);
      }
      const f32x4 z4 = {0.f, 0.f, 0.f, 0.f};
      const u32x4 yo = cat(pY, P[r][7]);
      f32x4 oa = mma(cat(pVa, P[r][5]), pb[0], z4), obb = mma(cat(pVb, P[r][6]), pb[1], z4);
      f32x4 ya = mma(yo, pb[0], z4), yb = mma(yo, pb[1], z4);
      const u32 base = WS_O + (rt * 8 + par * 4 + r) * 40960 + (hg >> 1) * 5120;
      const int pi = 2 * (hg & 1) + hp;
      gst128(wsr, base + pi * 1024 + lane * 16, cat(pk4(oa), pk4(obb)));
      if (g < 2) gst64(wsr, base + 4096 + (pi * 16 + i16) * 16 + g * 8, pk4(g ? yb : ya));
      pKa = P[r][2]; pKb = P[r][3]; pK4 = K4; pVa = P[r][5]; pVb = P[r][6]; pY = P[r][7];
    }
    STAMP(3);
    publish(ws, WS_BFLAG + (((rt * 2 + par) * 8 + hg) * 2 + hp) * 8, tag, lane);
    STAMP(4);
  } else {
    const int h = wave;
    const u32 cK = h * 40 + p4 * 8, cZ = h * 40 + 32 + (p4 == (h ^ 1) ? SEG : 0);
    f32x4 acc[8][3];
#pragma unroll
    for (int r = 0; r < 8; ++r)
#pragma unroll
      for (int f = 0; f < 3; ++f) acc[r][f] = f32x4{0.f, 0.f, 0.f, 0.f};
    struct Fr { u32x4 a[8], w[3]; };
    auto rd = [&](Fr& F, u32 xs, int u, int kk) {
      const u32 aa = xs + (aA0 ^ (kk << 6));
      const u32 b0 = lds0 + L_AW + (u & 3) * AW_SLOT + rB0, b1 = lds0 + L_AW + (u & 3) * AW_SLOT + rB1;
      F.w[0] = ldB(b0 + cK, b1 + cK); F.w[1] = ldB(b0 + cK + SEG, b1 + cK + SEG); F.w[2] = ldB(b0 + cZ, b1 + cZ);
#pragma unroll
      for (int r = 0; r < 8; ++r) F.a[r] = ld128(aa + r * 2048);
    };
    auto mm = [&](Fr& F, int r0_, int r1_) {
#pragma unroll
      for (int r = r0_; r < r1_; ++r)
#pragma unroll
        for (int f = 0; f < 3; ++f) acc[r][f] = mma(F.a[r], F.w[f], acc[r][f]);
    };
    auto ilv = [&]() {
#pragma unroll
      for (int n = 0; n < 16; ++n) { __builtin_amdgcn_sched_group_barrier(0x8, 1, 0); __builtin_amdgcn_sched_group_barrier(0x80, 1, 0); }
      __builtin_amdgcn_sched_group_barrier(0x8, 5, 0);
    };
    Fr F0, F1;
    u32 xs = lds0 + L_AX, ca = lds0 + L_CNT;
    const u32 dn = lds0 + L_DONE + wave * 4;
#pragma unroll
    for (int r = 0; r < 8; ++r)
#pragma unroll
      for (int f = 0; f < 3; ++f) asm volatile("" : "+v"(acc[r][f]));
    lds_await<1>(0, ca, 4);
    STAMP(1); CSTAMP(14);
    rd(F0, xs, 0, 0); fence_c(); stw(dn, 1);
    u32 cv = ldw(ca + 4);
#pragma nounroll
    for (int s = 0; s < 10; ++s) {
      SB();
      mm(F0, 0, 1); SB();
      lds_await<0>(cv, ca + 4, 4); SB();
      rd(F1, xs, 2 * s + 1, 1); fence_c(); stw(dn, 2 * s + 2); cv = ldw(ca + 8); mm(F0, 1, 8); ilv(); SB();
      mm(F1, 0, 1); SB();
      if (s < 9) lds_await<0>(cv, ca + 8, 4);
      SB();
      xs = xs == lds0 + L_AX + 2 * AX_SLOT ? lds0 + L_AX : xs + AX_SLOT;
      rd(F0, xs, 2 * s + 2, 0); fence_c(); stw(dn, 2 * s + 3); cv = ldw(ca + 12); mm(F1, 1, 8); ilv();
      ca += 8;
      SB();
    }
    STAMP(2); CSTAMP(15);
    __builtin_amdgcn_s_setprio(2);
    const bool eh = q4 == (h ^ 1), gh = g == h, zc = i16 == 4 * h;
    const u32x2 ones = {0x3F803F80u, 0x3F803F80u};
    f32x4 t00 = {}, t01 = {}, t10 = {}, t11 = {};
#pragma unroll
    for (int k = 0; k < 4; ++k) {
      const u32x4 pk = cat(pk4(phi4(acc[2 * k][0])), pk4(phi4(acc[2 * k + 1][0])));
      const u32x4 pz = cat(pk4(phi4(acc[2 * k][2])), pk4(phi4(acc[2 * k + 1][2])));
      const u32x4 vv = cat(pk4(acc[2 * k][1]), pk4(acc[2 * k + 1][1]));
      const u32x2 z0 = pk4(acc[2 * k][2]), z1 = pk4(acc[2 * k + 1][2]);
      const u32x4 zb = cat(eh ? z0 : ones, eh ? z1 : ones);
      t00 = mma(pk, vv, t00); t01 = mma(pk, zb, t01); t10 = mma(pz, vv, t10); t11 = mma(pz, zb, t11);
    }
    const u32 pb = WS_KVP + (hg * 4 + h) * 16 * KVP_STRIDE;
    {
      const u32 o = pb + rt * KVP_STRIDE;
      gst128(wsr, o + lane * 16, __builtin_bit_cast(u32x4, t00));
      if (gh) gst128(wsr, o + (64 + i16) * 16, __builtin_bit_cast(u32x4, t10));
      if (eh || zc) gst128(wsr, o + (eh ? 80 + p4 + 4 * g : 96 + g) * 16, __builtin_bit_cast(u32x4, t01));
      if (gh && (eh || zc)) gst128(wsr, o + (eh ? 100 + p4 : 104) * 16, __builtin_bit_cast(u32x4, t11));
    }
    STAMP(3);
    publish(ws, WS_KVFLAG + ((hg * 4 + h) * 16 + rt) * 8, tag, lane);
    STAMP(4);
    await(ws, WS_KVFLAG + ((hg * 4 + h) * 16 + i16) * 8, tag);
    STAMP(5);
    f32x4 sm = {}, st = {};
    {
      u32x4 vm[16], vt[16];
      const u32 tl = lane < 41 ? lane : 40;
#pragma unroll
      for (int r = 0; r < 16; ++r) { vm[r] = gld128(wsr, pb + r * KVP_STRIDE + lane * 16); vt[r] = gld128(wsr, pb + r * KVP_STRIDE + 1024 + tl * 16); }
#pragma unroll
      for (int r = 0; r < 16; ++r) { sm += __builtin_bit_cast(f32x4, vm[r]); st += __builtin_bit_cast(f32x4, vt[r]); }
    }
    const u32 tb = lds0 + L_TAB + h * TAB_STRIDE;
    st64(tb + lane * 8, pk4(sm));
    if (lane < 42) st64(tb + (64 + lane) * 8, lane < 41 ? pk4(st) : u32x2{0u, 0u});
    waitl0(); bar();
    STAMP(6);
  }

  {
    const int rp = wave & 1, kh = wave >> 1;
    const int rt2 = rg >> 1, par2 = rg & 1;
    const u32 blk0 = rg * 4 + rp * 2;
    const u32 wr = lds0 + (4 * g + q4) * 160 + p4 * 8;
    f32x4 acc[2][2][5];
#pragma unroll
    for (int hf = 0; hf < 2; ++hf)
#pragma unroll
      for (int r = 0; r < 2; ++r)
#pragma unroll
        for (int c = 0; c < 5; ++c) acc[hf][r][c] = f32x4{0.f, 0.f, 0.f, 0.f};
    u32x4 of[10][2];
    auto step = [&](int hf, int n, u32 slot) {
#pragma unroll
      for (int c = 0; c < 5; ++c) {
        const u32x4 w = ldB(wr + slot * 5120 + c * 32, wr + slot * 5120 + 2560 + c * 32);
        acc[hf][0][c] = mma(w, of[n][0], acc[hf][0][c]); acc[hf][1][c] = mma(w, of[n][1], acc[hf][1][c]);
      }
    };
    await(ws, WS_BFLAG + ((rt2 * 2 + par2) * 16 + kh * 8 + (lane & 7)) * 8, tag);
    STAMP(7);
#pragma unroll
    for (int n = 0; n < 10; ++n)
#pragma unroll
      for (int r = 0; r < 2; ++r) of[n][r] = gld128(wsr, WS_O + (blk0 + r) * 40960 + (kh * 10 + n) * 1024 + lane * 16);
    bar();
    STAMP(8);
#pragma unroll
    for (int n = 0; n < 10; ++n) step(0, n, kh * 10 + n);
    waitl0(); bar();
    STAMP(9);
    await(ws, WS_AFLAG + ((rt2 * 4 + par2 * 2 + rp) * 8 + kh * 4 + (lane & 3)) * 8, tag);
    STAMP(10);
#pragma unroll
    for (int n = 0; n < 10; ++n)
#pragma unroll
      for (int r = 0; r < 2; ++r) of[n][r] = gld128(wsr, WS_O + (blk0 + r) * 40960 + 20480 + (kh * 10 + n) * 1024 + lane * 16);
    float gt[2];
#pragma unroll
    for (int r = 0; r < 2; ++r) gt[r] = 1.f - __builtin_bit_cast(float, gld32(wsr, WS_GATE + (kh * 4 * 128 + blk0 + r) * 64 + i16 * 4));
#pragma unroll
    for (int n = 0; n < 5; ++n) step(1, n, 20 + kh * 5 + n);
    bar();
#pragma unroll
    for (int n = 5; n < 10; ++n) step(1, n, kh * 5 + n - 5);
    STAMP(11);
    const u32 xb = lds0 + 10 * 5120;
#pragma unroll
    for (int c = 0; c < 5; ++c) {
      const f32x4 v = kh ? acc[1][0][c] + acc[0][0][c] * gt[0] : acc[1][1][c] + acc[0][1][c] * gt[1];
      *(LAS f32x4*)(size_t)(xb + wave * 5120 + c * 1024 + lane * 16) = v;
    }
    waitl0(); bar();
    const u32 blk = blk0 + kh;
    const int tok = (blk >> 3) * 128 + (blk & 3) * 32 + ((blk >> 2) & 1) + 2 * i16;
    char* orow = out + tok * 1280 + ctile * 160 + g * 8;
#pragma unroll
    for (int c = 0; c < 5; ++c) {
      f32x4 v = kh ? acc[1][1][c] + acc[0][1][c] * gt[1] : acc[1][0][c] + acc[0][0][c] * gt[0];
      v += *(LAS f32x4*)(size_t)(xb + (wave ^ 2) * 5120 + c * 1024 + lane * 16);
      *(GAS u32x2*)(orow + c * 32) = pk4(v);
    }
    STAMP(12);
    waitv<0>(); STAMP(13); STAMP_OUT;
    if (b == DIAG_WG && wave == 0) { const u64 until = stv[0] + 6000 + 4 * (stv[DIAG_I] - stv[0]); while (__builtin_amdgcn_s_memrealtime() < until) __builtin_amdgcn_s_sleep(4); }
  }
}

extern "C" void kernel_launch(void* const* d_in, const int* in_sizes, int n_in, void* d_out, int out_size, void* d_ws, size_t ws_size,
                              hipStream_t stream) {
  (void)hipFuncSetAttribute((const void*)fused_kernel, hipFuncAttributeMaxDynamicSharedMemorySize, LDS_BYTES);
  fused_kernel<<<256, 512, LDS_BYTES, stream>>>((const char*)d_in[0], (const char*)d_in[1], (const char*)d_in[2], (const char*)d_in[4],
                                                (const char*)d_in[5], (const char*)d_in[6], (char*)d_out, (char*)d_ws);
}
```

```cpp
#define ROT 5
#include <hip/hip_runtime.h>
#include <hip/hip_bf16.h>

typedef unsigned long long u64;
typedef unsigned u32;
typedef u32 u32x2 __attribute__((ext_vector_type(2)));
typedef u32 u32x4 __attribute__((ext_vector_type(4)));
typedef float f32x2 __attribute__((ext_vector_type(2)));
typedef float f32x4 __attribute__((ext_vector_type(4)));
typedef short s16x4 __attribute__((ext_vector_type(4)));
typedef __bf16 bf16x2 __attribute__((ext_vector_type(2)));
typedef __bf16 bf16x8 __attribute__((ext_vector_type(8)));
#define LAS __attribute__((address_space(3)))
#define GAS __attribute__((address_space(1)))
#define DEV __device__ __forceinline__
#define SB() __builtin_amdgcn_sched_barrier(0)
#define PIN(x) asm volatile("" : "+v"(x))
#define RLX_AGENT __ATOMIC_RELAXED, __HIP_MEMORY_SCOPE_AGENT

#define STAMP_DECL
#define CSTAMP(i)
#define STAMP(i)
#define STAMP_OUT
#define TAILG 18
#define A_G0W 10
#define PACK_AT 0
#define A_PW 17
#define TOKS 0
#define AVN 4
#define QGATE 4
#define OUT_AUX 16
#define B_R1 19
#define POLL_SLEEP 2

constexpr u32 WS_EPOCH = 0;
constexpr u32 WS_KVFLAG = 4096;
constexpr u32 WS_BFLAG = WS_KVFLAG + 512 * 8;
constexpr u32 WS_AFLAG = WS_BFLAG + 1024 * 8;
constexpr u32 WS_PFLAG = WS_AFLAG + 512 * 8;
constexpr u32 WS_GATE = 32768;
constexpr u32 WS_KVP = WS_GATE + 8 * 128 * 64;
constexpr u32 KVP_STRIDE = 896;
constexpr u32 WS_O = WS_KVP + 512 * KVP_STRIDE;
constexpr u32 WS_WOP = WS_O + 128 * 40960;
constexpr u32 WS_WQP = WS_WOP + 1600 * 1024;
constexpr u32 WS_END = WS_WQP + 800 * 1024;
constexpr u64 TAG_STEP = 0x9E3779B97F4A7C15ull;
constexpr u32 SPIN_CAP = 1u << 18;
constexpr u32 OOB = 0x80000000u;

constexpr u32 SEG = 5136;
constexpr u32 BX_SLOT = 18432, BW_SLOT = 15424, L_BX = 36864, L_BW = L_BX + 3 * BX_SLOT;
constexpr u32 AX_SLOT = 16384, AW_SLOT = 2 * SEG, L_AX = 47104, L_AW = L_AX + 4 * AX_SLOT;
constexpr u32 L_MISC = L_BW + 4 * BW_SLOT;
constexpr u32 L_HO = L_BX + BX_SLOT;
constexpr u32 HO_STRIDE = 11264, L_YX = L_MISC;
constexpr u32 L_GATEW = L_MISC, L_TAB = L_MISC + 1280, TAB_STRIDE = 1024;
constexpr u32 L_CTL = L_MISC + 9216;
constexpr u32 L_DONE = L_CTL + 16;
constexpr u32 L_CNT = L_CTL + 64;
constexpr u32 L_WC = L_CTL + 192;
constexpr u32 L_D2 = L_CTL + 224;
constexpr u32 L_ISS = L_CTL + 244;
constexpr u32 L_PUB = L_CTL + 240;
constexpr u32 L_SCR = L_CTL + 256;
constexpr u32 CHUNK = 25600;
constexpr u32 LDS_BYTES = 163840;
static_assert(L_MISC >= 6 * CHUNK && L_AW + 4 * AW_SLOT <= L_MISC && L_SCR + 256 <= LDS_BYTES, "lds map");

template <int V> struct IC { static constexpr int v = V; };
template <int I, int N, class F> DEV void sfor(F&& f) { if constexpr (I < N) { f(IC<I>{}); sfor<I + 1, N>(f); } }
template <int N> DEV void waitv() { asm volatile("s_waitcnt vmcnt(%0)" ::"i"(N) : "memory"); }
DEV void waitl0() { asm volatile("s_waitcnt lgkmcnt(0)" ::: "memory"); }
DEV void bar() { asm volatile("s_barrier" ::: "memory"); }
DEV void fence_c() { asm volatile("" ::: "memory"); }
DEV void glds(const char* sbase, u32 voff, u32 lds) {
  asm volatile("s_mov_b32 m0, %2\n\ts_nop 0\n\tglobal_load_lds_dwordx4 %0, %1" ::"v"(voff), "s"(sbase), "s"(lds));
}
DEV void touch(const char* sbase, u32 voff, u32 lds) {
  asm volatile("s_mov_b32 m0, %2\n\ts_nop 0\n\tglobal_load_lds_dword %0, %1" ::"v"(voff), "s"(sbase), "s"(lds));
}
DEV void glds4(const char* sbase, u32 v0, u32 v1, u32 v2, u32 v3, u32 lds) {
  asm volatile("s_mov_b32 m0, %5\n\ts_nop 0\n\tglobal_load_lds_dwordx4 %0, %4\n\tglobal_load_lds_dwordx4 %1, %4 offset:1024\n\t"
               "global_load_lds_dwordx4 %2, %4 offset:2048\n\tglobal_load_lds_dwordx4 %3, %4 offset:3072"
               ::"v"(v0), "v"(v1), "v"(v2), "v"(v3), "s"(sbase), "s"(lds));
}
DEV void glds5p(const char* sbase, u32 v, u32 v4, u32 lds) {
  asm volatile("s_mov_b32 m0, %3\n\ts_nop 0\n\tglobal_load_lds_dwordx4 %0, %2 sc1\n\tglobal_load_lds_dwordx4 %0, %2 offset:1024 sc1\n\t"
               "global_load_lds_dwordx4 %0, %2 offset:2048 sc1\n\tglobal_load_lds_dwordx4 %0, %2 offset:3072 sc1\n\t"
               "s_add_u32 m0, %3, 4096\n\ts_nop 0\n\tglobal_load_lds_dwordx4 %1, %2 sc1" ::"v"(v), "v"(v4), "s"(sbase), "s"(lds) : "scc");
}
constexpr int wpos(int p) { return (p / 5) * (int)SEG + (p % 5) * 1024; }
template <int P> DEV void gldsw(const char* sbase, const u32 (&v)[15], u32 lds) {
  if constexpr (P < 12)
    asm volatile("s_add_u32 m0, %5, %6\n\ts_nop 0\n\tglobal_load_lds_dwordx4 %0, %4\n\tglobal_load_lds_dwordx4 %1, %4 offset:%7\n\t"
                 "global_load_lds_dwordx4 %2, %4 offset:%8\n\tglobal_load_lds_dwordx4 %3, %4 offset:%9"
                 ::"v"(v[P]), "v"(v[P + 1]), "v"(v[P + 2]), "v"(v[P + 3]), "s"(sbase), "s"(lds), "i"(wpos(P)), "i"(wpos(P + 1) - wpos(P)),
                   "i"(wpos(P + 2) - wpos(P)), "i"(wpos(P + 3) - wpos(P)) : "scc");
  else
    asm volatile("s_add_u32 m0, %4, %5\n\ts_nop 0\n\tglobal_load_lds_dwordx4 %0, %3\n\tglobal_load_lds_dwordx4 %1, %3 offset:%6\n\t"
                 "global_load_lds_dwordx4 %2, %3 offset:%7"
                 ::"v"(v[P]), "v"(v[P + 1]), "v"(v[P + 2]), "s"(sbase), "s"(lds), "i"(wpos(P)), "i"(wpos(P + 1) - wpos(P)),
                   "i"(wpos(P + 2) - wpos(P)) : "scc");
}
DEV u32x4 ld128(u32 a) { return *(LAS u32x4*)(size_t)a; }
DEV u32x2 ld64(u32 a) { return *(LAS u32x2*)(size_t)a; }
DEV void st64(u32 a, u32x2 v) { *(LAS u32x2*)(size_t)a = v; }
DEV u32x2 ldtr(u32 a) { return __builtin_bit_cast(u32x2, __builtin_amdgcn_ds_read_tr16_b64_v4i16((LAS s16x4*)(size_t)a)); }
DEV u32x4 cat(u32x2 a, u32x2 b) { return __builtin_shufflevector(a, b, 0, 1, 2, 3); }
DEV u32x4 ldB(u32 a0, u32 a1) { return cat(ldtr(a0), ldtr(a1)); }
DEV f32x4 mma(u32x4 a, u32x4 b, f32x4 c) {
  return __builtin_amdgcn_mfma_f32_16x16x32_bf16(__builtin_bit_cast(bf16x8, a), __builtin_bit_cast(bf16x8, b), c, 0, 0, 0);
}
DEV f32x4 mma16(u32x2 a, u32x2 b, f32x4 c) {
  return __builtin_amdgcn_mfma_f32_16x16x16bf16_1k(__builtin_bit_cast(s16x4, a), __builtin_bit_cast(s16x4, b), c, 0, 0, 0);
}
DEV void mmai(f32x4& c, const u32x4& a, const u32x4& b) { asm volatile("v_mfma_f32_16x16x32_bf16 %0, %1, %2, %0" : "+v"(c) : "v"(a), "v"(b)); }
DEV void mma_done() { asm volatile("s_nop 7\n\ts_nop 3"); SB(); }
DEV u32 pk2(float a, float b) { f32x2 v = {a, b}; return __builtin_bit_cast(u32, __builtin_convertvector(v, bf16x2)); }
DEV u32x2 pk4(f32x4 v) { return u32x2{pk2(v[0], v[1]), pk2(v[2], v[3])}; }
DEV f32x4 splat(float v) { return f32x4{v, v, v, v}; }
DEV float fmx(float a, float b) { return __builtin_elementwise_maximum(a, b); }
DEV float phi1(float u) {
  float e = __builtin_amdgcn_fmed3f(__builtin_amdgcn_exp2f(__builtin_fmaf(u, 1.44269504089f, -1.44269504089e-6f)), 0.f, 1.f);
  return fmx(u, 1e-6f) + e;
}
DEV float phi1b(float u, float one) {
  float e = __builtin_amdgcn_fmed3f(__builtin_amdgcn_exp2f(__builtin_fmaf(u, 1.44269504089f, -1.44269504089e-6f)), 0.f, 1.f);
  return __builtin_fmaf(e, one, fmx(u, 1e-6f));
}
DEV f32x4 phi4(f32x4 v) {
  float one = 1.f; asm("" : "+s"(one));
  return f32x4{phi1(v[0]), phi1b(v[1], one), phi1(v[2]), phi1b(v[3], one)};
}
DEV float asf(u32 u) { return __builtin_bit_cast(float, u); }
DEV u32 asu(float f) { return __builtin_bit_cast(u32, f); }
DEV void xmax4(float& a, float& b, float& c, float& d, float scale) {
  auto r = __builtin_amdgcn_permlane16_swap(asu(a), asu(b), false, false);
  auto t = __builtin_amdgcn_permlane16_swap(asu(c), asu(d), false, false);
  auto w = __builtin_amdgcn_permlane32_swap(asu(fmx(asf(r[0]), asf(r[1]))), asu(fmx(asf(t[0]), asf(t[1]))), false, false);
  const u32 z = asu(fmx(asf(w[0]), asf(w[1])) * scale);
  auto x = __builtin_amdgcn_permlane32_swap(z, z, false, false);
  auto y0 = __builtin_amdgcn_permlane16_swap(x[0], x[0], false, false);
  auto y1 = __builtin_amdgcn_permlane16_swap(x[1], x[1], false, false);
  a = asf(y0[0]); b = asf(y0[1]); c = asf(y1[0]); d = asf(y1[1]);
}
DEV u32 odd_rows(u32 x) { auto r = __builtin_amdgcn_permlane16_swap(x, x, false, false); return r[1]; }

template <class F> constexpr u64 lane_mask(F f) { u64 m = 0; for (int l = 0; l < 64; ++l) if (f(l >> 4, l & 15)) m |= 1ull << l; return m; }
#define LM(expr) ({ constexpr u64 m_ = lane_mask([](int g, int i) { return (bool)(expr); }); (bool)__builtin_amdgcn_inverse_ballot_w64(m_); })
typedef __amdgpu_buffer_rsrc_t rsrc_t;
DEV u32x4 gld128(rsrc_t r, u32 off) { return __builtin_amdgcn_raw_buffer_load_b128(r, off, 0, 16); }
DEV u32 gld32(rsrc_t r, u32 off) { return __builtin_amdgcn_raw_buffer_load_b32(r, off, 0, 16); }
DEV void gst128(rsrc_t r, u32 off, u32x4 v) { __builtin_amdgcn_raw_buffer_store_b128(v, r, off, 0, 16); }
DEV void gst64(rsrc_t r, u32 off, u32x2 v) { __builtin_amdgcn_raw_buffer_store_b64(v, r, off, 0, 16); }
DEV void gst32(rsrc_t r, u32 off, u32 v) { __builtin_amdgcn_raw_buffer_store_b32(v, r, off, 0, 16); }
DEV void publish(char* ws, u32 off, u64 tag, int lane) {
  waitv<0>();
  if (lane == 0) __hip_atomic_store((GAS u64*)(ws + off), tag, RLX_AGENT);
}
DEV void await(char* ws, u32 off, u64 tag) {
#pragma nounroll
  for (u32 spins = 0; spins < SPIN_CAP; ++spins) {
    u64 v = __hip_atomic_load((GAS u64*)(ws + off), RLX_AGENT);
    if (__all(v == tag)) break;
    __builtin_amdgcn_s_sleep(POLL_SLEEP);
  }
  fence_c();
}
DEV u32 ldw(u32 a) { return __hip_atomic_load((LAS u32*)(size_t)a, __ATOMIC_RELAXED, __HIP_MEMORY_SCOPE_WORKGROUP); }
DEV void stw(u32 a, u32 v) { __hip_atomic_store((LAS u32*)(size_t)a, v, __ATOMIC_RELAXED, __HIP_MEMORY_SCOPE_WORKGROUP); }
DEV void lds_arrive(u32 a, int lane, u32 n = 1) {
  if (lane == 0) __hip_atomic_fetch_add((LAS u32*)(size_t)a, n, __ATOMIC_RELAXED, __HIP_MEMORY_SCOPE_WORKGROUP);
}
template <int SLEEP> DEV void lds_spin(u32 a, int want) {
#pragma nounroll
  for (u32 spins = 0; spins < (SPIN_CAP << 4); ++spins) {
    if (__all((int)ldw(a) >= want)) break;
    if (SLEEP) __builtin_amdgcn_s_sleep(SLEEP);
  }
  fence_c();
}
template <int SLEEP> DEV void lds_await(bool ok, u32 a, int want) {
  if (__builtin_expect(!ok, 0)) lds_spin<SLEEP>(a, want);
  fence_c();
}
template <int NM> DEV void ilv() {
#pragma unroll
  for (int n = 0; n < NM; ++n) { __builtin_amdgcn_sched_group_barrier(0x8, 1, 0); __builtin_amdgcn_sched_group_barrier(0x80, 1, 0); }
}

typedef u32 u32x16 __attribute__((ext_vector_type(16)));
DEV u32 mul24(u32 a, u32 b) { return __umul24(a, b); }
DEV void w_offsets(int lane, u32 (&ow)[5]) {
#pragma unroll
  for (int i = 0; i < 5; ++i) {
    const u32 c = i * 64 + lane, pos = mul24(c, 6554u) >> 16;
    ow[i] = mul24(pos ^ ((pos >> 1) & 4), 3840u) + c * 16 - mul24(pos, 160u);
  }
}

__global__ __launch_bounds__(512, 2) void fused_kernel(const char*, const char*, const char*, const char*, const char*, const char*, char*, char*) {
  extern __shared__ __attribute__((aligned(16))) char smem[];
  u32x16 ka;
  asm volatile("s_load_dwordx16 %0, %1, 0x0" : "=s"(ka) : "s"(__builtin_amdgcn_kernarg_segment_ptr()));
  STAMP_DECL
  const u32 lds0 = (u32)(size_t)(LAS char*)smem;
  const int b = blockIdx.x, xcd = b & 7, jj = b >> 3;
  const int rt = (xcd & 1) * 8 + (jj & 7), ct = (xcd >> 1) * 4 + (jj >> 3);
  const int r0t = (((jj & 7) * 5 + (jj >> 3) * 10 + (xcd >> 1) * 3 + (xcd & 1) * 2) >> 2), r0 = r0t >= 10 ? r0t - 10 : r0t;
  const bool isB = ct >= 8;
  const int hg = ct & 7;
  const int wave = __builtin_amdgcn_readfirstlane(threadIdx.x >> 6);
  int lane = threadIdx.x & 63, g = lane >> 4, i16 = lane & 15, q4 = (lane >> 2) & 3, p4 = lane & 3;
  auto relane = [&]() {
    asm volatile("v_mbcnt_lo_u32_b32 %0, -1, 0\n\tv_mbcnt_hi_u32_b32 %0, -1, %0" : "=v"(lane));
    g = lane >> 4; i16 = lane & 15; q4 = (lane >> 2) & 3; p4 = lane & 3;
  };
  {
    u32 ow[5];
    u32 o0 = mul24(lane >> 3, 2560u) + (((lane & 7) ^ (lane >> 4)) << 4), o1 = (o0 ^ 64) + 19456, o2 = o0 + 2 * 19456, o3 = o1 + 2 * 19456;
    w_offsets(lane, ow);
    ow[1] -= 1024; ow[2] -= 2048; ow[3] -= 3072;
    PIN(o0); PIN(o1); PIN(o2); PIN(o3); PIN(ow[0]); PIN(ow[1]); PIN(ow[2]); PIN(ow[3]); PIN(ow[4]);
    if (wave == 4 && lane < 60) stw(lds0 + L_DONE + lane * 4, 0);
    const u32 xoff = (128 * rt + 64 * (wave & 1) + (wave >> 1)) * 1280 + r0 * 128, woff = r0 * (64 * 3840) + hg * 160 + (wave + !isB) * 1280;
    asm volatile("s_waitcnt lgkmcnt(0)" : "+s"(ka)::"memory");
    if (wave < 4) {
      const char* xp = (const char*)(((u64)ka[1] << 32) | ka[0]);
      glds4(xp + xoff, o0, o1, o2, o3, lds0 + (isB ? L_BX : L_AX) + wave * 4096);
      if (wave < (isB ? 3 : 2)) {
        const char* wp = (const char*)(((u64)(isB ? ka[7] : ka[3]) << 32) | (isB ? ka[6] : ka[2])) + woff;
        const u32 d = lds0 + (isB ? L_BW : L_AW) + wave * SEG;
        glds4(wp, ow[0], ow[1], ow[2], ow[3], d); glds(wp, ow[4], d + 4096);
      } else if (!isB) {
        if (wave == 2 || lane < 16) glds((const char*)(((u64)ka[11] << 32) | ka[10]) + (wave - 2) * 1024, lane * 16, lds0 + L_GATEW + (wave - 2) * 1024);
      } else {
        const char* hp_ = xp + (rt ? 128 * rt - 16 : 0) * 1280 + r0 * 128;
        glds(hp_, o0, lds0 + L_BX + 16384); glds(hp_ + 1280, o0 ^ 64, lds0 + L_BX + 17408);
      }
    }
    bar();
  }
  auto argp = [&](int n) { return (char*)(((u64)ka[2 * n + 1] << 32) | ka[2 * n]); };
  const char* const X = argp(0); const char* const WA = argp(1); const char* const WoA = argp(2); const char* const WB = argp(3);
  const char* const WoB = argp(4); const char* const GW = argp(5); char* const out = argp(6); char* const ws = argp(7);
  const rsrc_t wsr = __builtin_amdgcn_make_buffer_rsrc(ws, 0, WS_END, 0x00020000);
  const u32 aA0 = i16 * 128 + ((g ^ ((i16 >> 1) & 7)) << 4);
  const u32 rB0 = (8 * g + (q4 ^ (4 * (g & 1)))) * 160, rB1 = (8 * g + ((4 + q4) ^ (4 * (g & 1)))) * 160;
  const int rg = xcd * 4 + (jj >> 3), ctile = jj & 7, shr = (jj >> 3) * 2;
  auto wreg = [&](int gr) -> u32 { return lds0 + (gr == 2 ? 1 : ((gr == 1) == isB ? 2 : 0)) * (2 * CHUNK); };
  auto wo_issue = [&](int s0, int s1, int st) {
#pragma nounroll
    for (int s = s0; s < s1; s += st) {
      const int gr = (s * 26) >> 8;
      glds5p(ws + WS_WOP + (ctile * 40 + s) * 5120, lane * 16, lane * 16 + 4096, wreg(gr) + (s - gr * 10) * 5120);
    }
  };
  auto wo_post = [&](int s0, int s1, int st) {
    waitv<0>();
    const int s = s0 + lane * st;
    if (s < s1) __hip_atomic_fetch_add((LAS u32*)(size_t)(lds0 + L_WC + ((s * 52) >> 8) * 4), 1u, __ATOMIC_RELAXED, __HIP_MEMORY_SCOPE_WORKGROUP);
  };
  u64 tag;

  struct Att { u32 bk, bv, by, ya, mk, mv, my, ob, fo, b0; };
  auto att_prep = [&](int par, int hp, int half) {
    Att t;
    const u32 own = lds0 + L_HO + (hp * 2 + par) * HO_STRIDE + (hp ? 7 : 12) * 512, pi = 2 * (hg & 1) + hp;
    t.ya = lds0 + L_YX + par * 2048 + hp * 64;
    t.bk = half ? own : lds0 + L_HO + hp * 2 * HO_STRIDE + 17 * 512 + par * 64;
    t.bv = half ? own : lds0 + L_HO + (hp * 2 + 1) * HO_STRIDE + 14 * 512 + par * 256;
    t.by = half ? t.ya + 512 : t.bv + 7 * 512 + hp * 64;
    t.ya += half * 1024;
    t.mk = half ? 0x3F : 0x37; t.mv = half ? 0x3F : 0x1F; t.my = half ? 0x37 : 0x17;
    t.ob = WS_O + (rt * 8 + par * 4 + half * 2) * 40960 + (hg >> 1) * 5120 + pi * 256;
    t.fo = WS_BFLAG + ((((rt * 2 + par) * 2 + half) * 8 + hg) * 2 + hp) * 8 + (pi << 24);
    t.b0 = rt == 0 && !half ? 0xF149F2CAu : 0u;
    asm volatile("" : "+s"(t.bk), "+s"(t.bv), "+s"(t.by), "+s"(t.ya), "+s"(t.mk), "+s"(t.mv), "+s"(t.my), "+s"(t.ob), "+s"(t.fo), "+s"(t.b0));
    return t;
  };
  auto attention = [&](u32x2 (&P)[2][8], const Att& t) {
    u32x2 Q[2][8], K4[2];
    {
      const u32 ya = t.ya + (lane & 0x37) * 8, ak = t.bk + (lane & t.mk) * 8, av = t.bv + (lane & t.mv) * 8;
      Q[0][2] = ld64(ak + 1024); Q[0][3] = ld64(ak + 1536); Q[0][4] = ld64(t.bk + ((lane | 16) & t.mk) * 8 + 2048);
      P[0][7] = ld64(ya); P[1][7] = ld64(ya + 512);
      Q[0][5] = ld64(av + 2560); Q[0][6] = ld64(av + 3072); Q[0][7] = ld64(t.by + (lane & t.my) * 8);
    }
    const u32x2 tg = ld64(lds0 + L_CTL);
    tag = __builtin_bit_cast(u64, tg);
#pragma unroll
    for (int r = 0; r < 2; ++r) K4[r] = u32x2{odd_rows(P[r][4][0]), odd_rows(P[r][4][1])};
#pragma unroll
    for (int f = 2; f < 8; ++f) Q[1][f] = f == 4 ? K4[0] : P[0][f];
    const bool pv[4] = {LM(4 * g >= i + 9), LM(4 * g + 1 >= i + 9), LM(4 * g + 2 >= i + 9), LM(4 * g + 3 >= i + 9)};
    const f32x4 bc = {LM(4 * g <= i && 4 * g >= i - 7) ? 0.f : -1e30f, LM(4 * g + 1 <= i && 4 * g + 1 >= i - 7) ? 0.f : -1e30f,
                      LM(4 * g + 2 <= i && 4 * g + 2 >= i - 7) ? 0.f : -1e30f, LM(4 * g + 3 <= i && 4 * g + 3 >= i - 7) ? 0.f : -1e30f};
    const u32x2 mp = {pv[0] ? 0xFFFFFFFFu : (pv[1] ? 0xFFFF0000u : 0u), pv[2] ? 0xFFFFFFFFu : (pv[3] ? 0xFFFF0000u : 0u)};
    const bool godd = LM(g & 1), glow = LM(g < 2), gq[2] = {LM(g == 0), LM(g == 2)};
    const float cs = 0.22360679775f * 1.44269504089f, b0 = asf(t.b0);
    const u32x2 zero2 = {0u, 0u};
    const f32x4 z4 = splat(0.f);
    const u32x2 ones2 = {0x3F803F80u, 0x3F803F80u};
    f32x4 m[2][2];
    float mx[2][2];
#pragma unroll
    for (int r = 1; r >= 0; --r) {
#pragma unroll
      for (int h = 0; h < 2; ++h) {
        const u32x2 q4m = gq[h] ? P[r][4] : zero2;
        const f32x4 sc = mma16(K4[r], q4m, mma16(P[r][2 + h], P[r][h], bc));
        const f32x4 sp = mma16(Q[r][4], q4m, mma16(Q[r][2 + h], P[r][h], r ? z4 : splat(b0)));
#pragma unroll
        for (int j = 0; j < 4; ++j) m[r][h][j] = pv[j] ? sp[j] : sc[j];
        mx[r][h] = fmx(fmx(m[r][h][0], m[r][h][1]), fmx(m[r][h][2], m[r][h][3]));
      }
    }
    xmax4(mx[1][0], mx[1][1], mx[0][0], mx[0][1], -cs);
    u32x2 y[2];
#pragma unroll
    for (int r = 1; r >= 0; --r) {
      float rs[2];
      f32x4 o[2], yy[2];
#pragma unroll
      for (int h = 0; h < 2; ++h) {
        f32x4 e;
#pragma unroll
        for (int j = 0; j < 4; ++j) e[j] = __builtin_amdgcn_exp2f(__builtin_fmaf(m[r][h][j], cs, mx[r][h]));
        const u32x2 pp = pk4(e);
        const u32x2 p0 = pp & mp, p1 = pp & ~mp;
        rs[h] = __builtin_amdgcn_rcpf(mma16(ones2, pp, z4)[0]);
        o[h] = mma16(Q[r][5 + h], p0, mma16(P[r][5 + h], p1, z4)) * rs[h];
        yy[h] = mma16(Q[r][7], p0, mma16(P[r][7], p1, z4));
      }
      __builtin_amdgcn_raw_buffer_store_b128(cat(pk4(o[0]), pk4(o[1])), wsr, lane * 16, t.ob + (t.fo >> 24) * 768 + r * 40960, 16);
      y[r] = pk4((godd ? yy[1] : yy[0]) * (godd ? rs[1] : rs[0]));
    }
    __builtin_amdgcn_raw_buffer_store_b64(glow ? y[0] : y[1], wsr, (glow ? 0 : 40960) + (lane & 15) * 16 + ((lane >> 1) & 8), t.ob + 4096, 16);
    STAMP(3);
    fence_c(); lds_arrive(lds0 + L_PUB, lane);
    publish(ws, t.fo & 0xFFFFFF, tag, lane);
    STAMP(4);
    lds_spin<1>(lds0 + L_PUB, 8);
  };

  if (wave >= 4) {
    const int lw = wave - 4;
    __builtin_amdgcn_s_setprio(3);
    const int xpar = lw & 1;
    const u32 xo = isB && rt ? 16 * 1280 : 0;
    const char* const X0 = X + rt * (128 * 1280) - xo + xpar * 1280;
    u32 vx[9], vw[15];
    int xst = (lw >> 1) + 1;
    auto rot = [&](int st) { const int k = st + r0; return k >= 10 ? k - 10 : k; };
    auto issue_x = [&](u32 slot, bool halo) {
      const char* p = X0 + rot(xst) * 128;
      glds4(p, vx[0], vx[1], vx[2], vx[3], slot + xpar * 8192); glds4(p, vx[4], vx[5], vx[6], vx[7], slot + xpar * 8192 + 4096);
      if (halo) glds(p, vx[8], slot + 16384 + xpar * 1024);
    };
    auto setup = [&](bool forB) {
      relane();
      const u32 ox0 = (lane >> 3) * 2560 + (((lane & 7) ^ (lane >> 4)) << 4);
      vx[0] = ox0 + xo; vx[1] = (ox0 ^ 64) + xo + 20480 - 1024; vx[2] = vx[0] + 2 * (20480 - 1024); vx[3] = vx[1] + 2 * (20480 - 1024);
#pragma unroll
      for (int p = 0; p < 4; ++p) vx[4 + p] = vx[p] + 64 * 1280;
      vx[8] = ox0 ^ (xpar << 6);
      u32 ow[5];
      w_offsets(lane, ow);
#pragma unroll
      for (int p = 0; p < 15; ++p) { if (forB || p < 10) { vw[p] = ow[p % 5] + (p / 5) * 1280 - (wpos(p) - wpos(p & ~3)); PIN(vw[p]); } }
    };
    const char* Wt = (isB ? WB : WA) + hg * 160;
    auto wsrc = [&](int u) { return Wt + (rot(u >> 1) * 64 + (u & 1) * 32) * 3840; };
    const u32 cw = lds0 + L_CNT + (lw + 1) * 4;
    const bool qjob = lw == 2, pjob = qjob ? b >= 96 : (lw == 3 && b < 200);
    int jn = qjob ? b - 96 : b, jh, jl, sg, pst;
    const u32 pl = lds0 + (qjob ? 8192 : 0);
    auto pack_load = [&]() {
      asm volatile("" : "+s"(jn));
      jh = qjob ? (jn * 205) >> 12 : (jn * 205) >> 13; jl = jn - jh * (qjob ? 20 : 40);
      const int k20 = jl >= 20 ? jl - 20 : jl;
      sg = (k20 * 52) >> 8; pst = k20 - sg * 5;
      const u32 rs = qjob ? 3840 : 1280, v = g * rs + i16 * 16;
      const char* src = qjob ? WA + jl * (32 * 3840) + ((jh * 160) & ~127) : (jl < 20 ? WoB : WoA) + (sg * 160 + (pst < 4 ? 40 * pst : 16)) * 1280 + jh * 256;
      const u32 r1 = (qjob || pst < 4 ? 4 : 40) * rs, r4 = (qjob ? 16 : 20) * rs;
#pragma unroll
      for (int p = 0; p < 8; ++p) glds(src + (p & 3) * r1 + (p >> 2) * r4, v, pl + p * 1024);
    };
    auto pack_store = [&]() {
      const u32 a = pl + ((qjob ? 8 : 4) * g + q4) * 256 + (qjob ? (jh * 160) & 127 : 0), fs = qjob ? 40 : 32, a2 = qjob ? 1024 : 4096;
      u32x4 fr[8];
#pragma unroll
      for (int f = 0; f < 8; ++f) { const u32 x = a + (qjob && f >= 4 ? 32 + p4 * 40 : f * fs + p4 * 8); fr[f] = ldB(x, x + a2); }
      const int step = ((jl < 20 ? 0 : 2) + (sg & 1)) * 10 + (sg >> 1) * 5 + pst;
#pragma unroll
      for (int f = 0; f < 8; ++f) {
        const int F = jh * 8 + f, hi = (F * 52) >> 8, lo = F - hi * 5;
        const u32 d = qjob ? (f < 5 ? WS_WQP + (jn * 5 + f) * 1024 : OOB) : WS_WOP + ((hi * 40 + step) * 5 + lo) * 1024;
        __builtin_amdgcn_raw_buffer_store_b128(fr[f], wsr, d + lane * 16, 0, 16);
      }
    };
    auto pack_flag = [&]() {
      const u64 t = *(LAS u64*)(size_t)(lds0 + L_CTL);
      if (lane == 0) __hip_atomic_store((GAS u64*)(ws + WS_PFLAG + ((qjob ? 200 : 0) + jn) * 8), t, RLX_AGENT);
    };
    u32x2 ep;
    auto first = [&]() {
      if (pjob) pack_load();
      if (lw == 1) asm volatile("global_load_dwordx2 %0, %1, %2 sc1" : "=v"(ep) : "v"(0), "s"(ws + WS_EPOCH));
      lds_spin<TOKS>(lds0 + L_ISS, lw);
    };
    auto first_done = [&](auto NP) {
      if (pjob) { waitv<NP.v>(); pack_store(); waitv<8>(); }
      else {
        asm volatile("s_waitcnt vmcnt(0)" : "+v"(ep)::"memory");
        if (lw == 1) *(LAS u64*)(size_t)(lds0 + L_CTL) = __builtin_bit_cast(u64, ep) + TAG_STEP;
      }
    };
    auto pack_wait = [&]() {
      const u64 t = *(LAS u64*)(size_t)(lds0 + L_CTL);
      const u32 f0 = WS_PFLAG + (lane < 40 ? ((ctile * 5) >> 3) * 40 + lane : 200 + hg * 20 + (lane < 60 ? lane - 40 : 0)) * 8;
      const u32 f1 = WS_PFLAG + (((ctile * 5 + 4) >> 3) * 40 + (lane < 40 ? lane : 0)) * 8;
#pragma nounroll
      for (u32 spins = 0; spins < SPIN_CAP; ++spins) {
        const u64 v0 = __hip_atomic_load((GAS u64*)(ws + f0), RLX_AGENT), v1 = __hip_atomic_load((GAS u64*)(ws + f1), RLX_AGENT);
        if (__all(v0 == t && v1 == t)) break;
        __builtin_amdgcn_s_sleep(POLL_SLEEP);
      }
      fence_c();
    };
    if (isB) {
      setup(true); first();
      const u32 wd = lds0 + L_BW + ((lw + 1) & 3) * BW_SLOT, da = lds0 + L_DONE + (lane & 3) * 4;
      u32 xs = lds0 + L_BX + ((lw >> 1) + 1) * BX_SLOT;
#pragma nounroll
      for (int u = lw + 1; u < 20; u += 4) {
        if (u >= 4) lds_spin<1>(da, u - 3);
        { const char* wp = wsrc(u); gldsw<0>(wp, vw, wd); gldsw<4>(wp, vw, wd); gldsw<8>(wp, vw, wd); gldsw<12>(wp, vw, wd); }
        if (u < 19) issue_x(xs, true);
        if (u < 4) stw(lds0 + L_ISS, u);
        xst += 2; xs = xs == lds0 + L_BX ? xs + 2 * BX_SLOT : xs - BX_SLOT;
        if (u < 5) STAMP(9);
        if (u < 5) first_done(IC<24>{}); else waitv<0>();
        stw(lds0 + L_CNT + u * 4, 4);
        if (u < 5) STAMP(10); else STAMP(11);
        if (u == lw + 5 && pjob) pack_flag();
      }
      if (lw == 3) { pack_wait(); stw(lds0 + L_CNT + 23 * 4, 4); }
      lds_spin<1>(lds0 + L_CNT + ((lane & 1) ? TAILG : 23) * 4, 4);
      wo_issue(3 - lw, 7, 4);
      const Att at = att_prep(lw & 1, lw >> 1, (lw >> 1) ^ 1);
      lds_spin<1>(da, 20);
      wo_issue(7 + lw, 20, 4);
      __builtin_amdgcn_s_setprio(0);
      bar();
      u32x2 P[2][8];
      const u32 hl = lds0 + L_HO + lw * HO_STRIDE + lane * 8;
#pragma unroll
      for (int f = 0; f < 7; ++f) { P[0][f] = ld64(hl + f * 512); P[1][f] = ld64(hl + (7 + f) * 512); }
      attention(P, at);
    } else {
      setup(false); first();
      const u32 wd = lds0 + L_AW + ((lw + 1) & 3) * AW_SLOT, da = lds0 + L_DONE + (lane & 7) * 4;
      u32x4 xr[20][2];
      f32x4 gl[2] = {};
      u32 xa0 = lds0 + L_AX + lw * 4096 + i16 * 128 + ((g ^ ((i16 >> 1) & 7)) << 4), xa1 = xa0 ^ 64;
      PIN(xa0); PIN(xa1);
      auto take = [&](int st) {
        lds_spin<1>(lds0 + L_CNT + (st ? 2 * st - (lane & 1) : 0) * 4, 4);
        const u32 o = (st & 3) * AX_SLOT;
        xr[2 * st][0] = ld128(xa0 + o); xr[2 * st][1] = ld128(xa0 + o + 2048);
        xr[2 * st + 1][0] = ld128(xa1 + o); xr[2 * st + 1][1] = ld128(xa1 + o + 2048);
        fence_c(); stw(lds0 + L_DONE + wave * 4, st == 9 ? 30 : (st > 6 ? 19 : 2 * st + 5));
        const u32 go = lds0 + L_GATEW + rot(st) * 128 + g * 16;
        const u32x4 g0 = ld128(go), g1 = ld128(go + 64);
        gl[0] = mma(g1, xr[2 * st + 1][0], mma(g0, xr[2 * st][0], gl[0])); gl[1] = mma(g1, xr[2 * st + 1][1], mma(g0, xr[2 * st][1], gl[1]));
      };
      auto wq = [&](int n) { glds5p(ws + WS_WQP + (hg * 20 + rot(n >> 1) * 2 + (n & 1)) * 5120, lane * 16, lane * 16 + 4096, lds0 + n * 5120); };
      stw(lds0 + L_DONE + wave * 4, 3);
#pragma unroll
      for (int j = 0; j < 5; ++j) {
        const int u = 4 * j + lw + 1;
        if (j >= 1) take(2 * j - 2);
        if (j < 4 || lw < 3) {
          if (u >= 4) lds_spin<1>(da, u - 3);
          const char* wp = wsrc(u) + 1280; gldsw<0>(wp, vw, wd); gldsw<4>(wp, vw, wd);
          asm volatile("s_add_u32 m0, %3, %4\n\ts_nop 0\n\tglobal_load_lds_dwordx4 %0, %2\n\tglobal_load_lds_dwordx4 %1, %2 offset:1024"
                       ::"v"(vw[8]), "v"(vw[9]), "s"(wp), "s"(wd), "i"(wpos(8)) : "scc");
          if (u < 19) issue_x(lds0 + L_AX + (xst & 3) * AX_SLOT, false);
          if (j == 0) stw(lds0 + L_ISS, u);
          xst += 2;
        }
        if (j >= 1) take(2 * j - 1);
        if (j == 4 && lw == 3) { lds_spin<1>(lds0 + L_CNT + A_PW * 4, 4); pack_wait(); }
        if (j == 0) STAMP(9);
        if (j == 0) first_done(IC<18>{}); else waitv<0>();
        stw(cw + j * 16, 4);
        if (j == 0) STAMP(10); if (j == 4) STAMP(11);
        if (j == 1 && pjob) pack_flag();
      }
      take(8); take(9);
      lds_spin<1>(lds0 + L_CNT + 20 * 4, 4);
      wq(lw); wq(lw + 4);
      lds_spin<1>(da, 20);
      STAMP(12);
#pragma nounroll
      for (int n = 8 + lw; n < 20; n += 4) wq(n);
      wo_issue(3 - lw, 8, 4);
      waitv<25>(); lds_arrive(lds0 + L_CNT + 21 * 4, lane);
      __builtin_amdgcn_s_setprio(0);
      STAMP(1);
      relane();
      float gate[2];
#pragma unroll
      for (int r = 0; r < 2; ++r) { gate[r] = __builtin_amdgcn_rcpf(1.f + __builtin_amdgcn_exp2f(gl[r][0] * -1.44269504089f)); PIN(gate[r]); }
      f32x4 qa[2][5];
#pragma unroll
      for (int f = 0; f < 5; ++f) { qa[0][f] = qa[1][f] = splat(1e-6f); PIN(qa[0][f]); PIN(qa[1][f]); }
      {
        u32 q0 = lds0 + lane * 16;
        constexpr int D = 6;
        u32x4 w[D + 1];
        sfor<-D, 100>([&](auto T) {
          constexpr int t = T.v, n = t + D;
          if constexpr (n == 0) lds_spin<1>(lds0 + L_CNT + 21 * 4, QGATE);
          if constexpr (n == 20) { waitv<A_G0W>(); stw(lds0 + L_CNT + (24 + lw) * 4, 4); }
          if constexpr (n == 40) lds_spin<1>(lds0 + L_CNT + (24 + (lane & 3)) * 4, 4);
          if constexpr (n == 60) { q0 += 61440; PIN(q0); }
          SB();
          if constexpr (t >= 0) mmai(qa[0][t % 5], w[t % (D + 1)], xr[t / 5][0]);
          SB();
          if constexpr (n < 100) w[n % (D + 1)] = ld128(q0 + (n >= 60 ? n - 60 : n) * 1024);
          SB();
          if constexpr (t >= 0) mmai(qa[1][t % 5], w[t % (D + 1)], xr[t / 5][1]);
          SB();
        });
        mma_done();
      }
      __builtin_amdgcn_s_setprio(1);
      STAMP(2);
      relane();
      u32x2 pq[2][5];
#pragma unroll
      for (int r = 0; r < 2; ++r)
#pragma unroll
        for (int f = 0; f < 5; ++f) { pq[r][f] = pk4(phi4(qa[r][f])); PIN(pq[r][f]); }
      u32 ta[4][4];
      {
        const u32 tb = lds0 + L_TAB, tz = tb + 105 * 8, t0 = tb + (64 + i16) * 8, t1 = tb + (80 + p4 + 4 * g) * 8, t2 = tb + (100 + p4) * 8;
#pragma unroll
        for (int h = 0; h < 4; ++h) {
          const bool gh = g == h, eh = q4 == (h ^ 1);
          ta[h][0] = gh ? t0 : tz; ta[h][1] = eh ? t1 : tz; ta[h][2] = eh && gh ? t2 : tz; ta[h][3] = gh ? tz - 8 : tz;
#pragma unroll
          for (int i = 0; i < 4; ++i) PIN(ta[h][i]);
        }
      }
      wo_post(3 - lw, 8, 4);
      __builtin_amdgcn_s_setprio(3);
      waitl0(); bar();
      STAMP(3);
      tag = *(LAS u64*)(size_t)(lds0 + L_CTL);
      f32x4 n5[2] = {};
      u32x2 ob[2][4];
      float sc5[2] = {0.f, 0.f};
#pragma unroll
      for (int h = 0; h < 4; ++h) {
        const u32 tb = lds0 + L_TAB + h * TAB_STRIDE;
        const u32x4 a0 = cat(ld64(tb + lane * 8), ld64(ta[h][0] + h * TAB_STRIDE));
        const u32x4 a5 = cat(ld64(ta[h][1] + h * TAB_STRIDE), ld64(ta[h][2] + h * TAB_STRIDE));
        const u32x4 az = cat(ld64(tb + (96 + g) * 8), ld64(ta[h][3] + h * TAB_STRIDE));
#pragma unroll
        for (int r = 0; r < 2; ++r) {
          const u32x4 bo = cat(pq[r][h], pq[r][4]);
          const f32x4 num = mma(a0, bo, splat(0.f)), den = mma(az, bo, splat(1e-6f));
          n5[r] = mma(a5, bo, n5[r]);
          const float sc = gate[r] * __builtin_amdgcn_rcpf(den[0]);
          ob[r][h] = pk4(num * sc);
          sc5[r] = (g ^ 1) == h ? sc : sc5[r];
        }
      }
#pragma unroll
      for (int r = 0; r < 2; ++r) {
        const u32 blk = rt * 8 + lw * 2 + r;
        const u32 base = WS_O + blk * 40960 + 20480 + (hg >> 1) * 5120;
        gst128(wsr, base + (hg & 1) * 2048 + lane * 16, cat(ob[r][0], ob[r][1]));
        gst128(wsr, base + (hg & 1) * 2048 + 1024 + lane * 16, cat(ob[r][2], ob[r][3]));
        gst64(wsr, base + 4096 + ((2 * (hg & 1) + (g >> 1)) * 16 + i16) * 16 + ((g ^ 1) & 1) * 8, pk4(n5[r] * sc5[r]));
        gst32(wsr, lane < 16 ? WS_GATE + (hg * 128 + blk) * 64 + lane * 4 : OOB, asu(gate[r]));
      }
      STAMP(5);
      fence_c(); lds_arrive(lds0 + L_PUB, lane);
      publish(ws, WS_AFLAG + ((rt * 4 + lw) * 8 + hg) * 8, tag, lane);
      STAMP(4);
    }
    {
      if (isB) { wo_post(3 - lw, 7, 4); wo_post(7 + lw, 20, 4); }
      wo_issue(20 + lw, 30, 4); wo_post(20 + lw, 30, 4);
      STAMP(7);
      lds_spin<2>(lds0 + L_D2 + (lane & 3) * 4, 1);
      wo_issue(30 + lw, 40, 4); wo_post(30 + lw, 40, 4);
      STAMP(8);
    }
    if (b == 0 && lw == 0) {
#pragma unroll
      for (int r = 0; r < 2; ++r) {
        const int n = r * 64 + lane;
        await(ws, WS_BFLAG + (((n >> 3) * 4 * 8 + (n & 7)) * 2) * 8, tag);
        await(ws, WS_AFLAG + (((n >> 3) * 4) * 8 + (n & 7)) * 8, tag);
      }
      if (lane == 0) __hip_atomic_store((GAS u64*)(ws + WS_EPOCH), tag, RLX_AGENT);
    }
    STAMP(13); STAMP_OUT;
    asm volatile(".Lpf_end:");
    return;
  }

  {
    if (wave == 3) {
      const char* pc; u32 len;
      asm volatile("s_getpc_b64 %0\n.Lpf_here:\n\ts_mov_b32 %1, .Lpf_end-.Lpf_here" : "=s"(pc), "=s"(len));
      const u32 o = (lane * 32 + jj) * 128;
      if (o < len) touch(pc, o, lds0 + L_SCR);
      waitv<1>();
    } else
    waitv<0>();
    lds_arrive(lds0 + L_CNT, lane);
  }
  auto get_tag = [&]() {
    const u32x2 t = ld64(lds0 + L_CTL);
    tag = ((u64)(u32)__builtin_amdgcn_readfirstlane(t[1]) << 32) | (u32)__builtin_amdgcn_readfirstlane(t[0]);
  };
  u32 ca = lds0 + L_CNT, dcount = 1;
  u32 dn = lds0 + L_DONE + wave * 4;
  PIN(dn);
  if (isB) {
    const int par = wave & 1, hp = wave >> 1;
    u32 bM0 = lds0 + L_BW + rB0 + hp * 80 + p4 * 8, bM1 = bM0 - rB0 + rB1;
    u32 bX0 = lds0 + L_BW + rB0 + hp * 80 + 32 + (p4 & 1) * SEG + (p4 >> 1) * 40, bX1 = bX0 - rB0 + rB1;
    u32 bY0 = lds0 + L_BW + rB0 + 2 * SEG + 32 + p4 * 40, bY1 = bY0 - rB0 + rB1;
    u32 alo = aA0 + par * 8192 + hp * 4096, ahi = aA0 + par * 8192 + (hp ^ 1) * 4096;
    PIN(bM0); PIN(bM1); PIN(bX0); PIN(bX1); PIN(bY0); PIN(bY1); PIN(alo); PIN(ahi); PIN(ca); PIN(dcount);
    const Att at = att_prep(par, hp, hp);
    f32x4 acc[4][7], accy[2], acch[3];
#pragma unroll
    for (int f = 0; f < 3; ++f) { acch[f] = splat(0.f); PIN(acch[f]); }
#pragma unroll
    for (int r = 0; r < 4; ++r)
#pragma unroll
      for (int f = 0; f < 7; ++f) { acc[r][f] = splat(0.f); PIN(acc[r][f]); }
    accy[0] = accy[1] = splat(0.f); PIN(accy[0]); PIN(accy[1]);
    struct Fr { u32x4 a[4]; u32x2 w[8][2]; };
    u32 xs = lds0 + L_BX, x0 = xs + alo, x1 = xs + ahi;
    const u32 hoff = 16384 - par * 8192 - hp * 4096;
    u32x4 ah[2];
    auto rd1 = [&](Fr& F, int sl, int n) {
      const u32 o = sl * BW_SLOT;
      if (n < 4) { F.a[n] = ld128((n < 2 ? x0 : x1) + (n & 1) * 2048); return; }
      const int f = (n - 4) >> 1, t = n & 1;
      if (f == 4) F.w[4][t] = ldtr((t ? bX1 : bX0) + o);
      else if (f == 5) F.w[7][t] = ldtr((t ? bY1 : bY0) + o);
      else { const int ff = f < 4 ? f : f - 2; F.w[f < 4 ? f : f - 1][t] = ldtr((t ? bM1 : bM0) + o + (ff >> 1) * SEG + (ff & 1) * 40); }
    };
    auto mm1 = [&](Fr& F, int n) {
      if (n >= 20 && n < 22) { mmai(accy[n - 20], F.a[n - 20], cat(F.w[7][0], F.w[7][1])); return; }
      const int m = n < 20 ? n : n - 2, f = m >> 2, r = m & 3;
      const u32x4 w = cat(F.w[f][0], F.w[f][1]);
      if (f < 5) mmai(acc[r][f], w, F.a[r]); else mmai(acc[r][f], F.a[r], w);
    };
    Fr F0, F1;
    lds_spin<0>(ca, 4);
    STAMP(1); CSTAMP(14);
#pragma unroll
    for (int n = 0; n < 20; ++n) rd1(F0, 0, n);
    ah[0] = ld128(x0 + hoff);
    fence_c(); stw(dn, dcount);
    bool ok = false;
    x0 = xs + (alo ^ 64); x1 = xs + (ahi ^ 64);
    auto unit = [&](Fr& Fc, Fr& Fn, int k, bool last) {
      u32 cv;
#pragma unroll
      for (int n = 0; n < 30; ++n) {
        SB(); mm1(Fc, n); SB();
        if (n == 3) {
          asm volatile("s_cmp_lg_u32 %10, 0\n\ts_cbranch_scc1 .Lhv%=\n\t"
                       "v_mfma_f32_16x16x32_bf16 %0, %4, %3, %0\n\tv_mfma_f32_16x16x32_bf16 %1, %5, %3, %1\n\tv_mfma_f32_16x16x32_bf16 %2, %6, %3, %2\n\t"
                       "s_branch .Lhe%=\n.Lhv%=:\n\t"
                       "v_mfma_f32_16x16x32_bf16 %0, %3, %7, %0\n\tv_mfma_f32_16x16x32_bf16 %1, %3, %8, %1\n\tv_mfma_f32_16x16x32_bf16 %2, %3, %9, %2\n.Lhe%=:"
                       : "+v"(acch[0]), "+v"(acch[1]), "+v"(acch[2])
                       : "v"(ah[k & 1]), "v"(cat(Fc.w[2][0], Fc.w[2][1])), "v"(cat(Fc.w[3][0], Fc.w[3][1])), "v"(cat(Fc.w[4][0], Fc.w[4][1])),
                         "v"(cat(Fc.w[5][0], Fc.w[5][1])), "v"(cat(Fc.w[6][0], Fc.w[6][1])), "v"(cat(Fc.w[7][0], Fc.w[7][1])), "s"(par) : "scc");
          SB();
          if (!last) lds_await<0>(ok, ca + (k + 1) * 4, 4);
          cv = ldw(ca + (k + 2) * 4);
        }
        if (n >= 4 && n < 24) rd1(Fn, (k + 1) & 3, n - 4);
        if (n == 10) ++dcount;
        if (n == 24) ah[(k + 1) & 1] = ld128(x0 + hoff);
        if (n == 25) { fence_c(); stw(dn, dcount); }
        if (n == 28) ok = __all(cv >= 4);
        if (n == 26) { if (!(k & 1)) xs = xs == lds0 + L_BX + 2 * BX_SLOT ? lds0 + L_BX : xs + BX_SLOT; x0 = (k & 1) ? x0 ^ 64 : xs + alo; PIN(x0); }
        if (n == 27) { x1 = (k & 1) ? x1 ^ 64 : xs + ahi; PIN(x1); }
      }
      SB();
    };
#pragma nounroll
    for (int it = 0; it < 5; ++it) {
      unit(F0, F1, 0, false); unit(F1, F0, 1, false); unit(F0, F1, 2, false); unit(F1, F0, 3, it == 4);
      ca += 16;
    }
    mma_done();
    STAMP(2); CSTAMP(15);
    relane();
    u32x2 P[2][8];
    {
      const u32 dd = lds0 + L_DONE + (lane & 3) * 4;
      const int dv = ldw(dd);
      u32x2 H[2][7];
#pragma unroll
      for (int f = 0; f < 7; ++f) { H[0][f] = pk4(acc[2][f]); H[1][f] = pk4(acc[3][f]); PIN(H[0][f]); PIN(H[1][f]); }
      lds_await<0>(__all(dv >= 18), dd, 18);
      const u32 hl = lds0 + L_HO + wave * HO_STRIDE + lane * 8, yl = lds0 + L_YX + (par * 4 + 2 * hp) * 512 + lane * 8;
#pragma unroll
      for (int f = 0; f < 7; ++f) { st64(hl + f * 512, H[0][f]); st64(hl + (7 + f) * 512, H[1][f]); }
#pragma unroll
      for (int f = 2; f < 7; ++f) { P[1][f] = pk4(acc[1][f]); st64(hl + (12 + f) * 512, P[1][f]); }
#pragma unroll
      for (int f = 0; f < 3; ++f) st64(hl + (19 + f) * 512, pk4(acch[f]));
      st64(yl, pk4(accy[0])); st64(yl + 512, pk4(accy[1]));
    }
    STAMP(5);
    waitl0(); bar();
    STAMP(6);
#pragma unroll
    for (int f = 0; f < 7; ++f) P[0][f] = pk4(acc[0][f]);
    P[1][0] = pk4(acc[1][0]); P[1][1] = pk4(acc[1][1]);
    attention(P, at);
  } else {
    const int h = wave;
    u32 bK0 = lds0 + L_AW + rB0 + h * 40 + p4 * 8, bK1 = bK0 - rB0 + rB1;
    u32 bZ0 = lds0 + L_AW + rB0 + h * 40 + 32 + (p4 == (h ^ 1) ? SEG : 0), bZ1 = bZ0 - rB0 + rB1;
    PIN(bK0); PIN(bK1); PIN(bZ0); PIN(bZ1); PIN(ca); PIN(dcount);
    f32x4 acc[8][3];
#pragma unroll
    for (int r = 0; r < 8; ++r)
#pragma unroll
      for (int f = 0; f < 3; ++f) { acc[r][f] = splat(f == 0 ? 1e-6f : (f == 1 || q4 == (h ^ 1) ? 0.f : 1e-6f)); PIN(acc[r][f]); }
    struct Fr { u32x4 a[8]; u32x2 w[3][2]; };
    u32 xs = 0, xa = lds0 + L_AX + aA0, x0 = xa;
    PIN(xa);
    auto rd1 = [&](Fr& F, int sl, int n) {
      const u32 o = sl * AW_SLOT;
      if (n >= 6) { F.a[n - 6] = ld128(x0 + (n - 6) * 2048); return; }
      const int f = n >> 1, t = n & 1;
      F.w[f][t] = f < 2 ? ldtr((t ? bK1 : bK0) + o + f * SEG) : ldtr((t ? bZ1 : bZ0) + o);
    };
    Fr F0, F1;
    lds_spin<0>(ca, 4);
    STAMP(1); CSTAMP(14);
#pragma unroll
    for (int n = 0; n < 14; ++n) rd1(F0, 0, n);
    fence_c(); stw(dn, dcount);
    bool ok = false;
    x0 ^= 64;
    auto unit = [&](Fr& Fc, Fr& Fn, int k, bool last) {
      u32 cv;
#pragma unroll
      for (int n = 0; n < 24; ++n) {
        SB(); mmai(acc[n / 3][n % 3], Fc.a[n / 3], cat(Fc.w[n % 3][0], Fc.w[n % 3][1])); SB();
        if (n == 2 && !last) lds_await<0>(ok, ca + (k + 1) * 4, 4);
        if (n == 2) cv = ldw(ca + (k + 2) * 4);
        if (n >= 3 && n < 17) rd1(Fn, (k + 1) & 3, n - 3);
        if (n == 10) ++dcount;
        if (n == 17) { fence_c(); stw(dn, dcount); }
        if (n == 19) ok = __all(cv >= 4);
        if (n == 20) { if (!(k & 1)) xs = (xs + AX_SLOT) & (4 * AX_SLOT - 1); x0 = (k & 1) ? x0 ^ 64 : xs + xa; PIN(x0); }
      }
      SB();
    };
#pragma nounroll
    for (int it = 0; it < 5; ++it) {
      unit(F0, F1, 0, false); unit(F1, F0, 1, false); unit(F0, F1, 2, false); unit(F1, F0, 3, it == 4);
      ca += 16;
    }
    mma_done();
    STAMP(2); CSTAMP(15);
    relane(); get_tag();
    __builtin_amdgcn_s_setprio(2);
    const bool eh = q4 == (h ^ 1), gh = g == h, zc = i16 == 4 * h;
    const u32x2 ones = {0x3F803F80u, 0x3F803F80u};
    f32x4 t00 = {}, t01 = {}, t10 = {}, t11 = {};
#pragma unroll
    for (int k = 0; k < 4; ++k) {
      const u32x4 pk = cat(pk4(phi4(acc[2 * k][0])), pk4(phi4(acc[2 * k + 1][0])));
      const u32x4 pz = cat(pk4(phi4(acc[2 * k][2])), pk4(phi4(acc[2 * k + 1][2])));
      const u32x4 vv = cat(pk4(acc[2 * k][1]), pk4(acc[2 * k + 1][1]));
      const u32x2 z0 = pk4(acc[2 * k][2]), z1 = pk4(acc[2 * k + 1][2]);
      const u32x4 zb = cat(eh ? z0 : ones, eh ? z1 : ones);
      t00 = mma(pk, vv, t00); t01 = mma(pk, zb, t01); t10 = mma(pz, vv, t10); t11 = mma(pz, zb, t11);
    }
    const u32 pb = WS_KVP + (hg * 4 + h) * 16 * KVP_STRIDE, tb = lds0 + L_TAB + h * TAB_STRIDE;
    st64(tb + lane * 8, pk4(t00));
    st64(tb + (gh ? 64 + i16 : 106) * 8, pk4(t10));
    st64(tb + (eh ? 80 + p4 + 4 * g : (zc ? 96 + g : 106)) * 8, pk4(t01));
    st64(tb + (gh && eh ? 100 + p4 : (gh && zc ? 104 : 105)) * 8, pk4(t11));
    const u32 po = lane < 53 ? pb + lane * 16 : OOB;
    gst128(wsr, po + rt * KVP_STRIDE, ld128(tb + lane * 16));
    if (QGATE == 8) lds_arrive(lds0 + L_CNT + 21 * 4, lane);
    STAMP(3);
    publish(ws, WS_KVFLAG + ((hg * 4 + h) * 16 + rt) * 8, tag, lane);
    STAMP(4);
    __builtin_amdgcn_s_setprio(0);
    await(ws, WS_KVFLAG + ((hg * 4 + h) * 16 + i16) * 8, tag);
    __builtin_amdgcn_s_setprio(2);
    STAMP(5);
    {
      u32x4 vm[16];
#pragma unroll
      for (int r = 0; r < 16; ++r) vm[r] = __builtin_amdgcn_raw_buffer_load_b128(wsr, po, r * KVP_STRIDE, 16);
      float sm[8] = {};
      u32 sel0 = 0x00003F80u, sel1 = 0x3F800000u;
      PIN(sel0); PIN(sel1);
#pragma unroll
      for (int r = 0; r < 16; ++r)
#pragma unroll
        for (int e = 0; e < 8; ++e)
          sm[e] = __builtin_amdgcn_fdot2_f32_bf16(__builtin_bit_cast(bf16x2, (u32)(vm[r][e >> 1] + 0u)), __builtin_bit_cast(bf16x2, (e & 1) ? sel1 : sel0), sm[e], false);
      const bool tail = lane == 52;
      *(LAS u32x4*)(size_t)(tb + lane * 16) = u32x4{pk2(sm[0], sm[1]), pk2(sm[2], sm[3]), tail ? 0u : pk2(sm[4], sm[5]), tail ? 0u : pk2(sm[6], sm[7])};
    }
    waitl0(); bar();
    __builtin_amdgcn_s_setprio(0);
    STAMP(6);
    wo_issue(8 + h, 20, 4); wo_post(8 + h, 20, 4);
    lds_spin<2>(lds0 + L_PUB, 4);
  }

  {
    const int rp = wave & 1, kh = wave >> 1;
    const int rt2 = rg >> 1, par2 = rg & 1;
    const u32 blk0 = rg * 4 + rp * 2;
    f32x4 acc[2][5];
#pragma unroll
    for (int r = 0; r < 2; ++r)
#pragma unroll
      for (int c = 0; c < 5; ++c) { acc[r][c] = splat(0.f); PIN(acc[r][c]); }
    u32x4 ofb[10][2], ofa[10][2];
    float gt[2];
    const u32 oo = WS_O + blk0 * 40960 + kh * 10240 + lane * 16;
    const u32 ogr = WS_GATE + (kh * 4 * 128 + blk0) * 64 + i16 * 4;
    auto load_a = [&]() {
#pragma unroll
      for (int r = 0; r < 2; ++r) gt[r] = asf(gld32(wsr, ogr + r * 64));
#pragma unroll
      for (int n = 0; n < 10; ++n)
#pragma unroll
        for (int r = 0; r < 2; ++r) ofa[n][r] = gld128(wsr, oo + r * 40960 + 20480 + n * 1024);
    };
    const u32 fa = WS_AFLAG + ((rt2 * 4 + par2 * 2 + rp) * 8 + kh * 4 + (lane & 3)) * 8;
    await(ws, WS_BFLAG + (((rt2 * 2 + par2) * 2 + rp) * 16 + kh * 8 + (lane & 7)) * 8, tag);
    STAMP(7);
    u64 av;
#pragma unroll
    for (int n = 0; n < 10; ++n) {
#pragma unroll
      for (int r = 0; r < 2; ++r) ofb[n][r] = gld128(wsr, oo + r * 40960 + n * 1024);
      if (n == AVN) { SB(); av = __hip_atomic_load((GAS u64*)(ws + fa), RLX_AGENT); SB(); }
    }
    bool early = false;
    u32 oe = OOB, og = OOB;
    {
      constexpr int D = 6;
      u32x4 w[D + 1];
      u32 am = 0;
      sfor<-D, 100>([&](auto T) {
        constexpr int t = T.v, n = t + D, gr = n / 25;
        if constexpr (n < 100 && n % 25 == 0) {
          if constexpr (n == 25 || n == 50) { fence_c(); stw(lds0 + L_D2 + wave * 4, n / 25); }
          lds_spin<1>(lds0 + L_WC + (2 * gr + kh) * 4, 5);
          am = wreg(gr) + kh * CHUNK + lane * 16; PIN(am);
          if constexpr (n == 0) STAMP(8);
        }
        if constexpr (t == 4) { early = __all(av == tag); fence_c(); oe = early ? oo + 20480 : OOB; og = early ? ogr : OOB; }
        if constexpr (t == 4 || t == 5) gt[t & 1] = asf(gld32(wsr, og + (t & 1) * 64));
        if constexpr (t >= 6 && t < 46 && !(t & 1)) ofa[(t - 6) >> 2][(t >> 1 & 1) ^ 1] = gld128(wsr, oe + ((t >> 1 & 1) ^ 1) * 40960 + ((t - 6) >> 2) * 1024);
        if constexpr (t == 50) {
          STAMP(9);
          if (!early) { await(ws, fa, tag); load_a(); }
          STAMP(10);
          mma_done();
#pragma unroll
          for (int r = 0; r < 2; ++r)
#pragma unroll
            for (int cc = 0; cc < 5; ++cc) { acc[r][cc] *= 1.f - gt[r]; PIN(acc[r][cc]); }
          asm volatile("s_nop 1");
        }
        SB();
        if constexpr (t >= 0) mmai(acc[0][t % 5], w[t % (D + 1)], (t < 50 ? ofb : ofa)[t / 5 % 10][0]);
        SB();
        if constexpr (n < 100) w[n % (D + 1)] = ld128(am + (n % 25) * 1024);
        SB();
        if constexpr (t >= 0) mmai(acc[1][t % 5], w[t % (D + 1)], (t < 50 ? ofb : ofa)[t / 5 % 10][1]);
        SB();
      });
      mma_done();
    }
    STAMP(11);
    lds_spin<1>(lds0 + L_D2 + (lane & 3) * 4, 2);
    const u32 xb = wreg(1);
#pragma unroll
    for (int c = 0; c < 5; ++c) *(LAS f32x4*)(size_t)(xb + wave * 5120 + c * 1024 + lane * 16) = kh ? acc[0][c] : acc[1][c];
    fence_c(); stw(lds0 + L_D2 + wave * 4, 3);
    lds_spin<0>(lds0 + L_D2 + (wave ^ 2) * 4, 3);
    const u32 blk = blk0 + kh;
    const int tok = (blk >> 3) * 128 + (blk & 3) * 32 + ((blk >> 2) & 1) + 2 * i16;
    const rsrc_t outr = __builtin_amdgcn_make_buffer_rsrc(out, 0, 2048 * 1280, 0x00020000);
    const u32 orow = tok * 1280 + ctile * 160;
    u32x2 v[5];
#pragma unroll
    for (int c = 0; c < 5; ++c) v[c] = pk4((kh ? acc[1][c] : acc[0][c]) + *(LAS f32x4*)(size_t)(xb + (wave ^ 2) * 5120 + c * 1024 + lane * 16));
#pragma unroll
    for (int c = 0; c < 4; c += 2) {
      auto x = __builtin_amdgcn_permlane16_swap(v[c][0], v[c + 1][0], false, false);
      auto y = __builtin_amdgcn_permlane16_swap(v[c][1], v[c + 1][1], false, false);
      __builtin_amdgcn_raw_buffer_store_b128(u32x4{x[0], y[0], x[1], y[1]}, outr, orow + (c + (g & 1)) * 32 + (g >> 1) * 16, 0, OUT_AUX);
    }
    __builtin_amdgcn_raw_buffer_store_b64(v[4], outr, orow + 128 + g * 8, 0, OUT_AUX);
    STAMP(12);
    STAMP(13); STAMP_OUT;
  }
}

extern "C" void kernel_launch(void* const* d_in, const int* in_sizes, int n_in, void* d_out, int out_size, void* d_ws, size_t ws_size,
                              hipStream_t stream) {
  (void)hipFuncSetAttribute((const void*)fused_kernel, hipFuncAttributeMaxDynamicSharedMemorySize, LDS_BYTES);
  fused_kernel<<<256, 512, LDS_BYTES, stream>>>((const char*)d_in[0], (const char*)d_in[1], (const char*)d_in[2], (const char*)d_in[4],
                                                (const char*)d_in[5], (const char*)d_in[6], (char*)d_out, (char*)d_ws);
}
```

```cpp
#define QGATE 4
#include <hip/hip_runtime.h>
#include <hip/hip_bf16.h>

typedef unsigned long long u64;
typedef unsigned u32;
typedef u32 u32x2 __attribute__((ext_vector_type(2)));
typedef u32 u32x4 __attribute__((ext_vector_type(4)));
typedef float f32x2 __attribute__((ext_vector_type(2)));
typedef float f32x4 __attribute__((ext_vector_type(4)));
typedef short s16x4 __attribute__((ext_vector_type(4)));
typedef __bf16 bf16x2 __attribute__((ext_vector_type(2)));
typedef __bf16 bf16x8 __attribute__((ext_vector_type(8)));
#define LAS __attribute__((address_space(3)))
#define GAS __attribute__((address_space(1)))
#define DEV __device__ __forceinline__
#define SB() __builtin_amdgcn_sched_barrier(0)
#define PIN(x) asm volatile("" : "+v"(x))
#define RLX_AGENT __ATOMIC_RELAXED, __HIP_MEMORY_SCOPE_AGENT

#define STAMP_DECL
#define CSTAMP(i)
#define STAMP(i)
#define STAMP_OUT
#define ROT 5
#define TAILG 18
#define A_G0W 10
#define PACK_AT 0
#define A_PW 17
#define TOKS 0
#define AVN 4
#define OUT_AUX 16
#define FB 24
#define FA 18
#define ST_SLEEP 0
#define B_R1 19
#define POLL_SLEEP 2

constexpr u32 WS_EPOCH = 0;
#define FSTR 128
constexpr u32 WS_KVFLAG = 4096;
constexpr u32 WS_BFLAG = WS_KVFLAG + 32 * FSTR;
constexpr u32 WS_AFLAG = WS_BFLAG + 128 * FSTR;
constexpr u32 WS_PFLAG = WS_AFLAG + 128 * FSTR;
constexpr u32 WS_GATE = WS_PFLAG + 4096;
constexpr u32 WS_KVP = WS_GATE + 8 * 128 * 64;
constexpr u32 KVP_STRIDE = 896;
constexpr u32 WS_O = WS_KVP + 512 * KVP_STRIDE;
constexpr u32 WS_WOP = WS_O + 128 * 40960;
constexpr u32 WS_WQP = WS_WOP + 1600 * 1024;
constexpr u32 WS_END = WS_WQP + 800 * 1024;
constexpr u64 TAG_STEP = 0x9E3779B97F4A7C15ull;
constexpr u32 SPIN_CAP = 1u << 18;
constexpr u32 OOB = 0x80000000u;

constexpr u32 SEG = 5136;
constexpr u32 BX_SLOT = 18432, BW_SLOT = 15424, L_BX1 = 36864, L_BX2 = L_BX1 + BX_SLOT, L_BW = L_BX2 + BX_SLOT, L_BX0 = L_BW + 4 * BW_SLOT;
constexpr u32 AX_SLOT = 16384, AW_SLOT = 2 * SEG, L_AX = 47104, L_AW = L_AX + 4 * AX_SLOT;
constexpr u32 L_MISC = L_BX0 + BX_SLOT;
constexpr u32 L_HO = L_BW + 2 * BW_SLOT;
constexpr u32 HO_STRIDE = 11264, L_YX = L_MISC;
constexpr u32 L_GATEW = L_MISC, L_TAB = L_MISC + 1280, TAB_STRIDE = 1024;
constexpr u32 L_CTL = L_MISC + 9216;
constexpr u32 L_DONE = L_CTL + 16;
constexpr u32 L_CNT = L_CTL + 64;
constexpr u32 L_WC = L_CTL + 192;
constexpr u32 L_D2 = L_CTL + 224;
constexpr u32 L_ISS = L_CTL + 244;
constexpr u32 L_PUB = L_CTL + 240;
constexpr u32 L_SCR = L_CTL + 256;
constexpr u32 CHUNK = 25600;
constexpr u32 LDS_BYTES = 163840;
static_assert(L_BX0 % 128 == 0 && L_HO >= 4 * CHUNK && L_HO + 4 * 11264 <= L_MISC && L_MISC >= 6 * CHUNK && L_AW + 4 * AW_SLOT <= L_MISC && L_SCR + 256 <= LDS_BYTES, "lds map");

template <int V> struct IC { static constexpr int v = V; };
template <int I, int N, class F> DEV void sfor(F&& f) { if constexpr (I < N) { f(IC<I>{}); sfor<I + 1, N>(f); } }
template <int N> DEV void waitv() { asm volatile("s_waitcnt vmcnt(%0)" ::"i"(N) : "memory"); }
DEV void waitl0() { asm volatile("s_waitcnt lgkmcnt(0)" ::: "memory"); }
DEV void bar() { asm volatile("s_barrier" ::: "memory"); }
DEV void fence_c() { asm volatile("" ::: "memory"); }
DEV void glds(const char* sbase, u32 voff, u32 lds) {
  asm volatile("s_mov_b32 m0, %2\n\ts_nop 0\n\tglobal_load_lds_dwordx4 %0, %1" ::"v"(voff), "s"(sbase), "s"(lds));
}
DEV void touch(const char* sbase, u32 voff, u32 lds) {
  asm volatile("s_mov_b32 m0, %2\n\ts_nop 0\n\tglobal_load_lds_dword %0, %1 sc1" ::"v"(voff), "s"(sbase), "s"(lds));
}
DEV void glds4(const char* sbase, u32 v0, u32 v1, u32 v2, u32 v3, u32 lds) {
  asm volatile("s_mov_b32 m0, %5\n\ts_nop 0\n\tglobal_load_lds_dwordx4 %0, %4\n\tglobal_load_lds_dwordx4 %1, %4 offset:1024\n\t"
               "global_load_lds_dwordx4 %2, %4 offset:2048\n\tglobal_load_lds_dwordx4 %3, %4 offset:3072"
               ::"v"(v0), "v"(v1), "v"(v2), "v"(v3), "s"(sbase), "s"(lds));
}
DEV void glds5p(const char* sbase, u32 v, u32 v4, u32 lds) {
  asm volatile("s_mov_b32 m0, %3\n\ts_nop 0\n\tglobal_load_lds_dwordx4 %0, %2 sc1\n\tglobal_load_lds_dwordx4 %0, %2 offset:1024 sc1\n\t"
               "global_load_lds_dwordx4 %0, %2 offset:2048 sc1\n\tglobal_load_lds_dwordx4 %0, %2 offset:3072 sc1\n\t"
               "s_add_u32 m0, %3, 4096\n\ts_nop 0\n\tglobal_load_lds_dwordx4 %1, %2 sc1" ::"v"(v), "v"(v4), "s"(sbase), "s"(lds) : "scc");
}
constexpr int wpos(int p) { return (p / 5) * (int)SEG + (p % 5) * 1024; }
template <int P> DEV void gldsw(const char* sbase, const u32 (&v)[15], u32 lds) {
  if constexpr (P < 12)
    asm volatile("s_add_u32 m0, %5, %6\n\ts_nop 0\n\tglobal_load_lds_dwordx4 %0, %4\n\tglobal_load_lds_dwordx4 %1, %4 offset:%7\n\t"
                 "global_load_lds_dwordx4 %2, %4 offset:%8\n\tglobal_load_lds_dwordx4 %3, %4 offset:%9"
                 ::"v"(v[P]), "v"(v[P + 1]), "v"(v[P + 2]), "v"(v[P + 3]), "s"(sbase), "s"(lds), "i"(wpos(P)), "i"(wpos(P + 1) - wpos(P)),
                   "i"(wpos(P + 2) - wpos(P)), "i"(wpos(P + 3) - wpos(P)) : "scc");
  else
    asm volatile("s_add_u32 m0, %4, %5\n\ts_nop 0\n\tglobal_load_lds_dwordx4 %0, %3\n\tglobal_load_lds_dwordx4 %1, %3 offset:%6\n\t"
                 "global_load_lds_dwordx4 %2, %3 offset:%7"
                 ::"v"(v[P]), "v"(v[P + 1]), "v"(v[P + 2]), "s"(sbase), "s"(lds), "i"(wpos(P)), "i"(wpos(P + 1) - wpos(P)),
                   "i"(wpos(P + 2) - wpos(P)) : "scc");
}
DEV u32x4 ld128(u32 a) { return *(LAS u32x4*)(size_t)a; }
DEV u32x2 ld64(u32 a) { return *(LAS u32x2*)(size_t)a; }
DEV void st64(u32 a, u32x2 v) { *(LAS u32x2*)(size_t)a = v; }
DEV u32x2 ldtr(u32 a) { return __builtin_bit_cast(u32x2, __builtin_amdgcn_ds_read_tr16_b64_v4i16((LAS s16x4*)(size_t)a)); }
DEV u32x4 cat(u32x2 a, u32x2 b) { return __builtin_shufflevector(a, b, 0, 1, 2, 3); }
DEV u32x4 ldB(u32 a0, u32 a1) { return cat(ldtr(a0), ldtr(a1)); }
DEV f32x4 mma(u32x4 a, u32x4 b, f32x4 c) {
  return __builtin_amdgcn_mfma_f32_16x16x32_bf16(__builtin_bit_cast(bf16x8, a), __builtin_bit_cast(bf16x8, b), c, 0, 0, 0);
}
DEV f32x4 mma16(u32x2 a, u32x2 b, f32x4 c) {
  return __builtin_amdgcn_mfma_f32_16x16x16bf16_1k(__builtin_bit_cast(s16x4, a), __builtin_bit_cast(s16x4, b), c, 0, 0, 0);
}
DEV void mmai(f32x4& c, const u32x4& a, const u32x4& b) { asm volatile("v_mfma_f32_16x16x32_bf16 %0, %1, %2, %0" : "+v"(c) : "v"(a), "v"(b)); }
DEV void mma_done() { asm volatile("s_nop 7\n\ts_nop 3"); SB(); }
DEV u32 pk2(float a, float b) { f32x2 v = {a, b}; return __builtin_bit_cast(u32, __builtin_convertvector(v, bf16x2)); }
DEV u32x2 pk4(f32x4 v) { return u32x2{pk2(v[0], v[1]), pk2(v[2], v[3])}; }
DEV f32x4 splat(float v) { return f32x4{v, v, v, v}; }
DEV float fmx(float a, float b) { return __builtin_elementwise_maximum(a, b); }
DEV float phi1(float u) {
  float e = __builtin_amdgcn_fmed3f(__builtin_amdgcn_exp2f(__builtin_fmaf(u, 1.44269504089f, -1.44269504089e-6f)), 0.f, 1.f);
  return fmx(u, 1e-6f) + e;
}
DEV float phi1b(float u, float one) {
  float e = __builtin_amdgcn_fmed3f(__builtin_amdgcn_exp2f(__builtin_fmaf(u, 1.44269504089f, -1.44269504089e-6f)), 0.f, 1.f);
  return __builtin_fmaf(e, one, fmx(u, 1e-6f));
}
DEV f32x4 phi4(f32x4 v) {
  float one = 1.f; asm("" : "+s"(one));
  return f32x4{phi1(v[0]), phi1b(v[1], one), phi1(v[2]), phi1b(v[3], one)};
}
DEV float asf(u32 u) { return __builtin_bit_cast(float, u); }
DEV u32 asu(float f) { return __builtin_bit_cast(u32, f); }
DEV void xmax4(float& a, float& b, float& c, float& d, float scale) {
  auto r = __builtin_amdgcn_permlane16_swap(asu(a), asu(b), false, false);
  auto t = __builtin_amdgcn_permlane16_swap(asu(c), asu(d), false, false);
  auto w = __builtin_amdgcn_permlane32_swap(asu(fmx(asf(r[0]), asf(r[1]))), asu(fmx(asf(t[0]), asf(t[1]))), false, false);
  const u32 z = asu(fmx(asf(w[0]), asf(w[1])) * scale);
  auto x = __builtin_amdgcn_permlane32_swap(z, z, false, false);
  auto y0 = __builtin_amdgcn_permlane16_swap(x[0], x[0], false, false);
  auto y1 = __builtin_amdgcn_permlane16_swap(x[1], x[1], false, false);
  a = asf(y0[0]); b = asf(y0[1]); c = asf(y1[0]); d = asf(y1[1]);
}
DEV u32x4 pair_rows(u32x2 v) {
  auto a = __builtin_amdgcn_permlane16_swap(v[0], v[0], false, false);
  auto b = __builtin_amdgcn_permlane16_swap(v[1], v[1], false, false);
  return u32x4{a[0], b[0], a[1], b[1]};
}
DEV u32 odd_rows(u32 x) { auto r = __builtin_amdgcn_permlane16_swap(x, x, false, false); return r[1]; }

template <class F> constexpr u64 lane_mask(F f) { u64 m = 0; for (int l = 0; l < 64; ++l) if (f(l >> 4, l & 15)) m |= 1ull << l; return m; }
#define LM(expr) ({ constexpr u64 m_ = lane_mask([](int g, int i) { return (bool)(expr); }); (bool)__builtin_amdgcn_inverse_ballot_w64(m_); })
typedef __amdgpu_buffer_rsrc_t rsrc_t;
DEV u32x4 gld128(rsrc_t r, u32 off) { return __builtin_amdgcn_raw_buffer_load_b128(r, off, 0, 16); }
DEV u32 gld32(rsrc_t r, u32 off) { return __builtin_amdgcn_raw_buffer_load_b32(r, off, 0, 16); }
DEV void gst128(rsrc_t r, u32 off, u32x4 v) { __builtin_amdgcn_raw_buffer_store_b128(v, r, off, 0, 16); }
DEV void gst64(rsrc_t r, u32 off, u32x2 v) { __builtin_amdgcn_raw_buffer_store_b64(v, r, off, 0, 16); }
DEV void gst32(rsrc_t r, u32 off, u32 v) { __builtin_amdgcn_raw_buffer_store_b32(v, r, off, 0, 16); }
DEV void publish(char* ws, u32 off, u64 tag, int lane) {
  waitv<0>();
  if (lane == 0) __hip_atomic_store((GAS u64*)(ws + off), tag, RLX_AGENT);
}
DEV void await(char* ws, u32 off, u64 tag) {
#pragma nounroll
  for (u32 spins = 0; spins < SPIN_CAP; ++spins) {
    u64 v = __hip_atomic_load((GAS u64*)(ws + off), RLX_AGENT);
    if (__all(v == tag)) break;
    __builtin_amdgcn_s_sleep(POLL_SLEEP);
  }
  fence_c();
}
DEV u32 ldw(u32 a) { return __hip_atomic_load((LAS u32*)(size_t)a, __ATOMIC_RELAXED, __HIP_MEMORY_SCOPE_WORKGROUP); }
DEV void stw(u32 a, u32 v) { __hip_atomic_store((LAS u32*)(size_t)a, v, __ATOMIC_RELAXED, __HIP_MEMORY_SCOPE_WORKGROUP); }
DEV void lds_arrive(u32 a, int lane, u32 n = 1) {
  if (lane == 0) __hip_atomic_fetch_add((LAS u32*)(size_t)a, n, __ATOMIC_RELAXED, __HIP_MEMORY_SCOPE_WORKGROUP);
}
template <int SLEEP> DEV void lds_spin(u32 a, int want) {
#pragma nounroll
  for (u32 spins = 0; spins < (SPIN_CAP << 4); ++spins) {
    if (__all((int)ldw(a) >= want)) break;
    if (SLEEP) __builtin_amdgcn_s_sleep(SLEEP);
  }
  fence_c();
}
template <int SLEEP> DEV void lds_await(bool ok, u32 a, int want) {
  if (__builtin_expect(!ok, 0)) lds_spin<SLEEP>(a, want);
  fence_c();
}
template <int NM> DEV void ilv() {
#pragma unroll
  for (int n = 0; n < NM; ++n) { __builtin_amdgcn_sched_group_barrier(0x8, 1, 0); __builtin_amdgcn_sched_group_barrier(0x80, 1, 0); }
}

typedef u32 u32x16 __attribute__((ext_vector_type(16)));
DEV u32 mul24(u32 a, u32 b) { return __umul24(a, b); }
DEV void w_offsets(int lane, u32 (&ow)[5]) {
#pragma unroll
  for (int i = 0; i < 5; ++i) {
    const u32 c = i * 64 + lane, pos = mul24(c, 6554u) >> 16;
    ow[i] = mul24(pos ^ ((pos >> 1) & 4), 3840u) + c * 16 - mul24(pos, 160u);
  }
}

__global__ __launch_bounds__(512, 2) void fused_kernel(const char*, const char*, const char*, const char*, const char*, const char*, char*, char*) {
  extern __shared__ __attribute__((aligned(16))) char smem[];
  u32x16 ka;
  asm volatile("s_load_dwordx16 %0, %1, 0x0" : "=s"(ka) : "s"(__builtin_amdgcn_kernarg_segment_ptr()));
  STAMP_DECL
  const u32 lds0 = (u32)(size_t)(LAS char*)smem;
  const int b = blockIdx.x, xcd = b & 7, jj = b >> 3;
  const int rt = (xcd & 1) * 8 + (jj & 7), ct = (xcd >> 1) * 4 + (jj >> 3);
  const int r0t = (((jj & 7) * 5 + (jj >> 3) * 10 + (xcd >> 1) * 3 + (xcd & 1) * 2) >> 2), r0 = r0t >= 10 ? r0t - 10 : r0t;
  const bool isB = ct >= 8;
  const int hg = ct & 7;
  const int wave = __builtin_amdgcn_readfirstlane(threadIdx.x >> 6);
  int lane = threadIdx.x & 63, g = lane >> 4, i16 = lane & 15, q4 = (lane >> 2) & 3, p4 = lane & 3;
  auto relane = [&]() {
    asm volatile("v_mbcnt_lo_u32_b32 %0, -1, 0\n\tv_mbcnt_hi_u32_b32 %0, -1, %0" : "=v"(lane));
    g = lane >> 4; i16 = lane & 15; q4 = (lane >> 2) & 3; p4 = lane & 3;
  };
  if (wave == 7) {
    const char* pc; u32 len;
    asm volatile("s_getpc_b64 %0\n.Lpf_here:\n\ts_mov_b32 %1, .Lpf_end-.Lpf_here" : "=s"(pc), "=s"(len));
    const u32 o = (lane * 32 + jj) * 128;
    if (o < len) touch(pc, o, lds0 + L_SCR);
  }
  {
    u32 ow[5];
    u32 o0 = mul24(lane >> 3, 2560u) + (((lane & 7) ^ (lane >> 4)) << 4), o1 = (o0 ^ 64) + 19456, o2 = o0 + 2 * 19456, o3 = o1 + 2 * 19456;
    w_offsets(lane, ow);
    ow[1] -= 1024; ow[2] -= 2048; ow[3] -= 3072;
    PIN(o0); PIN(o1); PIN(o2); PIN(o3); PIN(ow[0]); PIN(ow[1]); PIN(ow[2]); PIN(ow[3]); PIN(ow[4]);
    if (wave == 4 && lane < 60) stw(lds0 + L_DONE + lane * 4, 0);
    const u32 xoff = (128 * rt + 64 * (wave & 1) + (wave >> 1)) * 1280 + r0 * 128, woff = r0 * (64 * 3840) + hg * 160 + (wave + !isB) * 1280;
    asm volatile("s_waitcnt lgkmcnt(0)" : "+s"(ka)::"memory");
    if (wave < 4) {
      const char* xp = (const char*)(((u64)ka[1] << 32) | ka[0]);
      glds4(xp + xoff, o0, o1, o2, o3, lds0 + (isB ? L_BX0 : L_AX) + wave * 4096);
      if (wave < (isB ? 3 : 2)) {
        const char* wp = (const char*)(((u64)(isB ? ka[7] : ka[3]) << 32) | (isB ? ka[6] : ka[2])) + woff;
        const u32 d = lds0 + (isB ? L_BW : L_AW) + wave * SEG;
        glds4(wp, ow[0], ow[1], ow[2], ow[3], d); glds(wp, ow[4], d + 4096);
      } else if (!isB) {
        if (wave == 2 || lane < 16) glds((const char*)(((u64)ka[11] << 32) | ka[10]) + (wave - 2) * 1024, lane * 16, lds0 + L_GATEW + (wave - 2) * 1024);
      } else {
        const char* hp_ = xp + (rt ? 128 * rt - 16 : 0) * 1280 + r0 * 128;
        glds(hp_, o0, lds0 + L_BX0 + 16384); glds(hp_ + 1280, o0 ^ 64, lds0 + L_BX0 + 17408);
      }
    }
    bar();
    if (wave == 7 && lane < 2) touch((const char*)(((u64)ka[15] << 32) | ka[14]), WS_KVFLAG + (b * 2 + lane) * 128, lds0 + L_SCR);
    if (wave >= 4 && wave < 7) {
      const int u = wave - 3, xs_ = (u + 1) >> 1, pr = (u + 1) & 1;
      const int kw = r0 + (u >> 1) >= 10 ? r0 + (u >> 1) - 10 : r0 + (u >> 1), kx = r0 + xs_ >= 10 ? r0 + xs_ - 10 : r0 + xs_;
      const char* wp = (const char*)(((u64)(isB ? ka[7] : ka[3]) << 32) | (isB ? ka[6] : ka[2])) + ((kw * 64 + (u & 1) * 32) * 3840 + hg * 160);
      u32 d = lds0 + (isB ? L_BW + u * BW_SLOT : L_AW + u * AW_SLOT - SEG);
      if (ST_SLEEP) for (int z = 4; z < wave; ++z) __builtin_amdgcn_s_sleep(ST_SLEEP);
#pragma nounroll
      for (int sg = !isB; sg < 3; ++sg) { glds4(wp + sg * 1280, ow[0], ow[1], ow[2], ow[3], d + sg * SEG); glds(wp + sg * 1280, ow[4], d + sg * SEG + 4096); }
      const char* xp = (const char*)(((u64)ka[1] << 32) | ka[0]) + ((128 * rt + pr) * 1280 + kx * 128);
      d = lds0 + (isB ? L_BX1 + (xs_ - 1) * BX_SLOT : L_AX + xs_ * AX_SLOT);
      glds4(xp, o0, o1, o2, o3, d + pr * 8192); glds4(xp + 64 * 1280, o0, o1, o2, o3, d + pr * 8192 + 4096);
      if (isB) glds(xp - (rt ? 16 * 1280 : 0), o0 ^ (pr << 6), d + 16384 + pr * 1024);
    }
  }
  auto argp = [&](int n) { return (char*)(((u64)ka[2 * n + 1] << 32) | ka[2 * n]); };
  const char* const X = argp(0); const char* const WA = argp(1); const char* const WoA = argp(2); const char* const WB = argp(3);
  const char* const WoB = argp(4); const char* const GW = argp(5); char* const out = argp(6); char* const ws = argp(7);
  const rsrc_t wsr = __builtin_amdgcn_make_buffer_rsrc(ws, 0, WS_END, 0x00020000);
  const u32 aA0 = i16 * 128 + ((g ^ ((i16 >> 1) & 7)) << 4);
  const u32 rB0 = (8 * g + (q4 ^ (4 * (g & 1)))) * 160, rB1 = (8 * g + ((4 + q4) ^ (4 * (g & 1)))) * 160;
  const int rg = (xcd >> 1) * 8 + (jj & 7), ctile = (xcd & 1) * 4 + (jj >> 3), shr = jj & 7;
  auto wreg = [&](int gr) -> u32 { return lds0 + (isB ? (gr == 3 ? 0 : gr) : (gr < 2 ? 2 : gr - 2)) * (2 * CHUNK); };
  auto wo_issue = [&](int s0, int s1, int st) {
#pragma nounroll
    for (int s = s0; s < s1; s += st) {
      const int gr = (s * 26) >> 8;
      glds5p(ws + WS_WOP + (ctile * 40 + s) * 5120, lane * 16, lane * 16 + 4096, wreg(gr) + (s - gr * 10) * 5120);
    }
  };
  auto out_touch = [&](int w) { if (w < 2) touch(out, ((rg >> 1) * 128 + (rg & 1) + 2 * lane) * 1280 + ctile * 160 + w * 156, lds0 + L_SCR); };
  auto wo_post = [&](int s0, int s1, int st) {
    waitv<0>();
    const int s = s0 + lane * st;
    if (s < s1) __hip_atomic_fetch_add((LAS u32*)(size_t)(lds0 + L_WC + ((s * 52) >> 8) * 4), 1u, __ATOMIC_RELAXED, __HIP_MEMORY_SCOPE_WORKGROUP);
  };
  u64 tag;

  struct Att { u32 bk, bv, by, ya, mk, mv, my, ob, fo, b0; };
  auto att_prep = [&](int par, int hp, int half) {
    Att t;
    const u32 own = lds0 + L_HO + (hp * 2 + par) * HO_STRIDE + (hp ? 7 : 12) * 512, pi = 2 * (hg & 1) + hp;
    t.ya = lds0 + L_YX + par * 2048 + hp * 64;
    t.bk = half ? own : lds0 + L_HO + hp * 2 * HO_STRIDE + 17 * 512 + par * 64;
    t.bv = half ? own : lds0 + L_HO + (hp * 2 + 1) * HO_STRIDE + 14 * 512 + par * 256;
    t.by = half ? t.ya + 512 : t.bv + 7 * 512 + hp * 64;
    t.ya += half * 1024;
    t.mk = half ? 0x3F : 0x37; t.mv = half ? 0x3F : 0x1F; t.my = half ? 0x37 : 0x17;
    t.ob = WS_O + (rt * 8 + par * 4 + half * 2) * 40960 + (hg >> 1) * 5120 + pi * 256;
    t.fo = WS_BFLAG + (((rt * 2 + par) * 2 + half) * 2 + (hg >> 2)) * FSTR + ((hg & 3) * 2 + hp) * 8 + (pi << 24);
    t.b0 = rt == 0 && !half ? 0xF149F2CAu : 0u;
    asm volatile("" : "+s"(t.bk), "+s"(t.bv), "+s"(t.by), "+s"(t.ya), "+s"(t.mk), "+s"(t.mv), "+s"(t.my), "+s"(t.ob), "+s"(t.fo), "+s"(t.b0));
    return t;
  };
  auto attention = [&](u32x2 (&P)[2][8], const Att& t) {
    u32x2 Q[2][8], K4[2];
    {
      const u32 ya = t.ya + (lane & 0x37) * 8, ak = t.bk + (lane & t.mk) * 8, av = t.bv + (lane & t.mv) * 8;
      Q[0][2] = ld64(ak + 1024); Q[0][3] = ld64(ak + 1536); Q[0][4] = ld64(t.bk + ((lane | 16) & t.mk) * 8 + 2048);
      P[0][7] = ld64(ya); P[1][7] = ld64(ya + 512);
      Q[0][5] = ld64(av + 2560); Q[0][6] = ld64(av + 3072); Q[0][7] = ld64(t.by + (lane & t.my) * 8);
    }
    const u32x2 tg = ld64(lds0 + L_CTL);
    tag = __builtin_bit_cast(u64, tg) + TAG_STEP;
#pragma unroll
    for (int r = 0; r < 2; ++r) K4[r] = u32x2{odd_rows(P[r][4][0]), odd_rows(P[r][4][1])};
#pragma unroll
    for (int f = 2; f < 8; ++f) Q[1][f] = f == 4 ? K4[0] : P[0][f];
    const bool pv[4] = {LM(4 * g >= i + 9), LM(4 * g + 1 >= i + 9), LM(4 * g + 2 >= i + 9), LM(4 * g + 3 >= i + 9)};
    const f32x4 bc = {LM(4 * g <= i && 4 * g >= i - 7) ? 0.f : -1e30f, LM(4 * g + 1 <= i && 4 * g + 1 >= i - 7) ? 0.f : -1e30f,
                      LM(4 * g + 2 <= i && 4 * g + 2 >= i - 7) ? 0.f : -1e30f, LM(4 * g + 3 <= i && 4 * g + 3 >= i - 7) ? 0.f : -1e30f};
    const u32x2 mp = {pv[0] ? 0xFFFFFFFFu : (pv[1] ? 0xFFFF0000u : 0u), pv[2] ? 0xFFFFFFFFu : (pv[3] ? 0xFFFF0000u : 0u)};
    const bool godd = LM(g & 1), glow = LM(g < 2), gq[2] = {LM(g == 0), LM(g == 2)};
    const float cs = 0.22360679775f * 1.44269504089f, b0 = asf(t.b0);
    const u32x2 zero2 = {0u, 0u};
    const f32x4 z4 = splat(0.f);
    const u32x2 ones2 = {0x3F803F80u, 0x3F803F80u};
    f32x4 m[2][2];
    float mx[2][2];
#pragma unroll
    for (int r = 1; r >= 0; --r) {
#pragma unroll
      for (int h = 0; h < 2; ++h) {
        const u32x2 q4m = gq[h] ? P[r][4] : zero2;
        const f32x4 sc = mma16(K4[r], q4m, mma16(P[r][2 + h], P[r][h], bc));
        const f32x4 sp = mma16(Q[r][4], q4m, mma16(Q[r][2 + h], P[r][h], r ? z4 : splat(b0)));
#pragma unroll
        for (int j = 0; j < 4; ++j) m[r][h][j] = pv[j] ? sp[j] : sc[j];
        mx[r][h] = fmx(fmx(m[r][h][0], m[r][h][1]), fmx(m[r][h][2], m[r][h][3]));
      }
    }
    xmax4(mx[1][0], mx[1][1], mx[0][0], mx[0][1], -cs);
    u32x2 y[2];
#pragma unroll
    for (int r = 1; r >= 0; --r) {
      float rs[2];
      f32x4 o[2], yy[2];
#pragma unroll
      for (int h = 0; h < 2; ++h) {
        f32x4 e;
#pragma unroll
        for (int j = 0; j < 4; ++j) e[j] = __builtin_amdgcn_exp2f(__builtin_fmaf(m[r][h][j], cs, mx[r][h]));
        const u32x2 pp = pk4(e);
        const u32x2 p0 = pp & mp, p1 = pp & ~mp;
        rs[h] = __builtin_amdgcn_rcpf(mma16(ones2, pp, z4)[0]);
        o[h] = mma16(Q[r][5 + h], p0, mma16(P[r][5 + h], p1, z4)) * rs[h];
        yy[h] = mma16(Q[r][7], p0, mma16(P[r][7], p1, z4));
      }
      __builtin_amdgcn_raw_buffer_store_b128(cat(pk4(o[0]), pk4(o[1])), wsr, lane * 16, t.ob + (t.fo >> 24) * 768 + r * 40960, 16);
      y[r] = pk4((godd ? yy[1] : yy[0]) * (godd ? rs[1] : rs[0]));
    }
    __builtin_amdgcn_raw_buffer_store_b128(pair_rows(glow ? y[0] : y[1]), wsr, (godd ? OOB : (glow ? 0 : 40960)) + (lane & 15) * 16, t.ob + 4096, 16);
    STAMP(3);
    publish(ws, t.fo & 0xFFFFFF, tag, lane);
    fence_c(); lds_arrive(lds0 + L_PUB, lane);
    STAMP(4);
  };

  if (wave >= 4) {
    const int lw = wave - 4;
    __builtin_amdgcn_s_setprio(3);
    const int xpar = lw & 1;
    const u32 xo = isB && rt ? 16 * 1280 : 0;
    const char* const X0 = X + rt * (128 * 1280) - xo;
    u32 vx[9], vw[15];
    int xst = (lw >> 1) + 1;
    auto rot = [&](int st) { const int k = st + r0; return k >= 10 ? k - 10 : k; };
    auto issue_xp = [&](u32 slot, bool halo, int st, int pr) {
      const char* p = X0 + (rot(st) * 128 + pr * 1280);
      glds4(p, vx[0], vx[1], vx[2], vx[3], slot + pr * 8192); glds4(p, vx[4], vx[5], vx[6], vx[7], slot + pr * 8192 + 4096);
      if (halo) glds(p, vx[8] ^ (pr << 6), slot + 16384 + pr * 1024);
    };
    auto issue_x = [&](u32 slot, bool halo) { issue_xp(slot, halo, xst, xpar); };
    auto setup = [&](bool forB) {
      relane();
      const u32 ox0 = (lane >> 3) * 2560 + (((lane & 7) ^ (lane >> 4)) << 4);
      vx[0] = ox0 + xo; vx[1] = (ox0 ^ 64) + xo + 20480 - 1024; vx[2] = vx[0] + 2 * (20480 - 1024); vx[3] = vx[1] + 2 * (20480 - 1024);
#pragma unroll
      for (int p = 0; p < 4; ++p) vx[4 + p] = vx[p] + 64 * 1280;
      vx[8] = ox0;
      u32 ow[5];
      w_offsets(lane, ow);
#pragma unroll
      for (int p = 0; p < 15; ++p) { if (forB || p < 10) { vw[p] = ow[p % 5] + (p / 5) * 1280 - (wpos(p) - wpos(p & ~3)); PIN(vw[p]); } }
    };
    const char* Wt = (isB ? WB : WA) + hg * 160;
    auto wsrc = [&](int u) { return Wt + (rot(u >> 1) * 64 + (u & 1) * 32) * 3840; };
    const u32 cw = lds0 + L_CNT + (lw + 1) * 4;
    const bool qjob = lw == 2, pjob = qjob ? b >= 96 : (lw == 3 && b < 200);
    int jn = qjob ? b - 96 : b, jh, jl, sg, pst;
    const u32 pl = lds0 + (qjob ? 8192 : 0);
    auto pack_load = [&]() {
      asm volatile("" : "+s"(jn));
      jh = qjob ? (jn * 205) >> 12 : (jn * 205) >> 13; jl = jn - jh * (qjob ? 20 : 40);
      const int k20 = jl >= 20 ? jl - 20 : jl;
      sg = (k20 * 52) >> 8; pst = k20 - sg * 5;
      const u32 rs = qjob ? 3840 : 1280, v = g * rs + i16 * 16;
      const char* src = qjob ? WA + jl * (32 * 3840) + ((jh * 160) & ~127) : (jl < 20 ? WoB : WoA) + (sg * 160 + (pst < 4 ? 40 * pst : 16)) * 1280 + jh * 256;
      const u32 r1 = (qjob || pst < 4 ? 4 : 40) * rs, r4 = (qjob ? 16 : 20) * rs;
#pragma unroll
      for (int p = 0; p < 8; ++p) glds(src + (p & 3) * r1 + (p >> 2) * r4, v, pl + p * 1024);
    };
    auto pack_store = [&]() {
      asm volatile("" : "+s"(jn), "+s"(jh), "+s"(jl), "+s"(sg), "+s"(pst));
      const u32 a = pl + ((qjob ? 8 : 4) * g + q4) * 256 + (qjob ? (jh * 160) & 127 : 0), fs = qjob ? 40 : 32, a2 = qjob ? 1024 : 4096;
      u32x4 fr[8];
#pragma unroll
      for (int f = 0; f < 8; ++f) { const u32 x = a + (qjob && f >= 4 ? 32 + p4 * 40 : f * fs + p4 * 8); fr[f] = ldB(x, x + a2); }
      const int step = ((jl < 20 ? 0 : 2) + (sg & 1)) * 10 + (sg >> 1) * 5 + pst;
#pragma unroll
      for (int f = 0; f < 8; ++f) {
        const int F = jh * 8 + f, hi = (F * 52) >> 8, lo = F - hi * 5;
        const u32 d = qjob ? (f < 5 ? WS_WQP + (jn * 5 + f) * 1024 : OOB) : WS_WOP + ((hi * 40 + step) * 5 + lo) * 1024;
        __builtin_amdgcn_raw_buffer_store_b128(fr[f], wsr, d + lane * 16, 0, 16);
      }
    };
    auto pack_flag = [&]() {
      const u64 t = *(LAS u64*)(size_t)(lds0 + L_CTL) + TAG_STEP;
      if (lane == 0) __hip_atomic_store((GAS u64*)(ws + WS_PFLAG + ((qjob ? 200 : 0) + jn) * 8), t, RLX_AGENT);
    };
    auto first = [&]() {
      if (pjob) pack_load();
      if (lw == 1 && lane < 2)
        asm volatile("s_mov_b32 m0, %2\n\ts_nop 0\n\tglobal_load_lds_dword %0, %1 sc1" ::"v"(lane * 4), "s"(ws + WS_EPOCH), "s"(lds0 + L_CTL));
    };
    auto first_done = [&](auto NP) {
      if (!pjob) { waitv<0>(); stw(cw, 4); }
      else if (qjob) { waitv<8>(); stw(cw, 4); waitv<0>(); pack_store(); }
      else { waitv<NP.v>(); pack_store(); waitv<8>(); stw(cw, 4); }
    };
    auto pack_wait = [&]() {
      const u64 t = *(LAS u64*)(size_t)(lds0 + L_CTL) + TAG_STEP;
      const u32 f0 = WS_PFLAG + (lane < 40 ? ((ctile * 5) >> 3) * 40 + lane : 200 + hg * 20 + (lane < 60 ? lane - 40 : 0)) * 8;
      const u32 f1 = WS_PFLAG + (((ctile * 5 + 4) >> 3) * 40 + (lane < 40 ? lane : 0)) * 8;
#pragma nounroll
      for (u32 spins = 0; spins < SPIN_CAP; ++spins) {
        const u64 v0 = __hip_atomic_load((GAS u64*)(ws + f0), RLX_AGENT), v1 = __hip_atomic_load((GAS u64*)(ws + f1), RLX_AGENT);
        if (__all(v0 == t && v1 == t)) break;
        __builtin_amdgcn_s_sleep(POLL_SLEEP);
      }
      fence_c();
    };
    if (isB) {
      setup(true); STAMP(1); first(); STAMP(2);
      const u32 wd = lds0 + L_BW + ((lw + 1) & 3) * BW_SLOT, da = lds0 + L_DONE + (lane & 3) * 4;
      u32 xs = lds0 + L_BX1 + (lw >> 1) * BX_SLOT;
      auto unit = [&](int u) {
        lds_spin<1>(da, u - 3);
        const char* wp = wsrc(u); gldsw<0>(wp, vw, wd); gldsw<4>(wp, vw, wd); gldsw<8>(wp, vw, wd); gldsw<12>(wp, vw, wd);
        if (u < 19) issue_x(xs, true);
      };
      if (lw == 3) unit(4);
      STAMP(9);
      first_done(IC<24>{});
      STAMP(10);
#pragma nounroll
      for (int u = lw + 5; u < 20; u += 4) {
        xst += 2; xs = lds0 + (xs == lds0 + L_BX0 ? L_BX2 : (xs == lds0 + L_BX2 ? L_BX1 : L_BX0));
        unit(u);
        waitv<0>(); stw(lds0 + L_CNT + u * 4, 4);
        if (u < 9 && pjob) pack_flag();
      }
      STAMP(11);
      if (lw == 3) { pack_wait(); stw(lds0 + L_CNT + 23 * 4, 4); }
      lds_spin<1>(lds0 + L_CNT + ((lane & 1) ? TAILG : 23) * 4, 4);
      wo_issue(3 - lw, 7, 4);
      const Att at = att_prep(lw & 1, lw >> 1, (lw >> 1) ^ 1);
      lds_spin<1>(da, 18);
      wo_issue(7 + lw, 20, 4);
      wo_post(3 - lw, 7, 4); wo_post(7 + lw, 20, 4);
      out_touch(lw);
      __builtin_amdgcn_s_setprio(0);
      bar();
      u32x2 P[2][8];
      const u32 hl = lds0 + L_HO + lw * HO_STRIDE + lane * 8;
#pragma unroll
      for (int f = 0; f < 7; ++f) { P[0][f] = ld64(hl + f * 512); P[1][f] = ld64(hl + (7 + f) * 512); }
      attention(P, at);
      lds_spin<1>(lds0 + L_PUB, 8);
    } else {
      setup(false); first();
      const u32 wd = lds0 + L_AW + ((lw + 1) & 3) * AW_SLOT, da = lds0 + L_DONE + (lane & 7) * 4;
      u32x4 xr[20][2];
      f32x4 gl[2] = {};
      u32 xa0 = lds0 + L_AX + lw * 4096 + i16 * 128 + ((g ^ ((i16 >> 1) & 7)) << 4), xa1 = xa0 ^ 64;
      PIN(xa0); PIN(xa1);
      auto take = [&](int st) {
        lds_spin<1>(lds0 + L_CNT + (st ? 2 * st - (lane & 1) : 0) * 4, 4);
        const u32 o = (st & 3) * AX_SLOT;
        xr[2 * st][0] = ld128(xa0 + o); xr[2 * st][1] = ld128(xa0 + o + 2048);
        xr[2 * st + 1][0] = ld128(xa1 + o); xr[2 * st + 1][1] = ld128(xa1 + o + 2048);
        fence_c(); stw(lds0 + L_DONE + wave * 4, st == 9 ? 30 : (st > 6 ? 19 : 2 * st + 5));
        const u32 go = lds0 + L_GATEW + rot(st) * 128 + g * 16;
        const u32x4 g0 = ld128(go), g1 = ld128(go + 64);
        gl[0] = mma(g1, xr[2 * st + 1][0], mma(g0, xr[2 * st][0], gl[0])); gl[1] = mma(g1, xr[2 * st + 1][1], mma(g0, xr[2 * st][1], gl[1]));
      };
      auto wq = [&](int n) { glds5p(ws + WS_WQP + (hg * 20 + rot(n >> 1) * 2 + (n & 1)) * 5120, lane * 16, lane * 16 + 4096, lds0 + n * 5120); };
      stw(lds0 + L_DONE + wave * 4, 3);
#pragma unroll
      for (int j = 0; j < 5; ++j) {
        const int u = 4 * j + lw + 1;
        if (j >= 1) take(2 * j - 2);
        if (j == 0 ? lw == 3 : (j < 4 || lw < 3)) {
          lds_spin<1>(da, u - 3);
          const char* wp = wsrc(u) + 1280; gldsw<0>(wp, vw, wd); gldsw<4>(wp, vw, wd);
          asm volatile("s_add_u32 m0, %3, %4\n\ts_nop 0\n\tglobal_load_lds_dwordx4 %0, %2\n\tglobal_load_lds_dwordx4 %1, %2 offset:1024"
                       ::"v"(vw[8]), "v"(vw[9]), "s"(wp), "s"(wd), "i"(wpos(8)) : "scc");
          if (u < 19) issue_x(lds0 + L_AX + (xst & 3) * AX_SLOT, false);
        }
        xst += 2;
        if (j >= 1) take(2 * j - 1);
        if (j == 4 && lw == 3) { lds_spin<1>(lds0 + L_CNT + A_PW * 4, 4); pack_wait(); }
        if (j == 0) STAMP(9);
        if (j == 0) first_done(IC<18>{}); else { waitv<0>(); stw(cw + j * 16, 4); }
        if (j == 0) STAMP(10); if (j == 4) STAMP(11);
        if (j == 1 && pjob) pack_flag();
      }
      take(8); take(9);
      lds_spin<1>(lds0 + L_CNT + 20 * 4, 4);
      wq(lw); wq(lw + 4);
      lds_spin<1>(da, 20);
      STAMP(12);
#pragma nounroll
      for (int n = 8 + lw; n < 20; n += 4) wq(n);
      waitv<15>(); lds_arrive(lds0 + L_CNT + 21 * 4, lane);
      wo_issue(3 - lw, 10, 4);
      __builtin_amdgcn_s_setprio(0);
      STAMP(1);
      relane();
      float gate[2];
#pragma unroll
      for (int r = 0; r < 2; ++r) { gate[r] = __builtin_amdgcn_rcpf(1.f + __builtin_amdgcn_exp2f(gl[r][0] * -1.44269504089f)); PIN(gate[r]); }
      f32x4 qa[2][5];
#pragma unroll
      for (int f = 0; f < 5; ++f) { qa[0][f] = qa[1][f] = splat(1e-6f); PIN(qa[0][f]); PIN(qa[1][f]); }
      {
        u32 q0 = lds0 + lane * 16;
        constexpr int D = 6;
        u32x4 w[D + 1];
        sfor<-D, 100>([&](auto T) {
          constexpr int t = T.v, n = t + D;
          if constexpr (n == 0) lds_spin<1>(lds0 + L_CNT + 21 * 4, QGATE);
          if constexpr (n == 20) { wo_post(3 - lw, 10, 4); stw(lds0 + L_CNT + (24 + lw) * 4, 4); }
          if constexpr (n == 40) lds_spin<1>(lds0 + L_CNT + (24 + (lane & 3)) * 4, 4);
          if constexpr (n == 60) { q0 += 61440; PIN(q0); }
          SB();
          if constexpr (t >= 0) mmai(qa[0][t % 5], w[t % (D + 1)], xr[t / 5][0]);
          SB();
          if constexpr (n < 100) w[n % (D + 1)] = ld128(q0 + (n >= 60 ? n - 60 : n) * 1024);
          SB();
          if constexpr (t >= 0) mmai(qa[1][t % 5], w[t % (D + 1)], xr[t / 5][1]);
          SB();
        });
        mma_done();
      }
      __builtin_amdgcn_s_setprio(1);
      STAMP(2);
      relane();
      u32x2 pq[2][5];
#pragma unroll
      for (int r = 0; r < 2; ++r)
#pragma unroll
        for (int f = 0; f < 5; ++f) { pq[r][f] = pk4(phi4(qa[r][f])); PIN(pq[r][f]); }
      u32 ta[4][4];
      {
        const u32 tb = lds0 + L_TAB, tz = tb + 105 * 8, t0 = tb + (64 + i16) * 8, t1 = tb + (80 + p4 + 4 * g) * 8, t2 = tb + (100 + p4) * 8;
#pragma unroll
        for (int h = 0; h < 4; ++h) {
          const bool gh = g == h, eh = q4 == (h ^ 1);
          ta[h][0] = gh ? t0 : tz; ta[h][1] = eh ? t1 : tz; ta[h][2] = eh && gh ? t2 : tz; ta[h][3] = gh ? tz - 8 : tz;
#pragma unroll
          for (int i = 0; i < 4; ++i) PIN(ta[h][i]);
        }
      }
      __builtin_amdgcn_s_setprio(3);
      waitl0(); bar();
      STAMP(3);
      tag = *(LAS u64*)(size_t)(lds0 + L_CTL) + TAG_STEP;
      f32x4 n5[2] = {};
      u32x2 ob[2][4];
      float sc5[2] = {0.f, 0.f};
#pragma unroll
      for (int h = 0; h < 4; ++h) {
        const u32 tb = lds0 + L_TAB + h * TAB_STRIDE;
        const u32x4 a0 = cat(ld64(tb + lane * 8), ld64(ta[h][0] + h * TAB_STRIDE));
        const u32x4 a5 = cat(ld64(ta[h][1] + h * TAB_STRIDE), ld64(ta[h][2] + h * TAB_STRIDE));
        const u32x4 az = cat(ld64(tb + (96 + g) * 8), ld64(ta[h][3] + h * TAB_STRIDE));
#pragma unroll
        for (int r = 0; r < 2; ++r) {
          const u32x4 bo = cat(pq[r][h], pq[r][4]);
          const f32x4 num = mma(a0, bo, splat(0.f)), den = mma(az, bo, splat(1e-6f));
          n5[r] = mma(a5, bo, n5[r]);
          const float sc = gate[r] * __builtin_amdgcn_rcpf(den[0]);
          ob[r][h] = pk4(num * sc);
          sc5[r] = (g ^ 1) == h ? sc : sc5[r];
        }
      }
#pragma unroll
      for (int r = 0; r < 2; ++r) {
        const u32 blk = rt * 8 + lw * 2 + r;
        const u32 base = WS_O + blk * 40960 + 20480 + (hg >> 1) * 5120;
        gst128(wsr, base + (hg & 1) * 2048 + lane * 16, cat(ob[r][0], ob[r][1]));
        gst128(wsr, base + (hg & 1) * 2048 + 1024 + lane * 16, cat(ob[r][2], ob[r][3]));
        const u32x4 o5 = pair_rows(pk4(n5[r] * sc5[r]));
        gst128(wsr, (g & 1) ? OOB : base + 4096 + ((2 * (hg & 1) + (g >> 1)) * 16 + i16) * 16, u32x4{o5[2], o5[3], o5[0], o5[1]});
      }
      gst32(wsr, lane < 32 ? WS_GATE + (hg * 128 + rt * 8 + lw * 2) * 64 + lane * 4 : OOB, asu(lane < 16 ? gate[0] : gate[1]));
      STAMP(5);
      fence_c(); lds_arrive(lds0 + L_PUB, lane);
      publish(ws, WS_AFLAG + ((rt * 4 + lw) * 2 + (hg >> 2)) * FSTR + (hg & 3) * 8, tag, lane);
      STAMP(4);
    }
    if (isB) { wo_issue(20 + lw, 30, 4); wo_post(20 + lw, 30, 4); }
    lds_spin<2>(lds0 + L_ISS, 4);
    wo_issue(30 + lw, 40, 4);
    wo_post(30 + lw, 40, 4);
    STAMP(8);
    if (b == 0 && lw == 0) {
#pragma unroll
      for (int r = 0; r < 2; ++r) {
        const int n = r * 64 + lane;
        await(ws, WS_BFLAG + ((n >> 3) * 8 + ((n >> 2) & 1)) * FSTR + (n & 3) * 16, tag);
        await(ws, WS_AFLAG + ((n >> 3) * 8 + ((n >> 2) & 1)) * FSTR + (n & 3) * 8, tag);
      }
      if (lane == 0) __hip_atomic_store((GAS u64*)(ws + WS_EPOCH), tag, RLX_AGENT);
    }
    STAMP(13); STAMP_OUT;
    asm volatile(".Lpf_end:");
    return;
  }

  {
  }
  auto get_tag = [&]() {
    const u32x2 t = ld64(lds0 + L_CTL);
    tag = ((u64)(u32)__builtin_amdgcn_readfirstlane(t[1]) << 32) | (u32)__builtin_amdgcn_readfirstlane(t[0]);
    tag += TAG_STEP;
  };
  u32x4 g0[25];
  auto take_g0 = [&]() {
    lds_spin<1>(lds0 + L_WC + (wave >> 1) * 4, 5);
    const u32 a = wreg(0) + (wave >> 1) * CHUNK + lane * 16;
#pragma unroll
    for (int n = 0; n < 25; ++n) g0[n] = ld128(a + n * 1024);
    fence_c(); stw(lds0 + L_D2 + wave * 4, 1);
  };
  u32 ca = lds0 + L_CNT, dcount = 1;
  u32 dn = lds0 + L_DONE + wave * 4;
  PIN(dn);
  if (isB) {
    const int par = wave & 1, hp = wave >> 1;
    u32 bM0 = lds0 + L_BW + rB0 + hp * 80 + p4 * 8, bM1 = bM0 - rB0 + rB1;
    u32 bX0 = lds0 + L_BW + rB0 + hp * 80 + 32 + (p4 & 1) * SEG + (p4 >> 1) * 40, bX1 = bX0 - rB0 + rB1;
    u32 bY0 = lds0 + L_BW + rB0 + 2 * SEG + 32 + p4 * 40, bY1 = bY0 - rB0 + rB1;
    u32 alo = aA0 + par * 8192 + hp * 4096, ahi = aA0 + par * 8192 + (hp ^ 1) * 4096;
    PIN(bM0); PIN(bM1); PIN(bX0); PIN(bX1); PIN(bY0); PIN(bY1); PIN(alo); PIN(ahi); PIN(ca); PIN(dcount);
    const Att at = att_prep(par, hp, hp);
    f32x4 acc[4][7], accy[2], acch[3];
#pragma unroll
    for (int f = 0; f < 3; ++f) { acch[f] = splat(0.f); PIN(acch[f]); }
#pragma unroll
    for (int r = 0; r < 4; ++r)
#pragma unroll
      for (int f = 0; f < 7; ++f) { acc[r][f] = splat(0.f); PIN(acc[r][f]); }
    accy[0] = accy[1] = splat(0.f); PIN(accy[0]); PIN(accy[1]);
    struct Fr { u32x4 a[4]; u32x2 w[8][2]; };
    u32 xs = lds0 + L_BX0, x0 = xs + alo, x1 = xs + ahi;
    const u32 hoff = 16384 - par * 8192 - hp * 4096;
    u32x4 ah[2];
    auto rd1 = [&](Fr& F, int sl, int n) {
      const u32 o = sl * BW_SLOT;
      if (n < 4) { F.a[n] = ld128((n < 2 ? x0 : x1) + (n & 1) * 2048); return; }
      const int f = (n - 4) >> 1, t = n & 1;
      if (f == 4) F.w[4][t] = ldtr((t ? bX1 : bX0) + o);
      else if (f == 5) F.w[7][t] = ldtr((t ? bY1 : bY0) + o);
      else { const int ff = f < 4 ? f : f - 2; F.w[f < 4 ? f : f - 1][t] = ldtr((t ? bM1 : bM0) + o + (ff >> 1) * SEG + (ff & 1) * 40); }
    };
    auto mm1 = [&](Fr& F, int n) {
      if (n >= 20 && n < 22) { mmai(accy[n - 20], F.a[n - 20], cat(F.w[7][0], F.w[7][1])); return; }
      const int m = n < 20 ? n : n - 2, f = m >> 2, r = m & 3;
      const u32x4 w = cat(F.w[f][0], F.w[f][1]);
      if (f < 5) mmai(acc[r][f], w, F.a[r]); else mmai(acc[r][f], F.a[r], w);
    };
    Fr F0, F1;
    waitv<0>(); lds_arrive(lds0 + L_CNT, lane);
    lds_spin<0>(ca, 4);
    STAMP(1); CSTAMP(14);
#pragma unroll
    for (int n = 0; n < 20; ++n) rd1(F0, 0, n);
    ah[0] = ld128(x0 + hoff);
    fence_c(); stw(dn, dcount);
    bool ok = false;
    x0 = xs + (alo ^ 64); x1 = xs + (ahi ^ 64);
    auto unit = [&](Fr& Fc, Fr& Fn, int k, bool last) {
      u32 cv;
#pragma unroll
      for (int n = 0; n < 30; ++n) {
        SB(); mm1(Fc, n); SB();
        if (n == 3) {
          asm volatile("s_cmp_lg_u32 %10, 0\n\ts_cbranch_scc1 .Lhv%=\n\t"
                       "v_mfma_f32_16x16x32_bf16 %0, %4, %3, %0\n\tv_mfma_f32_16x16x32_bf16 %1, %5, %3, %1\n\tv_mfma_f32_16x16x32_bf16 %2, %6, %3, %2\n\t"
                       "s_branch .Lhe%=\n.Lhv%=:\n\t"
                       "v_mfma_f32_16x16x32_bf16 %0, %3, %7, %0\n\tv_mfma_f32_16x16x32_bf16 %1, %3, %8, %1\n\tv_mfma_f32_16x16x32_bf16 %2, %3, %9, %2\n.Lhe%=:"
                       : "+v"(acch[0]), "+v"(acch[1]), "+v"(acch[2])
                       : "v"(ah[k & 1]), "v"(cat(Fc.w[2][0], Fc.w[2][1])), "v"(cat(Fc.w[3][0], Fc.w[3][1])), "v"(cat(Fc.w[4][0], Fc.w[4][1])),
                         "v"(cat(Fc.w[5][0], Fc.w[5][1])), "v"(cat(Fc.w[6][0], Fc.w[6][1])), "v"(cat(Fc.w[7][0], Fc.w[7][1])), "s"(par) : "scc");
          SB();
          if (!last) lds_await<0>(ok, ca + (k + 1) * 4, 4);
          cv = ldw(ca + (k + 2) * 4);
        }
        if (n >= 4 && n < 24) rd1(Fn, (k + 1) & 3, n - 4);
        if (n == 10) ++dcount;
        if (n == 24) ah[(k + 1) & 1] = ld128(x0 + hoff);
        if (n == 25) { fence_c(); stw(dn, dcount); }
        if (n == 28) ok = __all(cv >= 4);
        if (n == 26) { if (!(k & 1)) xs = lds0 + (xs == lds0 + L_BX0 ? L_BX1 : (xs == lds0 + L_BX1 ? L_BX2 : L_BX0)); x0 = (k & 1) ? x0 ^ 64 : xs + alo; PIN(x0); }
        if (n == 27) { x1 = (k & 1) ? x1 ^ 64 : xs + ahi; PIN(x1); }
      }
      SB();
    };
#pragma nounroll
    for (int it = 0; it < 5; ++it) {
      unit(F0, F1, 0, false); unit(F1, F0, 1, false); unit(F0, F1, 2, false); unit(F1, F0, 3, it == 4);
      ca += 16;
    }
    mma_done();
    STAMP(2); CSTAMP(15);
    relane();
    u32x2 P[2][8];
    {
      const u32 dd = lds0 + L_DONE + (lane & 3) * 4;
      const int dv = ldw(dd);
      u32x2 H[2][7];
#pragma unroll
      for (int f = 0; f < 7; ++f) { H[0][f] = pk4(acc[2][f]); H[1][f] = pk4(acc[3][f]); PIN(H[0][f]); PIN(H[1][f]); }
      lds_await<0>(__all(dv >= 20), dd, 20);
      const u32 hl = lds0 + L_HO + wave * HO_STRIDE + lane * 8, yl = lds0 + L_YX + (par * 4 + 2 * hp) * 512 + lane * 8;
#pragma unroll
      for (int f = 0; f < 7; ++f) { st64(hl + f * 512, H[0][f]); st64(hl + (7 + f) * 512, H[1][f]); }
#pragma unroll
      for (int f = 2; f < 7; ++f) { P[1][f] = pk4(acc[1][f]); st64(hl + (12 + f) * 512, P[1][f]); }
#pragma unroll
      for (int f = 0; f < 3; ++f) st64(hl + (19 + f) * 512, pk4(acch[f]));
      st64(yl, pk4(accy[0])); st64(yl + 512, pk4(accy[1]));
    }
    STAMP(5);
    waitl0(); bar();
    STAMP(6);
#pragma unroll
    for (int f = 0; f < 7; ++f) P[0][f] = pk4(acc[0][f]);
    P[1][0] = pk4(acc[1][0]); P[1][1] = pk4(acc[1][1]);
    attention(P, at);
    take_g0();
    lds_spin<1>(lds0 + L_PUB, 8);
  } else {
    const int h = wave;
    u32 bK0 = lds0 + L_AW + rB0 + h * 40 + p4 * 8, bK1 = bK0 - rB0 + rB1;
    u32 bZ0 = lds0 + L_AW + rB0 + h * 40 + 32 + (p4 == (h ^ 1) ? SEG : 0), bZ1 = bZ0 - rB0 + rB1;
    PIN(bK0); PIN(bK1); PIN(bZ0); PIN(bZ1); PIN(ca); PIN(dcount);
    f32x4 acc[8][3];
#pragma unroll
    for (int r = 0; r < 8; ++r)
#pragma unroll
      for (int f = 0; f < 3; ++f) { acc[r][f] = splat(f == 0 ? 1e-6f : (f == 1 || q4 == (h ^ 1) ? 0.f : 1e-6f)); PIN(acc[r][f]); }
    struct Fr { u32x4 a[8]; u32x2 w[3][2]; };
    u32 xs = 0, xa = lds0 + L_AX + aA0, x0 = xa;
    PIN(xa);
    auto rd1 = [&](Fr& F, int sl, int n) {
      const u32 o = sl * AW_SLOT;
      if (n >= 6) { F.a[n - 6] = ld128(x0 + (n - 6) * 2048); return; }
      const int f = n >> 1, t = n & 1;
      F.w[f][t] = f < 2 ? ldtr((t ? bK1 : bK0) + o + f * SEG) : ldtr((t ? bZ1 : bZ0) + o);
    };
    Fr F0, F1;
    waitv<0>(); lds_arrive(lds0 + L_CNT, lane);
    lds_spin<0>(ca, 4);
    STAMP(1); CSTAMP(14);
#pragma unroll
    for (int n = 0; n < 14; ++n) rd1(F0, 0, n);
    fence_c(); stw(dn, dcount);
    bool ok = false;
    x0 ^= 64;
    auto unit = [&](Fr& Fc, Fr& Fn, int k, bool last) {
      u32 cv;
#pragma unroll
      for (int n = 0; n < 24; ++n) {
        SB(); mmai(acc[n / 3][n % 3], Fc.a[n / 3], cat(Fc.w[n % 3][0], Fc.w[n % 3][1])); SB();
        if (n == 2 && !last) lds_await<0>(ok, ca + (k + 1) * 4, 4);
        if (n == 2) cv = ldw(ca + (k + 2) * 4);
        if (n >= 3 && n < 17) rd1(Fn, (k + 1) & 3, n - 3);
        if (n == 10) ++dcount;
        if (n == 17) { fence_c(); stw(dn, dcount); }
        if (n == 19) ok = __all(cv >= 4);
        if (n == 20) { if (!(k & 1)) xs = (xs + AX_SLOT) & (4 * AX_SLOT - 1); x0 = (k & 1) ? x0 ^ 64 : xs + xa; PIN(x0); }
      }
      SB();
    };
#pragma nounroll
    for (int it = 0; it < 5; ++it) {
      unit(F0, F1, 0, false); unit(F1, F0, 1, false); unit(F0, F1, 2, false); unit(F1, F0, 3, it == 4);
      ca += 16;
    }
    mma_done();
    STAMP(2); CSTAMP(15);
    relane(); get_tag();
    __builtin_amdgcn_s_setprio(2);
    const bool eh = q4 == (h ^ 1), gh = g == h, zc = i16 == 4 * h;
    const u32x2 ones = {0x3F803F80u, 0x3F803F80u};
    f32x4 t00 = {}, t01 = {}, t10 = {}, t11 = {};
#pragma unroll
    for (int k = 0; k < 4; ++k) {
      const u32x4 pk = cat(pk4(phi4(acc[2 * k][0])), pk4(phi4(acc[2 * k + 1][0])));
      const u32x4 pz = cat(pk4(phi4(acc[2 * k][2])), pk4(phi4(acc[2 * k + 1][2])));
      const u32x4 vv = cat(pk4(acc[2 * k][1]), pk4(acc[2 * k + 1][1]));
      const u32x2 z0 = pk4(acc[2 * k][2]), z1 = pk4(acc[2 * k + 1][2]);
      const u32x4 zb = cat(eh ? z0 : ones, eh ? z1 : ones);
      t00 = mma(pk, vv, t00); t01 = mma(pk, zb, t01); t10 = mma(pz, vv, t10); t11 = mma(pz, zb, t11);
    }
    const u32 pb = WS_KVP + (hg * 4 + h) * 16 * KVP_STRIDE, tb = lds0 + L_TAB + h * TAB_STRIDE;
    st64(tb + lane * 8, pk4(t00));
    st64(tb + (gh ? 64 + i16 : 106) * 8, pk4(t10));
    st64(tb + (eh ? 80 + p4 + 4 * g : (zc ? 96 + g : 106)) * 8, pk4(t01));
    st64(tb + (gh && eh ? 100 + p4 : (gh && zc ? 104 : 105)) * 8, pk4(t11));
    const u32 po = lane < 53 ? pb + lane * 16 : OOB;
    gst128(wsr, (lane < 56 ? pb + lane * 16 : OOB) + rt * KVP_STRIDE, ld128(tb + lane * 16));
    if (QGATE == 8) lds_arrive(lds0 + L_CNT + 21 * 4, lane);
    STAMP(3);
    publish(ws, WS_KVFLAG + (hg * 4 + h) * FSTR + rt * 8, tag, lane);
    STAMP(4);
    __builtin_amdgcn_s_setprio(0);
    take_g0();
    lds_spin<1>(lds0 + L_D2 + (lane & 3) * 4, 1);
    out_touch(h ^ 2);
    wo_issue(10 + h, 20, 4);
    await(ws, WS_KVFLAG + (hg * 4 + h) * FSTR + i16 * 8, tag);
    __builtin_amdgcn_s_setprio(2);
    STAMP(5);
    {
      u32x4 vm[16];
#pragma unroll
      for (int r = 0; r < 16; ++r) vm[r] = __builtin_amdgcn_raw_buffer_load_b128(wsr, po, r * KVP_STRIDE, 16);
      float sm[8] = {};
      u32 sel0 = 0x00003F80u, sel1 = 0x3F800000u;
      PIN(sel0); PIN(sel1);
#pragma unroll
      for (int r = 0; r < 16; ++r)
#pragma unroll
        for (int e = 0; e < 8; ++e)
          sm[e] = __builtin_amdgcn_fdot2_f32_bf16(__builtin_bit_cast(bf16x2, (u32)(vm[r][e >> 1] + 0u)), __builtin_bit_cast(bf16x2, (e & 1) ? sel1 : sel0), sm[e], false);
      const bool tail = lane == 52;
      *(LAS u32x4*)(size_t)(tb + lane * 16) = u32x4{pk2(sm[0], sm[1]), pk2(sm[2], sm[3]), tail ? 0u : pk2(sm[4], sm[5]), tail ? 0u : pk2(sm[6], sm[7])};
    }
    waitl0(); bar();
    __builtin_amdgcn_s_setprio(0);
    STAMP(6);
    wo_post(10 + h, 20, 4);
    wo_issue(20 + h, 30, 4);
    lds_spin<2>(lds0 + L_PUB, 4);
  }

  {
    const int rp = wave & 1, kh = wave >> 1;
    const int rt2 = rg >> 1, par2 = rg & 1;
    const u32 blk0 = rg * 4 + rp * 2;
    f32x4 acc[2][5];
#pragma unroll
    for (int r = 0; r < 2; ++r)
#pragma unroll
      for (int c = 0; c < 5; ++c) { acc[r][c] = splat(0.f); PIN(acc[r][c]); }
    u32x4 ofb[10][2], ofa[10][2];
    float gt[2];
    const u32 ob[2] = {WS_O + (blk0 + kh) * 40960 + kh * 10240, WS_O + (blk0 + (kh ^ 1)) * 40960 + kh * 10240};
    const u32 gb[2] = {WS_GATE + (kh * 4 * 128 + blk0 + kh) * 64, WS_GATE + (kh * 4 * 128 + blk0 + (kh ^ 1)) * 64};
    auto ldo = [&](u32 v, int r, int n) { return __builtin_amdgcn_raw_buffer_load_b128(wsr, v + (n & 3) * 1024, ob[r] + (n >> 2) * 4096, 16); };
    auto ldg = [&](u32 v, int r) { return asf(__builtin_amdgcn_raw_buffer_load_b32(wsr, v, gb[r], 16)); };
    const u32 l16 = lane * 16;
    auto load_a = [&]() {
#pragma unroll
      for (int r = 0; r < 2; ++r) gt[r] = ldg(i16 * 4, r);
#pragma unroll
      for (int n = 0; n < 10; ++n)
#pragma unroll
        for (int r = 0; r < 2; ++r) ofa[n][r] = ldo(l16, r, 20 + n);
    };
    const u32 fa = WS_AFLAG + ((rt2 * 4 + par2 * 2 + rp) * 2 + kh) * FSTR + (lane & 3) * 8;
    await(ws, WS_BFLAG + (((rt2 * 2 + par2) * 2 + rp) * 2 + kh) * FSTR + (lane & 7) * 8, tag);
    STAMP(7);
    if (!isB) wo_post(20 + wave, 30, 4);
    u64 av;
#pragma unroll
    for (int n = 0; n < 10; ++n) {
#pragma unroll
      for (int r = 0; r < 2; ++r) ofb[n][r] = ldo(l16, r, n);
      if (n == AVN) { SB(); av = __hip_atomic_load((GAS u64*)(ws + fa), RLX_AGENT); SB(); }
    }
    fence_c(); lds_arrive(lds0 + L_ISS, lane);
    bool early = false;
    u32 oe = OOB, og = OOB, so[3], xw, xr_, tw, trd;
    const u32 xb = wreg(1);
    {
      constexpr int D = 6;
      u32x4 w[D + 1];
      u32 am = 0;
      sfor<-D, 100>([&](auto T) {
        constexpr int t = T.v, n = t + D, gr = n / 25;
        if constexpr (n == 0) STAMP(8);
        if constexpr (n < 100 && n > 0 && n % 25 == 0) {
          if constexpr (n == 50) { fence_c(); stw(lds0 + L_D2 + wave * 4, 5); }
          lds_spin<1>(n == 75 && lane < 4 ? lds0 + L_D2 + lane * 4 : lds0 + L_WC + (2 * gr + kh) * 4, 5);
          am = wreg(gr) + kh * CHUNK + lane * 16; PIN(am);
        }
        if constexpr (t == 4) { early = __all(av == tag); fence_c(); oe = early ? l16 : OOB; og = early ? i16 * 4 : OOB; }
        if constexpr (t == 4 || t == 5) gt[t & 1] = ldg(og, t & 1);
        if constexpr (t >= 6 && t < 46 && !(t & 1)) ofa[(t - 6) >> 2][(t >> 1 & 1) ^ 1] = ldo(oe, (t >> 1 & 1) ^ 1, 20 + ((t - 6) >> 2));
        if constexpr (t >= 80 && t < 83) { const u32 id = (t - 80) * 64 + lane; so[t - 80] = id * 16 + mul24(mul24(id, 6554u) >> 16, 2400u) + (t == 82 ? (lane & 32) << 26 : 0); PIN(so[t - 80]); }
        if constexpr (t == 84) { xw = xb + wave * 5120 + l16; xr_ = xb + (wave ^ 2) * 5120 + l16; PIN(xw); PIN(xr_); }
        if constexpr (t == 85) { trd = xb + 20480 + wave * 2560 + l16; tw = xb + 20480 + wave * 2560 + i16 * 160 + g * 8; PIN(trd); PIN(tw); }
        if constexpr (t == 50) {
          STAMP(9);
          if (!early) { await(ws, fa, tag); load_a(); }
          STAMP(10);
          mma_done();
#pragma unroll
          for (int r = 0; r < 2; ++r)
#pragma unroll
            for (int cc = 0; cc < 5; ++cc) { acc[r][cc] *= 1.f - gt[r]; PIN(acc[r][cc]); }
          asm volatile("s_nop 1");
        }
        SB();
        if constexpr (t >= 0) mmai(acc[0][t % 5], t < 25 ? g0[t % 25] : w[t % (D + 1)], (t < 50 ? ofb : ofa)[t / 5 % 10][0]);
        SB();
        if constexpr (n >= 25 && n < 100) w[n % (D + 1)] = ld128(am + (n % 25) * 1024);
        SB();
        if constexpr (t >= 0) mmai(acc[1][t % 5], t < 25 ? g0[t % 25] : w[t % (D + 1)], (t < 50 ? ofb : ofa)[t / 5 % 10][1]);
        SB();
      });
      mma_done();
    }
    STAMP(11);
#pragma unroll
    for (int c = 0; c < 5; ++c) *(LAS f32x4*)(size_t)(xw + c * 1024) = acc[1][c];
    fence_c(); stw(lds0 + L_D2 + wave * 4, 6);
    const u32 blk = blk0 + kh;
    const rsrc_t outr = __builtin_amdgcn_make_buffer_rsrc(out, 0, 2048 * 1280, 0x00020000);
    u32 obase = ((blk >> 3) * 128 + (blk & 3) * 32 + ((blk >> 2) & 1)) * 1280 + ctile * 160;
    asm volatile("" : "+s"(obase));
    lds_spin<0>(lds0 + L_D2 + (wave ^ 2) * 4, 6);
    f32x4 pv[5];
#pragma unroll
    for (int c = 0; c < 5; ++c) pv[c] = *(LAS f32x4*)(size_t)(xr_ + c * 1024);
    SB();
#pragma unroll
    for (int c = 0; c < 5; ++c) st64(tw + c * 32, pk4(acc[0][c] + pv[c]));
#pragma unroll
    for (int i = 0; i < 3; ++i) __builtin_amdgcn_raw_buffer_store_b128(ld128(trd + i * 1024), outr, so[i], obase, OUT_AUX);
    STAMP(12);
    STAMP(13); STAMP_OUT;
  }
}

extern "C" void kernel_launch(void* const* d_in, const int* in_sizes, int n_in, void* d_out, int out_size, void* d_ws, size_t ws_size,
                              hipStream_t stream) {
  (void)hipFuncSetAttribute((const void*)fused_kernel, hipFuncAttributeMaxDynamicSharedMemorySize, LDS_BYTES);
  fused_kernel<<<256, 512, LDS_BYTES, stream>>>((const char*)d_in[0], (const char*)d_in[1], (const char*)d_in[2], (const char*)d_in[4],
                                                (const char*)d_in[5], (const char*)d_in[6], (char*)d_out, (char*)d_ws);
}
```

```cpp
#define GLATE
#include <hip/hip_runtime.h>
#include <hip/hip_bf16.h>

typedef unsigned long long u64;
typedef unsigned u32;
typedef u32 u32x2 __attribute__((ext_vector_type(2)));
typedef u32 u32x4 __attribute__((ext_vector_type(4)));
typedef float f32x2 __attribute__((ext_vector_type(2)));
typedef float f32x4 __attribute__((ext_vector_type(4)));
typedef short s16x4 __attribute__((ext_vector_type(4)));
typedef __bf16 bf16x2 __attribute__((ext_vector_type(2)));
typedef __bf16 bf16x8 __attribute__((ext_vector_type(8)));
#define LAS __attribute__((address_space(3)))
#define GAS __attribute__((address_space(1)))
#define DEV __device__ __forceinline__
#define SB() __builtin_amdgcn_sched_barrier(0)
#define PIN(x) asm volatile("" : "+v"(x))
#define RLX_AGENT __ATOMIC_RELAXED, __HIP_MEMORY_SCOPE_AGENT

#define STAMP_DECL
#define CSTAMP(i)
#define STAMP(i)
#define STAMP_OUT
#define ROT 5
#define TAILG 18
#define A_G0W 10
#define PACK_AT 0
#define A_PW 17
#define TOKS 0
#define AVN 4
#define QGATE 8
#define OUT_AUX 16
#define FB 24
#define FA 18
#define ST_SLEEP 0
#define PACK_TOK 3
#define PACK_R 0
#define QD 6
#define QPRIO 3
#define NOPHI 0
#define OT 1
#define B_R1 19
#define POLL_SLEEP 2

constexpr u32 WS_EPOCH = 0;
#define FSTR 128
constexpr u32 WS_KVFLAG = 4096;
constexpr u32 WS_BFLAG = WS_KVFLAG + 32 * FSTR;
constexpr u32 WS_AFLAG = WS_BFLAG + 128 * FSTR;
constexpr u32 WS_PFLAG = WS_AFLAG + 128 * FSTR;
constexpr u32 WS_GATE = WS_PFLAG + 4096;
constexpr u32 WS_KVP = WS_GATE + 8 * 128 * 64;
constexpr u32 KVP_STRIDE = 896;
constexpr u32 WS_O = WS_KVP + 512 * KVP_STRIDE;
constexpr u32 WS_WOP = WS_O + 128 * 40960;
constexpr u32 WS_WQP = WS_WOP + 1600 * 1024;
constexpr u32 WS_END = WS_WQP + 800 * 1024;
constexpr u64 TAG_STEP = 0x9E3779B97F4A7C15ull;
constexpr u32 SPIN_CAP = 1u << 18;
constexpr u32 OOB = 0x80000000u;

constexpr u32 SEG = 5136;
constexpr u32 BX_SLOT = 18432, BW_SLOT = 15424, L_BX1 = 36864, L_BX2 = L_BX1 + BX_SLOT, L_BW = L_BX2 + BX_SLOT, L_BX0 = L_BW + 4 * BW_SLOT;
constexpr u32 AX_SLOT = 16384, AW_SLOT = 2 * SEG, L_AX = 47104, L_AW = L_AX + 4 * AX_SLOT;
constexpr u32 L_MISC = L_BX0 + BX_SLOT;
constexpr u32 L_HO = L_BW + 2 * BW_SLOT;
constexpr u32 HO_STRIDE = 11264, L_YX = L_MISC;
constexpr u32 L_GATEW = L_MISC, L_TAB = L_MISC + 1280, TAB_STRIDE = 1024;
constexpr u32 L_CTL = L_MISC + 9216;
constexpr u32 L_DONE = L_CTL + 16;
constexpr u32 L_CNT = L_CTL + 64;
constexpr u32 L_WC = L_CTL + 192;
constexpr u32 L_D2 = L_CTL + 224;
constexpr u32 L_ISS = L_CTL + 244;
constexpr u32 L_TOK = L_CTL + 248;
constexpr u32 L_PUB = L_CTL + 240;
constexpr u32 L_SCR = L_CTL + 256;
constexpr u32 CHUNK = 25600;
constexpr u32 LDS_BYTES = 163840;
static_assert(L_BX0 % 128 == 0 && L_HO >= 4 * CHUNK && L_HO + 4 * 11264 <= L_MISC && L_MISC >= 6 * CHUNK && L_AW + 4 * AW_SLOT <= L_MISC && L_SCR + 256 <= LDS_BYTES, "lds map");

template <int V> struct IC { static constexpr int v = V; };
template <int I, int N, class F> DEV void sfor(F&& f) { if constexpr (I < N) { f(IC<I>{}); sfor<I + 1, N>(f); } }
template <int N> DEV void waitv() { asm volatile("s_waitcnt vmcnt(%0)" ::"i"(N) : "memory"); }
DEV void waitl0() { asm volatile("s_waitcnt lgkmcnt(0)" ::: "memory"); }
DEV void bar() { asm volatile("s_barrier" ::: "memory"); }
DEV void fence_c() { asm volatile("" ::: "memory"); }
DEV void glds(const char* sbase, u32 voff, u32 lds) {
  asm volatile("s_mov_b32 m0, %2\n\ts_nop 0\n\tglobal_load_lds_dwordx4 %0, %1" ::"v"(voff), "s"(sbase), "s"(lds));
}
DEV void touch(const char* sbase, u32 voff, u32 lds) {
  asm volatile("s_mov_b32 m0, %2\n\ts_nop 0\n\tglobal_load_lds_dword %0, %1 sc1" ::"v"(voff), "s"(sbase), "s"(lds));
}
DEV void glds4(const char* sbase, u32 v0, u32 v1, u32 v2, u32 v3, u32 lds) {
  asm volatile("s_mov_b32 m0, %5\n\ts_nop 0\n\tglobal_load_lds_dwordx4 %0, %4\n\tglobal_load_lds_dwordx4 %1, %4 offset:1024\n\t"
               "global_load_lds_dwordx4 %2, %4 offset:2048\n\tglobal_load_lds_dwordx4 %3, %4 offset:3072"
               ::"v"(v0), "v"(v1), "v"(v2), "v"(v3), "s"(sbase), "s"(lds));
}
DEV void glds5p(const char* sbase, u32 v, u32 v4, u32 lds) {
  asm volatile("s_mov_b32 m0, %3\n\ts_nop 0\n\tglobal_load_lds_dwordx4 %0, %2 sc1\n\tglobal_load_lds_dwordx4 %0, %2 offset:1024 sc1\n\t"
               "global_load_lds_dwordx4 %0, %2 offset:2048 sc1\n\tglobal_load_lds_dwordx4 %0, %2 offset:3072 sc1\n\t"
               "s_add_u32 m0, %3, 4096\n\ts_nop 0\n\tglobal_load_lds_dwordx4 %1, %2 sc1" ::"v"(v), "v"(v4), "s"(sbase), "s"(lds) : "scc");
}
constexpr int wpos(int p) { return (p / 5) * (int)SEG + (p % 5) * 1024; }
template <int P> DEV void gldsw(const char* sbase, const u32 (&v)[15], u32 lds) {
  if constexpr (P < 12)
    asm volatile("s_add_u32 m0, %5, %6\n\ts_nop 0\n\tglobal_load_lds_dwordx4 %0, %4\n\tglobal_load_lds_dwordx4 %1, %4 offset:%7\n\t"
                 "global_load_lds_dwordx4 %2, %4 offset:%8\n\tglobal_load_lds_dwordx4 %3, %4 offset:%9"
                 ::"v"(v[P]), "v"(v[P + 1]), "v"(v[P + 2]), "v"(v[P + 3]), "s"(sbase), "s"(lds), "i"(wpos(P)), "i"(wpos(P + 1) - wpos(P)),
                   "i"(wpos(P + 2) - wpos(P)), "i"(wpos(P + 3) - wpos(P)) : "scc");
  else
    asm volatile("s_add_u32 m0, %4, %5\n\ts_nop 0\n\tglobal_load_lds_dwordx4 %0, %3\n\tglobal_load_lds_dwordx4 %1, %3 offset:%6\n\t"
                 "global_load_lds_dwordx4 %2, %3 offset:%7"
                 ::"v"(v[P]), "v"(v[P + 1]), "v"(v[P + 2]), "s"(sbase), "s"(lds), "i"(wpos(P)), "i"(wpos(P + 1) - wpos(P)),
                   "i"(wpos(P + 2) - wpos(P)) : "scc");
}
DEV u32x4 ld128(u32 a) { return *(LAS u32x4*)(size_t)a; }
DEV u32x2 ld64(u32 a) { return *(LAS u32x2*)(size_t)a; }
DEV void st64(u32 a, u32x2 v) { *(LAS u32x2*)(size_t)a = v; }
DEV u32x2 ldtr(u32 a) { return __builtin_bit_cast(u32x2, __builtin_amdgcn_ds_read_tr16_b64_v4i16((LAS s16x4*)(size_t)a)); }
DEV u32x4 cat(u32x2 a, u32x2 b) { return __builtin_shufflevector(a, b, 0, 1, 2, 3); }
DEV u32x4 ldB(u32 a0, u32 a1) { return cat(ldtr(a0), ldtr(a1)); }
DEV f32x4 mma(u32x4 a, u32x4 b, f32x4 c) {
  return __builtin_amdgcn_mfma_f32_16x16x32_bf16(__builtin_bit_cast(bf16x8, a), __builtin_bit_cast(bf16x8, b), c, 0, 0, 0);
}
DEV f32x4 mma16(u32x2 a, u32x2 b, f32x4 c) {
  return __builtin_amdgcn_mfma_f32_16x16x16bf16_1k(__builtin_bit_cast(s16x4, a), __builtin_bit_cast(s16x4, b), c, 0, 0, 0);
}
DEV void mmai(f32x4& c, const u32x4& a, const u32x4& b) { asm volatile("v_mfma_f32_16x16x32_bf16 %0, %1, %2, %0" : "+v"(c) : "v"(a), "v"(b)); }
DEV void mma_done() { asm volatile("s_nop 7\n\ts_nop 3"); SB(); }
DEV u32 pk2(float a, float b) { f32x2 v = {a, b}; return __builtin_bit_cast(u32, __builtin_convertvector(v, bf16x2)); }
DEV u32x2 pk4(f32x4 v) { return u32x2{pk2(v[0], v[1]), pk2(v[2], v[3])}; }
DEV f32x4 splat(float v) { return f32x4{v, v, v, v}; }
DEV float fmx(float a, float b) { return __builtin_elementwise_maximum(a, b); }
DEV float phi1(float u) {
  float e = __builtin_amdgcn_fmed3f(__builtin_amdgcn_exp2f(__builtin_fmaf(u, 1.44269504089f, -1.44269504089e-6f)), 0.f, 1.f);
  return fmx(u, 1e-6f) + e;
}
DEV float phi1b(float u, float one) {
  float e = __builtin_amdgcn_fmed3f(__builtin_amdgcn_exp2f(__builtin_fmaf(u, 1.44269504089f, -1.44269504089e-6f)), 0.f, 1.f);
  return __builtin_fmaf(e, one, fmx(u, 1e-6f));
}
DEV f32x4 phi4(f32x4 v) {
  float one = 1.f; asm("" : "+s"(one));
  return f32x4{phi1(v[0]), phi1b(v[1], one), phi1(v[2]), phi1b(v[3], one)};
}
DEV f32x4 exp4(f32x4 u) {
  const f32x4 a = u * 1.44269504089f - 1.44269504089e-6f;
  f32x4 e;
#pragma unroll
  for (int j = 0; j < 4; ++j) e[j] = __builtin_amdgcn_fmed3f(__builtin_amdgcn_exp2f(a[j]), 0.f, 1.f);
  return e;
}
typedef short s16x8 __attribute__((ext_vector_type(8)));
DEV u32x4 relu8(u32x4 v) {
  return __builtin_bit_cast(u32x4, __builtin_elementwise_max(__builtin_bit_cast(s16x8, v), (s16x8)(short)0x3586));
}
DEV float asf(u32 u) { return __builtin_bit_cast(float, u); }
DEV u32 asu(float f) { return __builtin_bit_cast(u32, f); }
DEV void xmax4(float& a, float& b, float& c, float& d, float scale) {
  auto r = __builtin_amdgcn_permlane16_swap(asu(a), asu(b), false, false);
  auto t = __builtin_amdgcn_permlane16_swap(asu(c), asu(d), false, false);
  auto w = __builtin_amdgcn_permlane32_swap(asu(fmx(asf(r[0]), asf(r[1]))), asu(fmx(asf(t[0]), asf(t[1]))), false, false);
  const u32 z = asu(fmx(asf(w[0]), asf(w[1])) * scale);
  auto x = __builtin_amdgcn_permlane32_swap(z, z, false, false);
  auto y0 = __builtin_amdgcn_permlane16_swap(x[0], x[0], false, false);
  auto y1 = __builtin_amdgcn_permlane16_swap(x[1], x[1], false, false);
  a = asf(y0[0]); b = asf(y0[1]); c = asf(y1[0]); d = asf(y1[1]);
}
DEV u32x4 pair_rows(u32x2 v) {
  auto a = __builtin_amdgcn_permlane16_swap(v[0], v[0], false, false);
  auto b = __builtin_amdgcn_permlane16_swap(v[1], v[1], false, false);
  return u32x4{a[0], b[0], a[1], b[1]};
}
DEV u32 odd_rows(u32 x) { auto r = __builtin_amdgcn_permlane16_swap(x, x, false, false); return r[1]; }

template <class F> constexpr u64 lane_mask(F f) { u64 m = 0; for (int l = 0; l < 64; ++l) if (f(l >> 4, l & 15)) m |= 1ull << l; return m; }
#define LM(expr) ({ constexpr u64 m_ = lane_mask([](int g, int i) { return (bool)(expr); }); (bool)__builtin_amdgcn_inverse_ballot_w64(m_); })
typedef __amdgpu_buffer_rsrc_t rsrc_t;
DEV u32x4 gld128(rsrc_t r, u32 off) { return __builtin_amdgcn_raw_buffer_load_b128(r, off, 0, 16); }
DEV u32 gld32(rsrc_t r, u32 off) { return __builtin_amdgcn_raw_buffer_load_b32(r, off, 0, 16); }
DEV void gst128(rsrc_t r, u32 off, u32x4 v) { __builtin_amdgcn_raw_buffer_store_b128(v, r, off, 0, 16); }
DEV void gst64(rsrc_t r, u32 off, u32x2 v) { __builtin_amdgcn_raw_buffer_store_b64(v, r, off, 0, 16); }
DEV void gst32(rsrc_t r, u32 off, u32 v) { __builtin_amdgcn_raw_buffer_store_b32(v, r, off, 0, 16); }
DEV void publish(char* ws, u32 off, u64 tag, int lane) {
  waitv<0>();
  if (lane == 0) __hip_atomic_store((GAS u64*)(ws + off), tag, RLX_AGENT);
}
DEV void await(char* ws, u32 off, u64 tag) {
#pragma nounroll
  for (u32 spins = 0; spins < SPIN_CAP; ++spins) {
    u64 v = __hip_atomic_load((GAS u64*)(ws + off), RLX_AGENT);
    if (__all(v == tag)) break;
    __builtin_amdgcn_s_sleep(POLL_SLEEP);
  }
  fence_c();
}
DEV u32 ldw(u32 a) { return __hip_atomic_load((LAS u32*)(size_t)a, __ATOMIC_RELAXED, __HIP_MEMORY_SCOPE_WORKGROUP); }
DEV void stw(u32 a, u32 v) { __hip_atomic_store((LAS u32*)(size_t)a, v, __ATOMIC_RELAXED, __HIP_MEMORY_SCOPE_WORKGROUP); }
DEV void lds_arrive(u32 a, int lane, u32 n = 1) {
  if (lane == 0) __hip_atomic_fetch_add((LAS u32*)(size_t)a, n, __ATOMIC_RELAXED, __HIP_MEMORY_SCOPE_WORKGROUP);
}
template <int SLEEP> DEV void lds_spin(u32 a, int want) {
#pragma nounroll
  for (u32 spins = 0; spins < (SPIN_CAP << 4); ++spins) {
    if (__all((int)ldw(a) >= want)) break;
    if (SLEEP) __builtin_amdgcn_s_sleep(SLEEP);
  }
  fence_c();
}
template <int SLEEP> DEV void lds_await(bool ok, u32 a, int want) {
  if (__builtin_expect(!ok, 0)) lds_spin<SLEEP>(a, want);
  fence_c();
}
template <int NM> DEV void ilv() {
#pragma unroll
  for (int n = 0; n < NM; ++n) { __builtin_amdgcn_sched_group_barrier(0x8, 1, 0); __builtin_amdgcn_sched_group_barrier(0x80, 1, 0); }
}

typedef u32 u32x16 __attribute__((ext_vector_type(16)));
DEV u32 mul24(u32 a, u32 b) { return __umul24(a, b); }
DEV void w_offsets(int lane, u32 (&ow)[5]) {
#pragma unroll
  for (int i = 0; i < 5; ++i) {
    const u32 c = i * 64 + lane, pos = mul24(c, 6554u) >> 16;
    ow[i] = mul24(pos ^ ((pos >> 1) & 4), 3840u) + c * 16 - mul24(pos, 160u);
  }
}

__global__ __launch_bounds__(512, 2) void fused_kernel(const char*, const char*, const char*, const char*, const char*, const char*, char*, char*) {
  extern __shared__ __attribute__((aligned(16))) char smem[];
  u32x16 ka;
  asm volatile("s_load_dwordx16 %0, %1, 0x0" : "=s"(ka) : "s"(__builtin_amdgcn_kernarg_segment_ptr()));
  STAMP_DECL
  const u32 lds0 = (u32)(size_t)(LAS char*)smem;
  const int b = blockIdx.x, xcd = b & 7, jj = b >> 3;
  const int rt = (xcd & 1) * 8 + (jj & 7), ct = (xcd >> 1) * 4 + (jj >> 3);
  const int r0t = (((jj & 7) * 5 + (jj >> 3) * 10 + (xcd >> 1) * 3 + (xcd & 1) * 2) >> 2), r0 = r0t >= 10 ? r0t - 10 : r0t;
  const bool isB = ct >= 8;
  const int hg = ct & 7;
  const int wave = __builtin_amdgcn_readfirstlane(threadIdx.x >> 6);
  int lane = threadIdx.x & 63, g = lane >> 4, i16 = lane & 15, q4 = (lane >> 2) & 3, p4 = lane & 3;
  auto relane = [&]() {
    asm volatile("v_mbcnt_lo_u32_b32 %0, -1, 0\n\tv_mbcnt_hi_u32_b32 %0, -1, %0" : "=v"(lane));
    g = lane >> 4; i16 = lane & 15; q4 = (lane >> 2) & 3; p4 = lane & 3;
  };
  if (wave == 7) {
    const char* pc; u32 len;
    asm volatile("s_getpc_b64 %0\n.Lpf_here:\n\ts_mov_b32 %1, .Lpf_end-.Lpf_here" : "=s"(pc), "=s"(len));
    const u32 o = (lane * 32 + jj) * 128;
    if (o < len) touch(pc, o, lds0 + L_SCR);
  }
  {
    u32 ow[5];
    u32 o0 = mul24(lane >> 3, 2560u) + (((lane & 7) ^ (lane >> 4)) << 4), o1 = (o0 ^ 64) + 19456, o2 = o0 + 2 * 19456, o3 = o1 + 2 * 19456;
    w_offsets(lane, ow);
    ow[1] -= 1024; ow[2] -= 2048; ow[3] -= 3072;
    PIN(o0); PIN(o1); PIN(o2); PIN(o3); PIN(ow[0]); PIN(ow[1]); PIN(ow[2]); PIN(ow[3]); PIN(ow[4]);
    if (wave == 4 && lane < 60) stw(lds0 + L_DONE + lane * 4, 0);
    const u32 xoff = (128 * rt + 64 * (wave & 1) + (wave >> 1)) * 1280 + r0 * 128, woff = r0 * (64 * 3840) + hg * 160 + (wave + !isB) * 1280;
    asm volatile("s_waitcnt lgkmcnt(0)" : "+s"(ka)::"memory");
    if (wave < 4) {
      const char* xp = (const char*)(((u64)ka[1] << 32) | ka[0]);
      glds4(xp + xoff, o0, o1, o2, o3, lds0 + (isB ? L_BX0 : L_AX) + wave * 4096);
      if (wave < (isB ? 3 : 2)) {
        const char* wp = (const char*)(((u64)(isB ? ka[7] : ka[3]) << 32) | (isB ? ka[6] : ka[2])) + woff;
        const u32 d = lds0 + (isB ? L_BW : L_AW) + wave * SEG;
        glds4(wp, ow[0], ow[1], ow[2], ow[3], d); glds(wp, ow[4], d + 4096);
      } else if (!isB) {
        if (wave == 2 || lane < 16) glds((const char*)(((u64)ka[11] << 32) | ka[10]) + (wave - 2) * 1024, lane * 16, lds0 + L_GATEW + (wave - 2) * 1024);
      } else {
        const char* hp_ = xp + (rt ? 128 * rt - 16 : 0) * 1280 + r0 * 128;
        glds(hp_, o0, lds0 + L_BX0 + 16384); glds(hp_ + 1280, o0 ^ 64, lds0 + L_BX0 + 17408);
      }
    }
    bar();
    if (wave == 7 && lane < 2) touch((const char*)(((u64)ka[15] << 32) | ka[14]), WS_KVFLAG + (b * 2 + lane) * 128, lds0 + L_SCR);
    if (wave == 7) {
      const int cg = (b * 205) >> 13, ks = b - cg * 40, k20 = ks >= 20 ? ks - 20 : ks, sg = (k20 * 52) >> 8, st = k20 - sg * 5;
      const int qn = b - 96, qh = (qn * 205) >> 12;
      const char* so = (const char*)(((u64)(ks < 20 ? ka[9] : ka[5]) << 32) | (ks < 20 ? ka[8] : ka[4])) + ((sg * 160 + (st < 4 ? 40 * st : 16)) * 1280 + cg * 256);
      const char* sq = (const char*)(((u64)ka[3] << 32) | ka[2]) + ((qn - qh * 20) * (32 * 3840) + ((qh * 160) & ~127));
      const u32 r1 = (st < 4 ? 4 : 40) * 1280 - 1024, vo = g * 1280 + i16 * 16, vq = g * 3840 + i16 * 16;
      if (lane < 2) touch((const char*)(((u64)ka[15] << 32) | ka[14]) + WS_EPOCH, lane * 4, lds0 + L_CTL);
      lds_spin<0>(lds0 + L_TOK, PACK_TOK);
      if (b < 200) { glds4(so, vo, vo + r1, vo + 2 * r1, vo + 3 * r1, lds0); glds4(so + 20 * 1280, vo, vo + r1, vo + 2 * r1, vo + 3 * r1, lds0 + 4096); }
      if (b >= 96) {
        glds4(sq, vq, vq + 14336, vq + 2 * 14336, vq + 3 * 14336, lds0 + 8192);
        glds4(sq + 16 * 3840, vq, vq + 14336, vq + 2 * 14336, vq + 3 * 14336, lds0 + 12288);
      }
    }
    if (wave >= 4 && wave < 7) {
      const int u = wave - 3, xs_ = (u + 1) >> 1, pr = (u + 1) & 1;
      const int kw = r0 + (u >> 1) >= 10 ? r0 + (u >> 1) - 10 : r0 + (u >> 1), kx = r0 + xs_ >= 10 ? r0 + xs_ - 10 : r0 + xs_;
      const char* wp = (const char*)(((u64)(isB ? ka[7] : ka[3]) << 32) | (isB ? ka[6] : ka[2])) + ((kw * 64 + (u & 1) * 32) * 3840 + hg * 160);
      u32 d = lds0 + (isB ? L_BW + u * BW_SLOT : L_AW + u * AW_SLOT - SEG);
      asm volatile("" :: "s"(wp), "s"(d));
      if (u > 1) lds_spin<0>(lds0 + L_TOK, u - 1);
#pragma nounroll
      for (int sg = !isB; sg < 3; ++sg) { glds4(wp + sg * 1280, ow[0], ow[1], ow[2], ow[3], d + sg * SEG); glds(wp + sg * 1280, ow[4], d + sg * SEG + 4096); }
      const char* xp = (const char*)(((u64)ka[1] << 32) | ka[0]) + ((128 * rt + pr) * 1280 + kx * 128);
      d = lds0 + (isB ? L_BX1 + (xs_ - 1) * BX_SLOT : L_AX + xs_ * AX_SLOT);
      glds4(xp, o0, o1, o2, o3, d + pr * 8192); glds4(xp + 64 * 1280, o0, o1, o2, o3, d + pr * 8192 + 4096);
      if (isB) glds(xp - (rt ? 16 * 1280 : 0), o0 ^ (pr << 6), d + 16384 + pr * 1024);
      stw(lds0 + L_TOK, u);
      waitv<0>(); stw(lds0 + L_CNT + u * 4, 4);
    }
  }
  auto argp = [&](int n) { return (char*)(((u64)ka[2 * n + 1] << 32) | ka[2 * n]); };
  const char* const X = argp(0); const char* const WA = argp(1); const char* const WoA = argp(2); const char* const WB = argp(3);
  const char* const WoB = argp(4); const char* const GW = argp(5); char* const out = argp(6); char* const ws = argp(7);
  const rsrc_t wsr = __builtin_amdgcn_make_buffer_rsrc(ws, 0, WS_END, 0x00020000);
  const u32 aA0 = i16 * 128 + ((g ^ ((i16 >> 1) & 7)) << 4);
  const u32 rB0 = (8 * g + (q4 ^ (4 * (g & 1)))) * 160, rB1 = (8 * g + ((4 + q4) ^ (4 * (g & 1)))) * 160;
  const int rg = (xcd >> 1) * 8 + (jj & 7), ctile = (xcd & 1) * 4 + (jj >> 3), shr = jj & 7;
  auto wreg = [&](int gr) -> u32 { return lds0 + (isB ? (gr == 3 ? 0 : gr) : (gr < 2 ? 2 : gr - 2)) * (2 * CHUNK); };
  auto wo_issue = [&](int s0, int s1, int st) {
#pragma nounroll
    for (int s = s0; s < s1; s += st) {
      const int gr = (s * 26) >> 8;
      glds5p(ws + WS_WOP + (ctile * 40 + s) * 5120, lane * 16, lane * 16 + 4096, wreg(gr) + (s - gr * 10) * 5120);
    }
  };
  auto out_touch = [&](int w) { if (w < 2) touch(out, ((rg >> 1) * 128 + (rg & 1) + 2 * lane) * 1280 + ctile * 160 + w * 156, lds0 + L_SCR); };
  auto wo_post = [&](int s0, int s1, int st) {
    waitv<0>();
    const int s = s0 + lane * st;
    if (s < s1) __hip_atomic_fetch_add((LAS u32*)(size_t)(lds0 + L_WC + ((s * 52) >> 8) * 4), 1u, __ATOMIC_RELAXED, __HIP_MEMORY_SCOPE_WORKGROUP);
  };
  u64 tag;

  struct Att { u32 bk, bv, by, ya, mk, mv, my, ob, fo, b0; };
  auto att_prep = [&](int par, int hp, int half) {
    Att t;
    const u32 own = lds0 + L_HO + (hp * 2 + par) * HO_STRIDE + (hp ? 7 : 12) * 512, pi = 2 * (hg & 1) + hp;
    t.ya = lds0 + L_YX + par * 2048 + hp * 64;
    t.bk = half ? own : lds0 + L_HO + hp * 2 * HO_STRIDE + 17 * 512 + par * 64;
    t.bv = half ? own : lds0 + L_HO + (hp * 2 + 1) * HO_STRIDE + 14 * 512 + par * 256;
    t.by = half ? t.ya + 512 : t.bv + 7 * 512 + hp * 64;
    t.ya += half * 1024;
    t.mk = half ? 0x3F : 0x37; t.mv = half ? 0x3F : 0x1F; t.my = half ? 0x37 : 0x17;
    t.ob = WS_O + (rt * 8 + par * 4 + half * 2) * 40960 + (hg >> 1) * 5120 + pi * 256;
    t.fo = WS_BFLAG + (((rt * 2 + par) * 2 + half) * 2 + (hg >> 2)) * FSTR + ((hg & 3) * 2 + hp) * 8 + (pi << 24);
    t.b0 = rt == 0 && !half ? 0xF149F2CAu : 0u;
    asm volatile("" : "+s"(t.bk), "+s"(t.bv), "+s"(t.by), "+s"(t.ya), "+s"(t.mk), "+s"(t.mv), "+s"(t.my), "+s"(t.ob), "+s"(t.fo), "+s"(t.b0));
    return t;
  };
  auto attention = [&](u32x2 (&P)[2][8], const Att& t) {
    u32x2 Q[2][8], K4[2];
    {
      const u32 ya = t.ya + (lane & 0x37) * 8, ak = t.bk + (lane & t.mk) * 8, av = t.bv + (lane & t.mv) * 8;
      Q[0][2] = ld64(ak + 1024); Q[0][3] = ld64(ak + 1536); Q[0][4] = ld64(t.bk + ((lane | 16) & t.mk) * 8 + 2048);
      P[0][7] = ld64(ya); P[1][7] = ld64(ya + 512);
      Q[0][5] = ld64(av + 2560); Q[0][6] = ld64(av + 3072); Q[0][7] = ld64(t.by + (lane & t.my) * 8);
    }
    const u32x2 tg = ld64(lds0 + L_CTL);
    tag = __builtin_bit_cast(u64, tg) + TAG_STEP;
#pragma unroll
    for (int r = 0; r < 2; ++r) K4[r] = u32x2{odd_rows(P[r][4][0]), odd_rows(P[r][4][1])};
#pragma unroll
    for (int f = 2; f < 8; ++f) Q[1][f] = f == 4 ? K4[0] : P[0][f];
    const bool pv[4] = {LM(4 * g >= i + 9), LM(4 * g + 1 >= i + 9), LM(4 * g + 2 >= i + 9), LM(4 * g + 3 >= i + 9)};
    const f32x4 bc = {LM(4 * g <= i && 4 * g >= i - 7) ? 0.f : -1e30f, LM(4 * g + 1 <= i && 4 * g + 1 >= i - 7) ? 0.f : -1e30f,
                      LM(4 * g + 2 <= i && 4 * g + 2 >= i - 7) ? 0.f : -1e30f, LM(4 * g + 3 <= i && 4 * g + 3 >= i - 7) ? 0.f : -1e30f};
    const u32x2 mp = {pv[0] ? 0xFFFFFFFFu : (pv[1] ? 0xFFFF0000u : 0u), pv[2] ? 0xFFFFFFFFu : (pv[3] ? 0xFFFF0000u : 0u)};
    const bool godd = LM(g & 1), glow = LM(g < 2), gq[2] = {LM(g == 0), LM(g == 2)};
    const float cs = 0.22360679775f * 1.44269504089f, b0 = asf(t.b0);
    const u32x2 zero2 = {0u, 0u};
    const f32x4 z4 = splat(0.f);
    const u32x2 ones2 = {0x3F803F80u, 0x3F803F80u};
    f32x4 m[2][2];
    float mx[2][2];
#pragma unroll
    for (int r = 1; r >= 0; --r) {
#pragma unroll
      for (int h = 0; h < 2; ++h) {
        const u32x2 q4m = gq[h] ? P[r][4] : zero2;
        const f32x4 sc = mma16(K4[r], q4m, mma16(P[r][2 + h], P[r][h], bc));
        const f32x4 sp = mma16(Q[r][4], q4m, mma16(Q[r][2 + h], P[r][h], r ? z4 : splat(b0)));
#pragma unroll
        for (int j = 0; j < 4; ++j) m[r][h][j] = pv[j] ? sp[j] : sc[j];
        mx[r][h] = fmx(fmx(m[r][h][0], m[r][h][1]), fmx(m[r][h][2], m[r][h][3]));
      }
    }
    xmax4(mx[1][0], mx[1][1], mx[0][0], mx[0][1], -cs);
    u32x2 y[2];
#pragma unroll
    for (int r = 1; r >= 0; --r) {
      float rs[2];
      f32x4 o[2], yy[2];
#pragma unroll
      for (int h = 0; h < 2; ++h) {
        f32x4 e;
#pragma unroll
        for (int j = 0; j < 4; ++j) e[j] = __builtin_amdgcn_exp2f(__builtin_fmaf(m[r][h][j], cs, mx[r][h]));
        const u32x2 pp = pk4(e);
        const u32x2 p0 = pp & mp, p1 = pp & ~mp;
        rs[h] = __builtin_amdgcn_rcpf(mma16(ones2, pp, z4)[0]);
        o[h] = mma16(Q[r][5 + h], p0, mma16(P[r][5 + h], p1, z4)) * rs[h];
        yy[h] = mma16(Q[r][7], p0, mma16(P[r][7], p1, z4));
      }
      __builtin_amdgcn_raw_buffer_store_b128(cat(pk4(o[0]), pk4(o[1])), wsr, lane * 16, t.ob + (t.fo >> 24) * 768 + r * 40960, 16);
      y[r] = pk4((godd ? yy[1] : yy[0]) * (godd ? rs[1] : rs[0]));
    }
    __builtin_amdgcn_raw_buffer_store_b128(pair_rows(glow ? y[0] : y[1]), wsr, (godd ? OOB : (glow ? 0 : 40960)) + (lane & 15) * 16, t.ob + 4096, 16);
    STAMP(3);
    publish(ws, t.fo & 0xFFFFFF, tag, lane);
    fence_c(); lds_arrive(lds0 + L_PUB, lane);
    STAMP(4);
  };

  if (wave >= 4) {
    const int lw = wave - 4;
    __builtin_amdgcn_s_setprio(3);
    const int xpar = lw & 1;
    const u32 xo = isB && rt ? 16 * 1280 : 0;
    const char* const X0 = X + rt * (128 * 1280) - xo;
    u32 vx[9], vw[15];
    int xst = (lw >> 1) + 1;
    auto rot = [&](int st) { const int k = st + r0; return k >= 10 ? k - 10 : k; };
    auto issue_xp = [&](u32 slot, bool halo, int st, int pr) {
      const char* p = X0 + (rot(st) * 128 + pr * 1280);
      glds4(p, vx[0], vx[1], vx[2], vx[3], slot + pr * 8192); glds4(p, vx[4], vx[5], vx[6], vx[7], slot + pr * 8192 + 4096);
      if (halo) glds(p, vx[8] ^ (pr << 6), slot + 16384 + pr * 1024);
    };
    auto issue_x = [&](u32 slot, bool halo) { issue_xp(slot, halo, xst, xpar); };
    auto setup = [&](bool forB) {
      relane();
      const u32 ox0 = (lane >> 3) * 2560 + (((lane & 7) ^ (lane >> 4)) << 4);
      vx[0] = ox0 + xo; vx[1] = (ox0 ^ 64) + xo + 20480 - 1024; vx[2] = vx[0] + 2 * (20480 - 1024); vx[3] = vx[1] + 2 * (20480 - 1024);
#pragma unroll
      for (int p = 0; p < 4; ++p) vx[4 + p] = vx[p] + 64 * 1280;
      vx[8] = ox0;
      u32 ow[5];
      w_offsets(lane, ow);
#pragma unroll
      for (int p = 0; p < 15; ++p) { if (forB || p < 10) { vw[p] = ow[p % 5] + (p / 5) * 1280 - (wpos(p) - wpos(p & ~3)); PIN(vw[p]); } }
    };
    const char* Wt = (isB ? WB : WA) + hg * 160;
    auto wsrc = [&](int u) { return Wt + (rot(u >> 1) * 64 + (u & 1) * 32) * 3840; };
    const u32 cw = lds0 + L_CNT + (lw + 1) * 4;
    const bool qjob = lw == 2, pjob = qjob ? b >= 96 : (lw == 3 && b < 200);
    auto pack_store = [&]() {
      int jn = b;
      asm volatile("" : "+s"(jn));
      u32x4 fr[8];
      if (qjob) {
        lds_spin<1>(lds0 + L_CNT + 16, 4);
        jn -= 96;
        const u32 a = lds0 + 8192 + (8 * g + q4) * 256 + ((((jn * 205) >> 12) * 160) & 127), a0 = a + p4 * 8, a4 = a + 32 + p4 * 40;
#pragma unroll
        for (int f = 0; f < 5; ++f) fr[f] = f < 4 ? ldB(a0 + f * 40, a0 + f * 40 + 1024) : ldB(a4, a4 + 1024);
#pragma unroll
        for (int f = 0; f < 5; ++f) __builtin_amdgcn_raw_buffer_store_b128(fr[f], wsr, lane * 16 + (f & 3) * 1024, WS_WQP + jn * 5120 + (f >> 2) * 4096, 16);
      } else {
        const int cg = (jn * 205) >> 13, ks = jn - cg * 40, k20 = ks >= 20 ? ks - 20 : ks, sg = (k20 * 52) >> 8;
        const u32 a = lds0 + (4 * g + q4) * 256 + p4 * 8;
#pragma unroll
        for (int f = 0; f < 8; ++f) fr[f] = ldB(a + f * 32, a + f * 32 + 4096);
        const u32 d = WS_WOP + (((ks < 20 ? 0 : 2) + (sg & 1)) * 10 + (sg >> 1) * 5 + k20 - sg * 5) * 5120 + cg * 8192;
#pragma unroll
        for (int f = 0; f < 8; ++f) __builtin_amdgcn_raw_buffer_store_b128(fr[f], wsr, lane * 16, d + f * 1024 + (((cg * 8 + f) * 52) >> 8) * (39 * 5120), 16);
      }
    };
    auto pack_flag = [&]() {
      const u64 t = *(LAS u64*)(size_t)(lds0 + L_CTL) + TAG_STEP;
      if (lane == 0) __hip_atomic_store((GAS u64*)(ws + WS_PFLAG + ((qjob ? 104 : 0) + b) * 8), t, RLX_AGENT);
    };
    auto first_done = [&]() {
      if (OT == 2 || (OT == 3 && !isB)) out_touch(lw);
      if (lw == 3) { waitv<0>(); stw(cw, 4); }
      if (pjob && PACK_R == 0) pack_store();
    };
    auto pack_wait = [&]() {
      const u64 t = *(LAS u64*)(size_t)(lds0 + L_CTL) + TAG_STEP;
      const u32 f0 = WS_PFLAG + (lane < 40 ? ((ctile * 5) >> 3) * 40 + lane : 200 + hg * 20 + (lane < 60 ? lane - 40 : 0)) * 8;
      const u32 f1 = WS_PFLAG + (((ctile * 5 + 4) >> 3) * 40 + (lane < 40 ? lane : 0)) * 8;
#pragma nounroll
      for (u32 spins = 0; spins < SPIN_CAP; ++spins) {
        const u64 v0 = __hip_atomic_load((GAS u64*)(ws + f0), RLX_AGENT), v1 = __hip_atomic_load((GAS u64*)(ws + f1), RLX_AGENT);
        if (__all(v0 == t && v1 == t)) break;
        __builtin_amdgcn_s_sleep(POLL_SLEEP);
      }
      fence_c();
    };
    if (isB) {
      setup(true); STAMP(1); STAMP(2);
      const u32 wd = lds0 + L_BW + ((lw + 1) & 3) * BW_SLOT, da = lds0 + L_DONE + (lane & 3) * 4;
      u32 xs = lds0 + L_BX1 + (lw >> 1) * BX_SLOT;
      auto unit = [&](int u) {
        lds_spin<1>(da, u - 3);
        const char* wp = wsrc(u); gldsw<0>(wp, vw, wd); gldsw<4>(wp, vw, wd); gldsw<8>(wp, vw, wd); gldsw<12>(wp, vw, wd);
        if (u < 19) issue_x(xs, true);
      };
      if (lw == 3) unit(4);
      STAMP(9);
      first_done();
      STAMP(10);
#pragma nounroll
      for (int u = lw + 5; u < 20; u += 4) {
        xst += 2; xs = lds0 + (xs == lds0 + L_BX0 ? L_BX2 : (xs == lds0 + L_BX2 ? L_BX1 : L_BX0));
        unit(u);
        waitv<0>(); stw(lds0 + L_CNT + u * 4, 4);
        if (pjob && u == lw + 1 + 4 * PACK_R) pack_store();
        if (pjob && u == lw + 5 + 4 * PACK_R) pack_flag();
      }
      STAMP(11);
      if (lw == 3) { pack_wait(); stw(lds0 + L_CNT + 23 * 4, 4); }
      lds_spin<1>(lds0 + L_CNT + ((lane & 1) ? TAILG : 23) * 4, 4);
      wo_issue(3 - lw, 7, 4);
      const Att at = att_prep(lw & 1, lw >> 1, (lw >> 1) ^ 1);
      u32x4 park[25];
      lds_spin<1>(da, 18);
      wo_issue(7 + lw, 20, 4);
#pragma unroll
      for (int n = 0; n < 25; ++n)
        park[n] = __builtin_amdgcn_raw_buffer_load_b128(wsr, lane * 16 + (n & 3) * 1024, WS_WOP + (ctile * 8 + 4 + lw) * CHUNK + (n >> 2) * 4096, 16);
      wo_post(3 - lw, 7, 4); wo_post(7 + lw, 20, 4);
      out_touch(lw);
      __builtin_amdgcn_s_setprio(0);
      bar();
      u32x2 P[2][8];
      const u32 hl = lds0 + L_HO + lw * HO_STRIDE + lane * 8;
#pragma unroll
      for (int f = 0; f < 7; ++f) { P[0][f] = ld64(hl + f * 512); P[1][f] = ld64(hl + (7 + f) * 512); }
      attention(P, at);
      lds_spin<1>(lds0 + L_PUB, 8);
      lds_spin<1>(lds0 + L_D2 + (lane & 3) * 4, 1);
      {
        const u32 a = wreg(2 + (lw >> 1)) + (lw & 1) * CHUNK + lane * 16;
#pragma unroll
        for (int n = 0; n < 25; ++n) *(LAS u32x4*)(size_t)(a + n * 1024) = park[n];
        fence_c(); stw(lds0 + L_WC + (4 + lw) * 4, 5);
      }
    } else {
      setup(false);
      const u32 wd = lds0 + L_AW + ((lw + 1) & 3) * AW_SLOT, da = lds0 + L_DONE + (lane & 7) * 4;
      u32x4 xr[20][2];
      f32x4 gl[2] = {};
      u32 xa0 = lds0 + L_AX + lw * 4096 + i16 * 128 + ((g ^ ((i16 >> 1) & 7)) << 4), xa1 = xa0 ^ 64;
      PIN(xa0); PIN(xa1);
      auto take = [&](int st) {
        lds_spin<1>(lds0 + L_CNT + (st ? 2 * st - (lane & 1) : 0) * 4, 4);
        const u32 o = (st & 3) * AX_SLOT;
        xr[2 * st][0] = ld128(xa0 + o); xr[2 * st][1] = ld128(xa0 + o + 2048);
        xr[2 * st + 1][0] = ld128(xa1 + o); xr[2 * st + 1][1] = ld128(xa1 + o + 2048);
        fence_c(); stw(lds0 + L_DONE + wave * 4, st == 9 ? 30 : (st > 6 ? 19 : 2 * st + 5));
      };
      auto gate_mm = [&](int st) {
        const u32 go = lds0 + L_GATEW + rot(st) * 128 + g * 16;
        const u32x4 g0 = ld128(go), g1 = ld128(go + 64);
        gl[0] = mma(g1, xr[2 * st + 1][0], mma(g0, xr[2 * st][0], gl[0])); gl[1] = mma(g1, xr[2 * st + 1][1], mma(g0, xr[2 * st][1], gl[1]));
      };
      auto wq = [&](int n) { glds5p(ws + WS_WQP + (hg * 20 + rot(n >> 1) * 2 + (n & 1)) * 5120, lane * 16, lane * 16 + 4096, lds0 + n * 5120); };
      stw(lds0 + L_DONE + wave * 4, 3);
#pragma unroll
      for (int j = 0; j < 5; ++j) {
        const int u = 4 * j + lw + 1;
        if (j >= 1) take(2 * j - 2);
        if (j == 0 ? lw == 3 : (j < 4 || lw < 3)) {
          lds_spin<1>(da, u - 3);
          const char* wp = wsrc(u) + 1280; gldsw<0>(wp, vw, wd); gldsw<4>(wp, vw, wd);
          asm volatile("s_add_u32 m0, %3, %4\n\ts_nop 0\n\tglobal_load_lds_dwordx4 %0, %2\n\tglobal_load_lds_dwordx4 %1, %2 offset:1024"
                       ::"v"(vw[8]), "v"(vw[9]), "s"(wp), "s"(wd), "i"(wpos(8)) : "scc");
          if (u < 19) issue_x(lds0 + L_AX + (xst & 3) * AX_SLOT, false);
        }
        xst += 2;
        if (j >= 1) take(2 * j - 1);
        if (j == 4 && lw == 3) { lds_spin<1>(lds0 + L_CNT + A_PW * 4, 4); pack_wait(); }
        if (j == 0) STAMP(9);
        if (j == 0) first_done(); else { waitv<0>(); stw(cw + j * 16, 4); }
        if (j == 0) STAMP(10); if (j == 4) STAMP(11);
        if (j > 0 && j == PACK_R && pjob) pack_store();
        if (j == PACK_R + 1 && pjob) pack_flag();
      }
      take(8); take(9);
      lds_spin<1>(lds0 + L_CNT + 20 * 4, 4);
      wq(lw); wq(lw + 4);
      out_touch(lw);
      lds_spin<1>(da, 20);
      STAMP(12);
#pragma nounroll
      for (int n = 8 + lw; n < 20; n += 4) wq(n);
      wo_issue(3 - lw, 10, 4);
      __builtin_amdgcn_s_setprio(0);
      STAMP(1);
      relane();
#pragma unroll
      for (int st = 0; st < 8; ++st) gate_mm(st);
      gate_mm(8); gate_mm(9);
      float gate[2];
#pragma unroll
      for (int r = 0; r < 2; ++r) { gate[r] = __builtin_amdgcn_rcpf(1.f + __builtin_amdgcn_exp2f(gl[r][0] * -1.44269504089f)); PIN(gate[r]); }
      wo_post(3 - lw, 10, 4); lds_arrive(lds0 + L_CNT + 21 * 4, lane);
      u32x2 pq[2][5];
      {
        f32x4 qa[2][5];
        float pe[4], pm[4];
        auto phi_step = [&](auto F, auto Y) {
          constexpr int y = Y.v, r = y / 18, k = y % 18, j = k & 3;
          if constexpr (y >= 0 && y < 36) {
            if constexpr (k < 4) { pe[j] = __builtin_fmaf(qa[r][F.v][j], 1.44269504089f, -1.44269504089e-6f); PIN(pe[j]); }
            else if constexpr (k < 8) { pe[j] = __builtin_amdgcn_fmed3f(__builtin_amdgcn_exp2f(pe[j]), 0.f, 1.f); PIN(pe[j]); }
            else if constexpr (k < 12) { pm[j] = fmx(qa[r][F.v][j], 1e-6f); PIN(pm[j]); }
            else if constexpr (k < 16) { pm[j] += pe[j]; PIN(pm[j]); }
            else { u32 x = pk2(pm[2 * j], pm[2 * j + 1]); PIN(x); pq[r][F.v][j] = x; }
          }
        };
        u32 q0 = lds0 + lane * 16, q1 = q0 + 61440;
        PIN(q0); PIN(q1);
        constexpr int D = QD;
        u32x4 w[D + 1];
        __builtin_amdgcn_s_setprio(QPRIO);
        sfor<-D, 100>([&](auto T) {
          constexpr int t = T.v, n = t + D, f = t / 20, pf = f - 1;
          if constexpr (n == 0) lds_spin<1>(lds0 + L_CNT + 21 * 4, 8);
          if constexpr (n == 0) { STAMP(6); CSTAMP(14); }
          if constexpr (n % 20 == 0 && n < 100) { qa[0][n / 20] = qa[1][n / 20] = splat(1e-6f); PIN(qa[0][n / 20]); PIN(qa[1][n / 20]); }
          SB();
          if constexpr (t >= 0) mmai(qa[0][f], w[t % (D + 1)], xr[t % 20][0]);
          SB();
          if constexpr (n < 100) w[n % (D + 1)] = ld128((n % 20 >= 12 ? q1 + (n % 20 - 12) * 5120 : q0 + (n % 20) * 5120) + (n / 20) * 1024);
          if constexpr (t >= 20) phi_step(IC<pf>{}, IC<2 * (t % 20) - 4>{});
          SB();
          if constexpr (t >= 0) mmai(qa[1][f], w[t % (D + 1)], xr[t % 20][1]);
          SB();
          if constexpr (t >= 20) phi_step(IC<pf>{}, IC<2 * (t % 20) - 3>{});
        });
        mma_done();
        __builtin_amdgcn_s_setprio(1);
        STAMP(2); CSTAMP(15);
        pq[0][4] = pk4(phi4(qa[0][4])); pq[1][4] = pk4(phi4(qa[1][4])); PIN(pq[0][4]); PIN(pq[1][4]);
      }
      relane();
      u32 ta[4][4];
      {
        const u32 tb = lds0 + L_TAB, tz = tb + 105 * 8, t0 = tb + (64 + i16) * 8, t1 = tb + (80 + p4 + 4 * g) * 8, t2 = tb + (100 + p4) * 8;
#pragma unroll
        for (int h = 0; h < 4; ++h) {
          const bool gh = __builtin_amdgcn_inverse_ballot_w64(0xFFFFull << (16 * h)), eh = __builtin_amdgcn_inverse_ballot_w64(0x000F000F000F000Full << (4 * (h ^ 1)));
          ta[h][0] = gh ? t0 : tz; ta[h][1] = eh ? t1 : tz; ta[h][2] = eh && gh ? t2 : tz; ta[h][3] = gh ? tz - 8 : tz;
#pragma unroll
          for (int i = 0; i < 4; ++i) PIN(ta[h][i]);
        }
      }
      __builtin_amdgcn_s_setprio(3);
      waitl0(); bar();
      STAMP(3);
      tag = *(LAS u64*)(size_t)(lds0 + L_CTL) + TAG_STEP;
      f32x4 n5[2] = {};
      u32x2 ob[2][4];
      float sc5[2] = {0.f, 0.f};
#pragma unroll
      for (int h = 0; h < 4; ++h) {
        const u32 tb = lds0 + L_TAB + h * TAB_STRIDE;
        const u32x2 a0 = ld64(tb + lane * 8), a1 = ld64(ta[h][0] + h * TAB_STRIDE), a5 = ld64(ta[h][1] + h * TAB_STRIDE), a6 = ld64(ta[h][2] + h * TAB_STRIDE);
        const u32x2 z0 = ld64(tb + (96 + g) * 8), z1 = ld64(ta[h][3] + h * TAB_STRIDE);
#pragma unroll
        for (int r = 0; r < 2; ++r) {
          const f32x4 den = mma16(z1, pq[r][4], mma16(z0, pq[r][h], splat(1e-6f))), num = mma16(a1, pq[r][4], mma16(a0, pq[r][h], splat(0.f)));
          n5[r] = mma16(a6, pq[r][4], mma16(a5, pq[r][h], n5[r]));
          const float sc = gate[r] * __builtin_amdgcn_rcpf(den[0]);
          ob[r][h] = pk4(num * sc);
          sc5[r] = __builtin_amdgcn_inverse_ballot_w64(0xFFFFull << (16 * (h ^ 1))) ? sc : sc5[r];
        }
      }
#pragma unroll
      for (int r = 0; r < 2; ++r) {
        const u32 blk = rt * 8 + lw * 2 + r;
        const u32 base = WS_O + blk * 40960 + 20480 + (hg >> 1) * 5120;
        gst128(wsr, base + (hg & 1) * 2048 + lane * 16, cat(ob[r][0], ob[r][1]));
        gst128(wsr, base + (hg & 1) * 2048 + 1024 + lane * 16, cat(ob[r][2], ob[r][3]));
        const u32x4 o5 = pair_rows(pk4(n5[r] * sc5[r]));
        gst128(wsr, (g & 1) ? OOB : base + 4096 + ((2 * (hg & 1) + (g >> 1)) * 16 + i16) * 16, u32x4{o5[2], o5[3], o5[0], o5[1]});
      }
      gst32(wsr, lane < 32 ? WS_GATE + (hg * 128 + rt * 8 + lw * 2) * 64 + lane * 4 : OOB, asu(lane < 16 ? gate[0] : gate[1]));
      STAMP(5);
      fence_c(); lds_arrive(lds0 + L_PUB, lane);
      publish(ws, WS_AFLAG + ((rt * 4 + lw) * 2 + (hg >> 2)) * FSTR + (hg & 3) * 8, tag, lane);
      STAMP(4);
    }
    if (!isB)
    {
      lds_spin<2>(lds0 + L_ISS, 4);
      wo_issue(30 + lw, 40, 4);
      wo_post(30 + lw, 40, 4);
    }
    STAMP(8);
    if (b == 0 && lw == 0) {
      const u32 fo = ((lane >> 3) * 8 + ((lane >> 2) & 1)) * FSTR;
#pragma nounroll
      for (u32 spins = 0; spins < SPIN_CAP; ++spins) {
        bool ok = true;
#pragma unroll
        for (int r = 0; r < 2; ++r) {
          ok &= __hip_atomic_load((GAS u64*)(ws + WS_BFLAG + fo + r * 64 * FSTR + (lane & 3) * 16), RLX_AGENT) == tag;
          ok &= __hip_atomic_load((GAS u64*)(ws + WS_AFLAG + fo + r * 64 * FSTR + (lane & 3) * 8), RLX_AGENT) == tag;
        }
        if (__all(ok)) break;
        __builtin_amdgcn_s_sleep(8);
      }
      if (lane == 0) __hip_atomic_store((GAS u64*)(ws + WS_EPOCH), tag, RLX_AGENT);
    }
    STAMP(13); STAMP_OUT;
    asm volatile(".Lpf_end:");
    return;
  }

  {
  }
  auto get_tag = [&]() {
    const u32x2 t = ld64(lds0 + L_CTL);
    tag = ((u64)(u32)__builtin_amdgcn_readfirstlane(t[1]) << 32) | (u32)__builtin_amdgcn_readfirstlane(t[0]);
    tag += TAG_STEP;
  };
  u32x4 g0[25];
  auto take_g0 = [&]() {
    lds_spin<1>(lds0 + L_WC + (wave >> 1) * 4, 5);
    const u32 a = wreg(0) + (wave >> 1) * CHUNK + lane * 16;
#pragma unroll
    for (int n = 0; n < 25; ++n) g0[n] = ld128(a + n * 1024);
    fence_c(); stw(lds0 + L_D2 + wave * 4, 1);
  };
  u32 ca = lds0 + L_CNT, dcount = 1;
  u32 dn = lds0 + L_DONE + wave * 4;
  PIN(dn);
  if (isB) {
    const int par = wave & 1, hp = wave >> 1;
    u32 bM0 = lds0 + L_BW + rB0 + hp * 80 + p4 * 8, bM1 = bM0 - rB0 + rB1;
    u32 bX0 = lds0 + L_BW + rB0 + hp * 80 + 32 + (p4 & 1) * SEG + (p4 >> 1) * 40, bX1 = bX0 - rB0 + rB1;
    u32 bY0 = lds0 + L_BW + rB0 + 2 * SEG + 32 + p4 * 40, bY1 = bY0 - rB0 + rB1;
    u32 alo = aA0 + par * 8192 + hp * 4096, ahi = aA0 + par * 8192 + (hp ^ 1) * 4096;
    PIN(bM0); PIN(bM1); PIN(bX0); PIN(bX1); PIN(bY0); PIN(bY1); PIN(alo); PIN(ahi); PIN(ca); PIN(dcount);
    const Att at = att_prep(par, hp, hp);
    f32x4 acc[4][7], accy[2], acch[3];
#pragma unroll
    for (int f = 0; f < 3; ++f) { acch[f] = splat(0.f); PIN(acch[f]); }
#pragma unroll
    for (int r = 0; r < 4; ++r)
#pragma unroll
      for (int f = 0; f < 7; ++f) { acc[r][f] = splat(0.f); PIN(acc[r][f]); }
    accy[0] = accy[1] = splat(0.f); PIN(accy[0]); PIN(accy[1]);
    struct Fr { u32x4 a[4]; u32x2 w[8][2]; };
    u32 xs = lds0 + L_BX0, x0 = xs + alo, x1 = xs + ahi;
    const u32 hoff = 16384 - par * 8192 - hp * 4096;
    u32x4 ah[2];
    auto rd1 = [&](Fr& F, int sl, int n) {
      const u32 o = sl * BW_SLOT;
      if (n < 4) { F.a[n] = ld128((n < 2 ? x0 : x1) + (n & 1) * 2048); return; }
      const int f = (n - 4) >> 1, t = n & 1;
      if (f == 4) F.w[4][t] = ldtr((t ? bX1 : bX0) + o);
      else if (f == 5) F.w[7][t] = ldtr((t ? bY1 : bY0) + o);
      else { const int ff = f < 4 ? f : f - 2; F.w[f < 4 ? f : f - 1][t] = ldtr((t ? bM1 : bM0) + o + (ff >> 1) * SEG + (ff & 1) * 40); }
    };
    auto mm1 = [&](Fr& F, int n) {
      if (n >= 20 && n < 22) { mmai(accy[n - 20], F.a[n - 20], cat(F.w[7][0], F.w[7][1])); return; }
      const int m = n < 20 ? n : n - 2, f = m >> 2, r = m & 3;
      const u32x4 w = cat(F.w[f][0], F.w[f][1]);
      if (f < 5) mmai(acc[r][f], w, F.a[r]); else mmai(acc[r][f], F.a[r], w);
    };
    Fr F0, F1;
    waitv<0>(); lds_arrive(lds0 + L_CNT, lane);
    lds_spin<0>(ca, 4);
    STAMP(1); CSTAMP(14);
#pragma unroll
    for (int n = 0; n < 20; ++n) rd1(F0, 0, n);
    ah[0] = ld128(x0 + hoff);
    fence_c(); stw(dn, dcount);
    bool ok = false;
    x0 = xs + (alo ^ 64); x1 = xs + (ahi ^ 64);
    auto unit = [&](Fr& Fc, Fr& Fn, int k, bool last) {
      u32 cv;
#pragma unroll
      for (int n = 0; n < 30; ++n) {
        SB(); mm1(Fc, n); SB();
        if (n == 3) {
          asm volatile("s_cmp_lg_u32 %10, 0\n\ts_cbranch_scc1 .Lhv%=\n\t"
                       "v_mfma_f32_16x16x32_bf16 %0, %4, %3, %0\n\tv_mfma_f32_16x16x32_bf16 %1, %5, %3, %1\n\tv_mfma_f32_16x16x32_bf16 %2, %6, %3, %2\n\t"
                       "s_branch .Lhe%=\n.Lhv%=:\n\t"
                       "v_mfma_f32_16x16x32_bf16 %0, %3, %7, %0\n\tv_mfma_f32_16x16x32_bf16 %1, %3, %8, %1\n\tv_mfma_f32_16x16x32_bf16 %2, %3, %9, %2\n.Lhe%=:"
                       : "+v"(acch[0]), "+v"(acch[1]), "+v"(acch[2])
                       : "v"(ah[k & 1]), "v"(cat(Fc.w[2][0], Fc.w[2][1])), "v"(cat(Fc.w[3][0], Fc.w[3][1])), "v"(cat(Fc.w[4][0], Fc.w[4][1])),
                         "v"(cat(Fc.w[5][0], Fc.w[5][1])), "v"(cat(Fc.w[6][0], Fc.w[6][1])), "v"(cat(Fc.w[7][0], Fc.w[7][1])), "s"(par) : "scc");
          SB();
          if (!last) lds_await<0>(ok, ca + (k + 1) * 4, 4);
          cv = ldw(ca + (k + 2) * 4);
        }
        if (n >= 4 && n < 24) rd1(Fn, (k + 1) & 3, n - 4);
        if (n == 10) ++dcount;
        if (n == 24) ah[(k + 1) & 1] = ld128(x0 + hoff);
        if (n == 25) { fence_c(); stw(dn, dcount); }
        if (n == 28) ok = __all(cv >= 4);
        if (n == 26) { if (!(k & 1)) xs = lds0 + (xs == lds0 + L_BX0 ? L_BX1 : (xs == lds0 + L_BX1 ? L_BX2 : L_BX0)); x0 = (k & 1) ? x0 ^ 64 : xs + alo; PIN(x0); }
        if (n == 27) { x1 = (k & 1) ? x1 ^ 64 : xs + ahi; PIN(x1); }
      }
      SB();
    };
#pragma nounroll
    for (int it = 0; it < 5; ++it) {
      unit(F0, F1, 0, false); unit(F1, F0, 1, false); unit(F0, F1, 2, false); unit(F1, F0, 3, it == 4);
      ca += 16;
    }
    mma_done();
    STAMP(2); CSTAMP(15);
    relane();
    u32x2 P[2][8];
    {
      const u32 dd = lds0 + L_DONE + (lane & 3) * 4;
      const int dv = ldw(dd);
      u32x2 H[2][7];
#pragma unroll
      for (int f = 0; f < 7; ++f) { H[0][f] = pk4(acc[2][f]); H[1][f] = pk4(acc[3][f]); PIN(H[0][f]); PIN(H[1][f]); }
      lds_await<0>(__all(dv >= 20), dd, 20);
      const u32 hl = lds0 + L_HO + wave * HO_STRIDE + lane * 8, yl = lds0 + L_YX + (par * 4 + 2 * hp) * 512 + lane * 8;
#pragma unroll
      for (int f = 0; f < 7; ++f) { st64(hl + f * 512, H[0][f]); st64(hl + (7 + f) * 512, H[1][f]); }
#pragma unroll
      for (int f = 2; f < 7; ++f) { P[1][f] = pk4(acc[1][f]); st64(hl + (12 + f) * 512, P[1][f]); }
#pragma unroll
      for (int f = 0; f < 3; ++f) st64(hl + (19 + f) * 512, pk4(acch[f]));
      st64(yl, pk4(accy[0])); st64(yl + 512, pk4(accy[1]));
    }
    STAMP(5);
    waitl0(); bar();
    STAMP(6);
#pragma unroll
    for (int f = 0; f < 7; ++f) P[0][f] = pk4(acc[0][f]);
    P[1][0] = pk4(acc[1][0]); P[1][1] = pk4(acc[1][1]);
    attention(P, at);
    take_g0();
    lds_spin<1>(lds0 + L_PUB, 8);
  } else {
    const int h = wave;
    u32 bK0 = lds0 + L_AW + rB0 + h * 40 + p4 * 8, bK1 = bK0 - rB0 + rB1;
    u32 bZ0 = lds0 + L_AW + rB0 + h * 40 + 32 + (p4 == (h ^ 1) ? SEG : 0), bZ1 = bZ0 - rB0 + rB1;
    PIN(bK0); PIN(bK1); PIN(bZ0); PIN(bZ1); PIN(ca); PIN(dcount);
    f32x4 acc[8][3];
#pragma unroll
    for (int r = 0; r < 8; ++r)
#pragma unroll
      for (int f = 0; f < 3; ++f) { acc[r][f] = splat(f == 0 ? 1e-6f : (f == 1 || q4 == (h ^ 1) ? 0.f : 1e-6f)); PIN(acc[r][f]); }
    struct Fr { u32x4 a[8]; u32x2 w[3][2]; };
    u32 xs = 0, xa = lds0 + L_AX + aA0, x0 = xa;
    PIN(xa);
    auto rd1 = [&](Fr& F, int sl, int n) {
      const u32 o = sl * AW_SLOT;
      if (n >= 6) { F.a[n - 6] = ld128(x0 + (n - 6) * 2048); return; }
      const int f = n >> 1, t = n & 1;
      F.w[f][t] = f < 2 ? ldtr((t ? bK1 : bK0) + o + f * SEG) : ldtr((t ? bZ1 : bZ0) + o);
    };
    Fr F0, F1;
    waitv<0>(); lds_arrive(lds0 + L_CNT, lane);
    lds_spin<0>(ca, 4);
    STAMP(1); CSTAMP(14);
#pragma unroll
    for (int n = 0; n < 14; ++n) rd1(F0, 0, n);
    fence_c(); stw(dn, dcount);
    bool ok = false;
    x0 ^= 64;
    auto unit = [&](Fr& Fc, Fr& Fn, int k, bool last) {
      u32 cv;
#pragma unroll
      for (int n = 0; n < 24; ++n) {
        SB(); mmai(acc[n / 3][n % 3], Fc.a[n / 3], cat(Fc.w[n % 3][0], Fc.w[n % 3][1])); SB();
        if (n == 2 && !last) lds_await<0>(ok, ca + (k + 1) * 4, 4);
        if (n == 2) cv = ldw(ca + (k + 2) * 4);
        if (n >= 3 && n < 17) rd1(Fn, (k + 1) & 3, n - 3);
        if (n == 10) ++dcount;
        if (n == 17) { fence_c(); stw(dn, dcount); }
        if (n == 19) ok = __all(cv >= 4);
        if (n == 20) { if (!(k & 1)) xs = (xs + AX_SLOT) & (4 * AX_SLOT - 1); x0 = (k & 1) ? x0 ^ 64 : xs + xa; PIN(x0); }
      }
      SB();
    };
#pragma nounroll
    for (int it = 0; it < 5; ++it) {
      unit(F0, F1, 0, false); unit(F1, F0, 1, false); unit(F0, F1, 2, false); unit(F1, F0, 3, it == 4);
      ca += 16;
    }
    mma_done();
    STAMP(2); CSTAMP(15);
    relane(); get_tag();
    __builtin_amdgcn_s_setprio(2);
    const bool eh = q4 == (h ^ 1), gh = g == h, zc = i16 == 4 * h;
    const u32x4 ones = {0x3F803F80u, 0x3F803F80u, 0x3F803F80u, 0x3F803F80u};
    f32x4 t00 = {}, t01 = {}, t10 = {}, t11 = {};
#pragma unroll
    for (int k = 0; k < 4; ++k) {
      const u32x4 ek = cat(pk4(exp4(acc[2 * k][0])), pk4(exp4(acc[2 * k + 1][0]))), ez = cat(pk4(exp4(acc[2 * k][2])), pk4(exp4(acc[2 * k + 1][2])));
      const u32x4 zz = cat(pk4(acc[2 * k][2]), pk4(acc[2 * k + 1][2]));
      const u32x4 rk = relu8(cat(pk4(acc[2 * k][0]), pk4(acc[2 * k + 1][0]))), rz = relu8(zz);
      const u32x4 vv = cat(pk4(acc[2 * k][1]), pk4(acc[2 * k + 1][1])), zb = eh ? zz : ones;
      t00 = mma(ek, vv, mma(rk, vv, t00)); t01 = mma(ek, zb, mma(rk, zb, t01));
      t10 = mma(ez, vv, mma(rz, vv, t10)); t11 = mma(ez, zb, mma(rz, zb, t11));
    }
    const u32 pb = WS_KVP + (hg * 4 + h) * 16 * KVP_STRIDE, tb = lds0 + L_TAB + h * TAB_STRIDE;
    st64(tb + lane * 8, pk4(t00));
    st64(tb + (gh ? 64 + i16 : 106) * 8, pk4(t10));
    st64(tb + (eh ? 80 + p4 + 4 * g : (zc ? 96 + g : 106)) * 8, pk4(t01));
    st64(tb + (gh && eh ? 100 + p4 : (gh && zc ? 104 : 105)) * 8, pk4(t11));
    const u32 po = lane < 53 ? pb + lane * 16 : OOB;
    gst128(wsr, (lane < 56 ? pb + lane * 16 : OOB) + rt * KVP_STRIDE, ld128(tb + lane * 16));
    lds_arrive(lds0 + L_CNT + 21 * 4, lane);
    STAMP(3);
    publish(ws, WS_KVFLAG + (hg * 4 + h) * FSTR + rt * 8, tag, lane);
    STAMP(4);
    __builtin_amdgcn_s_setprio(0);
    take_g0();
    lds_spin<1>(lds0 + L_D2 + (lane & 3) * 4, 1);
    wo_issue(10 + h, 20, 4);
    await(ws, WS_KVFLAG + (hg * 4 + h) * FSTR + i16 * 8, tag);
    __builtin_amdgcn_s_setprio(2);
    STAMP(5);
    {
      u32x4 vm[16];
#pragma unroll
      for (int r = 0; r < 16; ++r) vm[r] = __builtin_amdgcn_raw_buffer_load_b128(wsr, po, r * KVP_STRIDE, 16);
      float sm[8] = {};
      u32 sel0 = 0x00003F80u, sel1 = 0x3F800000u;
      PIN(sel0); PIN(sel1);
#pragma unroll
      for (int r = 0; r < 16; ++r)
#pragma unroll
        for (int e = 0; e < 8; ++e)
          sm[e] = __builtin_amdgcn_fdot2_f32_bf16(__builtin_bit_cast(bf16x2, (u32)(vm[r][e >> 1] + 0u)), __builtin_bit_cast(bf16x2, (e & 1) ? sel1 : sel0), sm[e], false);
      const bool tail = lane == 52;
      *(LAS u32x4*)(size_t)(tb + lane * 16) = u32x4{pk2(sm[0], sm[1]), pk2(sm[2], sm[3]), tail ? 0u : pk2(sm[4], sm[5]), tail ? 0u : pk2(sm[6], sm[7])};
    }
    waitl0(); bar();
    __builtin_amdgcn_s_setprio(0);
    STAMP(6);
    wo_post(10 + h, 20, 4);
    wo_issue(20 + h, 30, 4);
    lds_spin<2>(lds0 + L_PUB, 4);
  }

  {
    const int rp = wave & 1, kh = wave >> 1;
    const int rt2 = rg >> 1, par2 = rg & 1;
    const u32 blk0 = rg * 4 + rp * 2;
    f32x4 acc[2][5];
#pragma unroll
    for (int r = 0; r < 2; ++r)
#pragma unroll
      for (int c = 0; c < 5; ++c) { acc[r][c] = splat(0.f); PIN(acc[r][c]); }
    u32x4 ofb[10][2], ofa[10][2];
    float gt[2];
    const u32 ob[2] = {WS_O + (blk0 + kh) * 40960 + kh * 10240, WS_O + (blk0 + (kh ^ 1)) * 40960 + kh * 10240};
    const u32 gb[2] = {WS_GATE + (kh * 4 * 128 + blk0 + kh) * 64, WS_GATE + (kh * 4 * 128 + blk0 + (kh ^ 1)) * 64};
    auto ldo = [&](u32 v, int r, int n) { return __builtin_amdgcn_raw_buffer_load_b128(wsr, v + (n & 3) * 1024, ob[r] + (n >> 2) * 4096, 16); };
    auto ldg = [&](u32 v, int r) { return asf(__builtin_amdgcn_raw_buffer_load_b32(wsr, v, gb[r], 16)); };
    const u32 l16 = lane * 16;
    auto load_a = [&]() {
#pragma unroll
      for (int r = 0; r < 2; ++r) gt[r] = ldg(i16 * 4, r);
#pragma unroll
      for (int n = 0; n < 10; ++n)
#pragma unroll
        for (int r = 0; r < 2; ++r) ofa[n][r] = ldo(l16, r, 20 + n);
    };
    const u32 fa = WS_AFLAG + ((rt2 * 4 + par2 * 2 + rp) * 2 + kh) * FSTR + (lane & 3) * 8;
    await(ws, WS_BFLAG + (((rt2 * 2 + par2) * 2 + rp) * 2 + kh) * FSTR + (lane & 7) * 8, tag);
    STAMP(7);
    if (!isB) wo_post(20 + wave, 30, 4);
    u64 av;
#pragma unroll
    for (int n = 0; n < 10; ++n) {
#pragma unroll
      for (int r = 0; r < 2; ++r) ofb[n][r] = ldo(l16, r, n);
      if (n == AVN) { SB(); av = __hip_atomic_load((GAS u64*)(ws + fa), RLX_AGENT); SB(); }
    }
    fence_c(); lds_arrive(lds0 + L_ISS, lane);
    bool early = false;
    u32 oe = OOB, og = OOB, so[3], xw, xr_, tw, trd;
    const u32 xb = wreg(1);
    {
      constexpr int D = 6;
      u32x4 w[D + 1];
      u32 am = 0;
      sfor<-D, 100>([&](auto T) {
        constexpr int t = T.v, n = t + D, gr = n / 25;
        if constexpr (n == 0) STAMP(8);
        if constexpr (n < 100 && n > 0 && n % 25 == 0) {
          if constexpr (n == 50) { fence_c(); stw(lds0 + L_D2 + wave * 4, 5); }
          lds_spin<1>(n == 75 && lane < 4 ? lds0 + L_D2 + lane * 4 : lds0 + L_WC + (2 * gr + kh) * 4, 5);
          am = wreg(gr) + kh * CHUNK + lane * 16; PIN(am);
        }
        if constexpr (t == 4) { early = __all(av == tag); fence_c(); oe = early ? l16 : OOB; og = early ? i16 * 4 : OOB; }
        if constexpr (t == 4 || t == 5) gt[t & 1] = ldg(og, t & 1);
        if constexpr (t >= 6 && t < 46 && !(t & 1)) ofa[(t - 6) >> 2][(t >> 1 & 1) ^ 1] = ldo(oe, (t >> 1 & 1) ^ 1, 20 + ((t - 6) >> 2));
        if constexpr (t >= 80 && t < 83) { const u32 id = (t - 80) * 64 + lane; so[t - 80] = id * 16 + mul24(mul24(id, 6554u) >> 16, 2400u) + (t == 82 ? (lane & 32) << 26 : 0); PIN(so[t - 80]); }
        if constexpr (t == 84) { xw = xb + wave * 5120 + l16; xr_ = xb + (wave ^ 2) * 5120 + l16; PIN(xw); PIN(xr_); }
        if constexpr (t == 85) { trd = xb + 20480 + wave * 2560 + l16; tw = xb + 20480 + wave * 2560 + i16 * 160 + g * 8; PIN(trd); PIN(tw); }
        if constexpr (t == 50) {
          STAMP(9);
          if (!early) { await(ws, fa, tag); load_a(); }
          STAMP(10);
          mma_done();
#pragma unroll
          for (int r = 0; r < 2; ++r)
#pragma unroll
            for (int cc = 0; cc < 5; ++cc) { acc[r][cc] *= 1.f - gt[r]; PIN(acc[r][cc]); }
          asm volatile("s_nop 1");
        }
        SB();
        if constexpr (t >= 0) mmai(acc[0][t % 5], t < 25 ? g0[t % 25] : w[t % (D + 1)], (t < 50 ? ofb : ofa)[t / 5 % 10][0]);
        SB();
        if constexpr (n >= 25 && n < 100) w[n % (D + 1)] = ld128(am + (n % 25) * 1024);
        SB();
        if constexpr (t >= 0) mmai(acc[1][t % 5], t < 25 ? g0[t % 25] : w[t % (D + 1)], (t < 50 ? ofb : ofa)[t / 5 % 10][1]);
        SB();
      });
      mma_done();
    }
    STAMP(11);
#pragma unroll
    for (int c = 0; c < 5; ++c) *(LAS f32x4*)(size_t)(xw + c * 1024) = acc[1][c];
    fence_c(); stw(lds0 + L_D2 + wave * 4, 6);
    const u32 blk = blk0 + kh;
    const rsrc_t outr = __builtin_amdgcn_make_buffer_rsrc(out, 0, 2048 * 1280, 0x00020000);
    u32 obase = ((blk >> 3) * 128 + (blk & 3) * 32 + ((blk >> 2) & 1)) * 1280 + ctile * 160;
    asm volatile("" : "+s"(obase));
    lds_spin<0>(lds0 + L_D2 + (wave ^ 2) * 4, 6);
    f32x4 pv[5];
#pragma unroll
    for (int c = 0; c < 5; ++c) pv[c] = *(LAS f32x4*)(size_t)(xr_ + c * 1024);
    SB();
#pragma unroll
    for (int c = 0; c < 5; ++c) st64(tw + c * 32, pk4(acc[0][c] + pv[c]));
#pragma unroll
    for (int i = 0; i < 3; ++i) __builtin_amdgcn_raw_buffer_store_b128(ld128(trd + i * 1024), outr, so[i], obase, OUT_AUX);
    STAMP(12);
    STAMP(13); STAMP_OUT;
  }
}

extern "C" void kernel_launch(void* const* d_in, const int* in_sizes, int n_in, void* d_out, int out_size, void* d_ws, size_t ws_size,
                              hipStream_t stream) {
  (void)hipFuncSetAttribute((const void*)fused_kernel, hipFuncAttributeMaxDynamicSharedMemorySize, LDS_BYTES);
  fused_kernel<<<256, 512, LDS_BYTES, stream>>>((const char*)d_in[0], (const char*)d_in[1], (const char*)d_in[2], (const char*)d_in[4],
                                                (const char*)d_in[5], (const char*)d_in[6], (char*)d_out, (char*)d_ws);
}
```

```cpp
#define DIAG_WG 64
#define DIAG_W 0
#define DIAG_I 2
#define GLATE
#include <hip/hip_runtime.h>
#include <hip/hip_bf16.h>

typedef unsigned long long u64;
typedef unsigned u32;
typedef u32 u32x2 __attribute__((ext_vector_type(2)));
typedef u32 u32x4 __attribute__((ext_vector_type(4)));
typedef float f32x2 __attribute__((ext_vector_type(2)));
typedef float f32x4 __attribute__((ext_vector_type(4)));
typedef short s16x4 __attribute__((ext_vector_type(4)));
typedef __bf16 bf16x2 __attribute__((ext_vector_type(2)));
typedef __bf16 bf16x8 __attribute__((ext_vector_type(8)));
#define LAS __attribute__((address_space(3)))
#define GAS __attribute__((address_space(1)))
#define DEV __device__ __forceinline__
#define SB() __builtin_amdgcn_sched_barrier(0)
#define PIN(x) asm volatile("" : "+v"(x))
#define RLX_AGENT __ATOMIC_RELAXED, __HIP_MEMORY_SCOPE_AGENT

#define STAMP_DECL u64 st0, sti = 0; asm volatile("s_memrealtime %0" : "=s"(st0));
#define CSTAMP(i)
#define STAMP(i) do { if constexpr (i == DIAG_I) { SB(); asm volatile("s_memrealtime %0\n\ts_waitcnt lgkmcnt(0)" : "=s"(sti) :: "memory"); SB(); } } while (0)
#define STAMP_OUT do { if (b == DIAG_WG && wave == DIAG_W) { const u64 until = st0 + 6000 + 4 * (sti - st0); while (__builtin_amdgcn_s_memrealtime() < until) __builtin_amdgcn_s_sleep(4); } } while (0)
#define ROT 5
#define TAILG 18
#define A_G0W 10
#define PACK_AT 0
#define A_PW 17
#define TOKS 0
#define AVN 4
#define QGATE 8
#define OUT_AUX 16
#define FB 24
#define FA 18
#define ST_SLEEP 0
#define PACK_TOK 3
#define PACK_R 0
#define QD 6
#define QPRIO 3
#define NOPHI 0
#define OT 1
#define B_R1 19
#define POLL_SLEEP 2

constexpr u32 WS_EPOCH = 0;
#define FSTR 128
constexpr u32 WS_KVFLAG = 4096;
constexpr u32 WS_BFLAG = WS_KVFLAG + 32 * FSTR;
constexpr u32 WS_AFLAG = WS_BFLAG + 128 * FSTR;
constexpr u32 WS_PFLAG = WS_AFLAG + 128 * FSTR;
constexpr u32 WS_GATE = WS_PFLAG + 4096;
constexpr u32 WS_KVP = WS_GATE + 8 * 128 * 64;
constexpr u32 KVP_STRIDE = 896;
constexpr u32 WS_O = WS_KVP + 512 * KVP_STRIDE;
constexpr u32 WS_WOP = WS_O + 128 * 40960;
constexpr u32 WS_WQP = WS_WOP + 1600 * 1024;
constexpr u32 WS_END = WS_WQP + 800 * 1024;
constexpr u64 TAG_STEP = 0x9E3779B97F4A7C15ull;
constexpr u32 SPIN_CAP = 1u << 18;
constexpr u32 OOB = 0x80000000u;

constexpr u32 SEG = 5136;
constexpr u32 BX_SLOT = 18432, BW_SLOT = 15424, L_BX1 = 36864, L_BX2 = L_BX1 + BX_SLOT, L_BW = L_BX2 + BX_SLOT, L_BX0 = L_BW + 4 * BW_SLOT;
constexpr u32 AX_SLOT = 16384, AW_SLOT = 2 * SEG, L_AX = 47104, L_AW = L_AX + 4 * AX_SLOT;
constexpr u32 L_MISC = L_BX0 + BX_SLOT;
constexpr u32 L_HO = L_BW + 2 * BW_SLOT;
constexpr u32 HO_STRIDE = 11264, L_YX = L_MISC;
constexpr u32 L_GATEW = L_MISC, L_TAB = L_MISC + 1280, TAB_STRIDE = 1024;
constexpr u32 L_CTL = L_MISC + 9216;
constexpr u32 L_DONE = L_CTL + 16;
constexpr u32 L_CNT = L_CTL + 64;
constexpr u32 L_WC = L_CTL + 192;
constexpr u32 L_D2 = L_CTL + 224;
constexpr u32 L_ISS = L_CTL + 244;
constexpr u32 L_TOK = L_CTL + 248;
constexpr u32 L_PUB = L_CTL + 240;
constexpr u32 L_SCR = L_CTL + 256;
constexpr u32 CHUNK = 25600;
constexpr u32 LDS_BYTES = 163840;
static_assert(L_BX0 % 128 == 0 && L_HO >= 4 * CHUNK && L_HO + 4 * 11264 <= L_MISC && L_MISC >= 6 * CHUNK && L_AW + 4 * AW_SLOT <= L_MISC && L_SCR + 256 <= LDS_BYTES, "lds map");

template <int V> struct IC { static constexpr int v = V; };
template <int I, int N, class F> DEV void sfor(F&& f) { if constexpr (I < N) { f(IC<I>{}); sfor<I + 1, N>(f); } }
template <int N> DEV void waitv() { asm volatile("s_waitcnt vmcnt(%0)" ::"i"(N) : "memory"); }
DEV void waitl0() { asm volatile("s_waitcnt lgkmcnt(0)" ::: "memory"); }
DEV void bar() { asm volatile("s_barrier" ::: "memory"); }
DEV void fence_c() { asm volatile("" ::: "memory"); }
DEV void glds(const char* sbase, u32 voff, u32 lds) {
  asm volatile("s_mov_b32 m0, %2\n\ts_nop 0\n\tglobal_load_lds_dwordx4 %0, %1" ::"v"(voff), "s"(sbase), "s"(lds));
}
DEV void touch(const char* sbase, u32 voff, u32 lds) {
  asm volatile("s_mov_b32 m0, %2\n\ts_nop 0\n\tglobal_load_lds_dword %0, %1 sc1" ::"v"(voff), "s"(sbase), "s"(lds));
}
DEV void glds4(const char* sbase, u32 v0, u32 v1, u32 v2, u32 v3, u32 lds) {
  asm volatile("s_mov_b32 m0, %5\n\ts_nop 0\n\tglobal_load_lds_dwordx4 %0, %4\n\tglobal_load_lds_dwordx4 %1, %4 offset:1024\n\t"
               "global_load_lds_dwordx4 %2, %4 offset:2048\n\tglobal_load_lds_dwordx4 %3, %4 offset:3072"
               ::"v"(v0), "v"(v1), "v"(v2), "v"(v3), "s"(sbase), "s"(lds));
}
DEV void glds5p(const char* sbase, u32 v, u32 v4, u32 lds) {
  asm volatile("s_mov_b32 m0, %3\n\ts_nop 0\n\tglobal_load_lds_dwordx4 %0, %2 sc1\n\tglobal_load_lds_dwordx4 %0, %2 offset:1024 sc1\n\t"
               "global_load_lds_dwordx4 %0, %2 offset:2048 sc1\n\tglobal_load_lds_dwordx4 %0, %2 offset:3072 sc1\n\t"
               "s_add_u32 m0, %3, 4096\n\ts_nop 0\n\tglobal_load_lds_dwordx4 %1, %2 sc1" ::"v"(v), "v"(v4), "s"(sbase), "s"(lds) : "scc");
}
constexpr int wpos(int p) { return (p / 5) * (int)SEG + (p % 5) * 1024; }
template <int P> DEV void gldsw(const char* sbase, const u32 (&v)[15], u32 lds) {
  if constexpr (P < 12)
    asm volatile("s_add_u32 m0, %5, %6\n\ts_nop 0\n\tglobal_load_lds_dwordx4 %0, %4\n\tglobal_load_lds_dwordx4 %1, %4 offset:%7\n\t"
                 "global_load_lds_dwordx4 %2, %4 offset:%8\n\tglobal_load_lds_dwordx4 %3, %4 offset:%9"
                 ::"v"(v[P]), "v"(v[P + 1]), "v"(v[P + 2]), "v"(v[P + 3]), "s"(sbase), "s"(lds), "i"(wpos(P)), "i"(wpos(P + 1) - wpos(P)),
                   "i"(wpos(P + 2) - wpos(P)), "i"(wpos(P + 3) - wpos(P)) : "scc");
  else
    asm volatile("s_add_u32 m0, %4, %5\n\ts_nop 0\n\tglobal_load_lds_dwordx4 %0, %3\n\tglobal_load_lds_dwordx4 %1, %3 offset:%6\n\t"
                 "global_load_lds_dwordx4 %2, %3 offset:%7"
                 ::"v"(v[P]), "v"(v[P + 1]), "v"(v[P + 2]), "s"(sbase), "s"(lds), "i"(wpos(P)), "i"(wpos(P + 1) - wpos(P)),
                   "i"(wpos(P + 2) - wpos(P)) : "scc");
}
DEV u32x4 ld128(u32 a) { return *(LAS u32x4*)(size_t)a; }
DEV u32x2 ld64(u32 a) { return *(LAS u32x2*)(size_t)a; }
DEV void st64(u32 a, u32x2 v) { *(LAS u32x2*)(size_t)a = v; }
DEV u32x2 ldtr(u32 a) { return __builtin_bit_cast(u32x2, __builtin_amdgcn_ds_read_tr16_b64_v4i16((LAS s16x4*)(size_t)a)); }
DEV u32x4 cat(u32x2 a, u32x2 b) { return __builtin_shufflevector(a, b, 0, 1, 2, 3); }
DEV u32x4 ldB(u32 a0, u32 a1) { return cat(ldtr(a0), ldtr(a1)); }
DEV f32x4 mma(u32x4 a, u32x4 b, f32x4 c) {
  return __builtin_amdgcn_mfma_f32_16x16x32_bf16(__builtin_bit_cast(bf16x8, a), __builtin_bit_cast(bf16x8, b), c, 0, 0, 0);
}
DEV f32x4 mma16(u32x2 a, u32x2 b, f32x4 c) {
  return __builtin_amdgcn_mfma_f32_16x16x16bf16_1k(__builtin_bit_cast(s16x4, a), __builtin_bit_cast(s16x4, b), c, 0, 0, 0);
}
DEV void mmai(f32x4& c, const u32x4& a, const u32x4& b) { asm volatile("v_mfma_f32_16x16x32_bf16 %0, %1, %2, %0" : "+v"(c) : "v"(a), "v"(b)); }
DEV void mma_done() { asm volatile("s_nop 7\n\ts_nop 3"); SB(); }
DEV u32 pk2(float a, float b) { f32x2 v = {a, b}; return __builtin_bit_cast(u32, __builtin_convertvector(v, bf16x2)); }
DEV u32x2 pk4(f32x4 v) { return u32x2{pk2(v[0], v[1]), pk2(v[2], v[3])}; }
DEV f32x4 splat(float v) { return f32x4{v, v, v, v}; }
DEV float fmx(float a, float b) { return __builtin_elementwise_maximum(a, b); }
DEV float phi1(float u) {
  float e = __builtin_amdgcn_fmed3f(__builtin_amdgcn_exp2f(__builtin_fmaf(u, 1.44269504089f, -1.44269504089e-6f)), 0.f, 1.f);
  return fmx(u, 1e-6f) + e;
}
DEV float phi1b(float u, float one) {
  float e = __builtin_amdgcn_fmed3f(__builtin_amdgcn_exp2f(__builtin_fmaf(u, 1.44269504089f, -1.44269504089e-6f)), 0.f, 1.f);
  return __builtin_fmaf(e, one, fmx(u, 1e-6f));
}
DEV f32x4 phi4(f32x4 v) {
  float one = 1.f; asm("" : "+s"(one));
  return f32x4{phi1(v[0]), phi1b(v[1], one), phi1(v[2]), phi1b(v[3], one)};
}
DEV f32x4 exp4(f32x4 u) {
  const f32x4 a = u * 1.44269504089f - 1.44269504089e-6f;
  f32x4 e;
#pragma unroll
  for (int j = 0; j < 4; ++j) e[j] = __builtin_amdgcn_fmed3f(__builtin_amdgcn_exp2f(a[j]), 0.f, 1.f);
  return e;
}
typedef short s16x8 __attribute__((ext_vector_type(8)));
DEV u32x4 relu8(u32x4 v) {
  return __builtin_bit_cast(u32x4, __builtin_elementwise_max(__builtin_bit_cast(s16x8, v), (s16x8)(short)0x3586));
}
DEV float asf(u32 u) { return __builtin_bit_cast(float, u); }
DEV u32 asu(float f) { return __builtin_bit_cast(u32, f); }
DEV void xmax4(float& a, float& b, float& c, float& d, float scale) {
  auto r = __builtin_amdgcn_permlane16_swap(asu(a), asu(b), false, false);
  auto t = __builtin_amdgcn_permlane16_swap(asu(c), asu(d), false, false);
  auto w = __builtin_amdgcn_permlane32_swap(asu(fmx(asf(r[0]), asf(r[1]))), asu(fmx(asf(t[0]), asf(t[1]))), false, false);
  const u32 z = asu(fmx(asf(w[0]), asf(w[1])) * scale);
  auto x = __builtin_amdgcn_permlane32_swap(z, z, false, false);
  auto y0 = __builtin_amdgcn_permlane16_swap(x[0], x[0], false, false);
  auto y1 = __builtin_amdgcn_permlane16_swap(x[1], x[1], false, false);
  a = asf(y0[0]); b = asf(y0[1]); c = asf(y1[0]); d = asf(y1[1]);
}
DEV u32x4 pair_rows(u32x2 v) {
  auto a = __builtin_amdgcn_permlane16_swap(v[0], v[0], false, false);
  auto b = __builtin_amdgcn_permlane16_swap(v[1], v[1], false, false);
  return u32x4{a[0], b[0], a[1], b[1]};
}
DEV u32 odd_rows(u32 x) { auto r = __builtin_amdgcn_permlane16_swap(x, x, false, false); return r[1]; }

template <class F> constexpr u64 lane_mask(F f) { u64 m = 0; for (int l = 0; l < 64; ++l) if (f(l >> 4, l & 15)) m |= 1ull << l; return m; }
#define LM(expr) ({ constexpr u64 m_ = lane_mask([](int g, int i) { return (bool)(expr); }); (bool)__builtin_amdgcn_inverse_ballot_w64(m_); })
typedef __amdgpu_buffer_rsrc_t rsrc_t;
DEV u32x4 gld128(rsrc_t r, u32 off) { return __builtin_amdgcn_raw_buffer_load_b128(r, off, 0, 16); }
DEV u32 gld32(rsrc_t r, u32 off) { return __builtin_amdgcn_raw_buffer_load_b32(r, off, 0, 16); }
DEV void gst128(rsrc_t r, u32 off, u32x4 v) { __builtin_amdgcn_raw_buffer_store_b128(v, r, off, 0, 16); }
DEV void gst64(rsrc_t r, u32 off, u32x2 v) { __builtin_amdgcn_raw_buffer_store_b64(v, r, off, 0, 16); }
DEV void gst32(rsrc_t r, u32 off, u32 v) { __builtin_amdgcn_raw_buffer_store_b32(v, r, off, 0, 16); }
DEV void publish(char* ws, u32 off, u64 tag, int lane) {
  waitv<0>();
  if (lane == 0) __hip_atomic_store((GAS u64*)(ws + off), tag, RLX_AGENT);
}
DEV void await(char* ws, u32 off, u64 tag) {
#pragma nounroll
  for (u32 spins = 0; spins < SPIN_CAP; ++spins) {
    u64 v = __hip_atomic_load((GAS u64*)(ws + off), RLX_AGENT);
    if (__all(v == tag)) break;
    __builtin_amdgcn_s_sleep(POLL_SLEEP);
  }
  fence_c();
}
DEV u32 ldw(u32 a) { return __hip_atomic_load((LAS u32*)(size_t)a, __ATOMIC_RELAXED, __HIP_MEMORY_SCOPE_WORKGROUP); }
DEV void stw(u32 a, u32 v) { __hip_atomic_store((LAS u32*)(size_t)a, v, __ATOMIC_RELAXED, __HIP_MEMORY_SCOPE_WORKGROUP); }
DEV void lds_arrive(u32 a, int lane, u32 n = 1) {
  if (lane == 0) __hip_atomic_fetch_add((LAS u32*)(size_t)a, n, __ATOMIC_RELAXED, __HIP_MEMORY_SCOPE_WORKGROUP);
}
template <int SLEEP> DEV void lds_spin(u32 a, int want) {
#pragma nounroll
  for (u32 spins = 0; spins < (SPIN_CAP << 4); ++spins) {
    if (__all((int)ldw(a) >= want)) break;
    if (SLEEP) __builtin_amdgcn_s_sleep(SLEEP);
  }
  fence_c();
}
template <int SLEEP> DEV void lds_await(bool ok, u32 a, int want) {
  if (__builtin_expect(!ok, 0)) lds_spin<SLEEP>(a, want);
  fence_c();
}
template <int NM> DEV void ilv() {
#pragma unroll
  for (int n = 0; n < NM; ++n) { __builtin_amdgcn_sched_group_barrier(0x8, 1, 0); __builtin_amdgcn_sched_group_barrier(0x80, 1, 0); }
}

typedef u32 u32x16 __attribute__((ext_vector_type(16)));
DEV u32 mul24(u32 a, u32 b) { return __umul24(a, b); }
DEV void w_offsets(int lane, u32 (&ow)[5]) {
#pragma unroll
  for (int i = 0; i < 5; ++i) {
    const u32 c = i * 64 + lane, pos = mul24(c, 6554u) >> 16;
    ow[i] = mul24(pos ^ ((pos >> 1) & 4), 3840u) + c * 16 - mul24(pos, 160u);
  }
}

__global__ __launch_bounds__(512, 2) void fused_kernel(const char*, const char*, const char*, const char*, const char*, const char*, char*, char*) {
  extern __shared__ __attribute__((aligned(16))) char smem[];
  u32x16 ka;
  asm volatile("s_load_dwordx16 %0, %1, 0x0" : "=s"(ka) : "s"(__builtin_amdgcn_kernarg_segment_ptr()));
  STAMP_DECL
  const u32 lds0 = (u32)(size_t)(LAS char*)smem;
  const int b = blockIdx.x, xcd = b & 7, jj = b >> 3;
  const int rt = (xcd & 1) * 8 + (jj & 7), ct = (xcd >> 1) * 4 + (jj >> 3);
  const int r0t = (((jj & 7) * 5 + (jj >> 3) * 10 + (xcd >> 1) * 3 + (xcd & 1) * 2) >> 2), r0 = r0t >= 10 ? r0t - 10 : r0t;
  const bool isB = ct >= 8;
  const int hg = ct & 7;
  const int wave = __builtin_amdgcn_readfirstlane(threadIdx.x >> 6);
  int lane = threadIdx.x & 63, g = lane >> 4, i16 = lane & 15, q4 = (lane >> 2) & 3, p4 = lane & 3;
  auto relane = [&]() {
    asm volatile("v_mbcnt_lo_u32_b32 %0, -1, 0\n\tv_mbcnt_hi_u32_b32 %0, -1, %0" : "=v"(lane));
    g = lane >> 4; i16 = lane & 15; q4 = (lane >> 2) & 3; p4 = lane & 3;
  };
  if (wave == 7) {
    const char* pc; u32 len;
    asm volatile("s_getpc_b64 %0\n.Lpf_here:\n\ts_mov_b32 %1, .Lpf_end-.Lpf_here" : "=s"(pc), "=s"(len));
    const u32 o = (lane * 32 + jj) * 128;
    if (o < len) touch(pc, o, lds0 + L_SCR);
  }
  {
    u32 ow[5];
    u32 o0 = mul24(lane >> 3, 2560u) + (((lane & 7) ^ (lane >> 4)) << 4), o1 = (o0 ^ 64) + 19456, o2 = o0 + 2 * 19456, o3 = o1 + 2 * 19456;
    w_offsets(lane, ow);
    ow[1] -= 1024; ow[2] -= 2048; ow[3] -= 3072;
    PIN(o0); PIN(o1); PIN(o2); PIN(o3); PIN(ow[0]); PIN(ow[1]); PIN(ow[2]); PIN(ow[3]); PIN(ow[4]);
    if (wave == 4 && lane < 60) stw(lds0 + L_DONE + lane * 4, 0);
    const u32 xoff = (128 * rt + 64 * (wave & 1) + (wave >> 1)) * 1280 + r0 * 128, woff = r0 * (64 * 3840) + hg * 160 + (wave + !isB) * 1280;
    asm volatile("s_waitcnt lgkmcnt(0)" : "+s"(ka)::"memory");
    if (wave < 4) {
      const char* xp = (const char*)(((u64)ka[1] << 32) | ka[0]);
      glds4(xp + xoff, o0, o1, o2, o3, lds0 + (isB ? L_BX0 : L_AX) + wave * 4096);
      if (wave < (isB ? 3 : 2)) {
        const char* wp = (const char*)(((u64)(isB ? ka[7] : ka[3]) << 32) | (isB ? ka[6] : ka[2])) + woff;
        const u32 d = lds0 + (isB ? L_BW : L_AW) + wave * SEG;
        glds4(wp, ow[0], ow[1], ow[2], ow[3], d); glds(wp, ow[4], d + 4096);
      } else if (!isB) {
        if (wave == 2 || lane < 16) glds((const char*)(((u64)ka[11] << 32) | ka[10]) + (wave - 2) * 1024, lane * 16, lds0 + L_GATEW + (wave - 2) * 1024);
      } else {
        const char* hp_ = xp + (rt ? 128 * rt - 16 : 0) * 1280 + r0 * 128;
        glds(hp_, o0, lds0 + L_BX0 + 16384); glds(hp_ + 1280, o0 ^ 64, lds0 + L_BX0 + 17408);
      }
    }
    bar();
    if (wave == 7 && lane < 2) touch((const char*)(((u64)ka[15] << 32) | ka[14]), WS_KVFLAG + (b * 2 + lane) * 128, lds0 + L_SCR);
    if (wave == 7) {
      const int cg = (b * 205) >> 13, ks = b - cg * 40, k20 = ks >= 20 ? ks - 20 : ks, sg = (k20 * 52) >> 8, st = k20 - sg * 5;
      const int qn = b - 96, qh = (qn * 205) >> 12;
      const char* so = (const char*)(((u64)(ks < 20 ? ka[9] : ka[5]) << 32) | (ks < 20 ? ka[8] : ka[4])) + ((sg * 160 + (st < 4 ? 40 * st : 16)) * 1280 + cg * 256);
      const char* sq = (const char*)(((u64)ka[3] << 32) | ka[2]) + ((qn - qh * 20) * (32 * 3840) + ((qh * 160) & ~127));
      const u32 r1 = (st < 4 ? 4 : 40) * 1280 - 1024, vo = g * 1280 + i16 * 16, vq = g * 3840 + i16 * 16;
      if (lane < 2) touch((const char*)(((u64)ka[15] << 32) | ka[14]) + WS_EPOCH, lane * 4, lds0 + L_CTL);
      lds_spin<0>(lds0 + L_TOK, PACK_TOK);
      if (b < 200) { glds4(so, vo, vo + r1, vo + 2 * r1, vo + 3 * r1, lds0); glds4(so + 20 * 1280, vo, vo + r1, vo + 2 * r1, vo + 3 * r1, lds0 + 4096); }
      if (b >= 96) {
        glds4(sq, vq, vq + 14336, vq + 2 * 14336, vq + 3 * 14336, lds0 + 8192);
        glds4(sq + 16 * 3840, vq, vq + 14336, vq + 2 * 14336, vq + 3 * 14336, lds0 + 12288);
      }
    }
    if (wave >= 4 && wave < 7) {
      const int u = wave - 3, xs_ = (u + 1) >> 1, pr = (u + 1) & 1;
      const int kw = r0 + (u >> 1) >= 10 ? r0 + (u >> 1) - 10 : r0 + (u >> 1), kx = r0 + xs_ >= 10 ? r0 + xs_ - 10 : r0 + xs_;
      const char* wp = (const char*)(((u64)(isB ? ka[7] : ka[3]) << 32) | (isB ? ka[6] : ka[2])) + ((kw * 64 + (u & 1) * 32) * 3840 + hg * 160);
      u32 d = lds0 + (isB ? L_BW + u * BW_SLOT : L_AW + u * AW_SLOT - SEG);
      asm volatile("" :: "s"(wp), "s"(d));
      if (u > 1) lds_spin<0>(lds0 + L_TOK, u - 1);
#pragma nounroll
      for (int sg = !isB; sg < 3; ++sg) { glds4(wp + sg * 1280, ow[0], ow[1], ow[2], ow[3], d + sg * SEG); glds(wp + sg * 1280, ow[4], d + sg * SEG + 4096); }
      const char* xp = (const char*)(((u64)ka[1] << 32) | ka[0]) + ((128 * rt + pr) * 1280 + kx * 128);
      d = lds0 + (isB ? L_BX1 + (xs_ - 1) * BX_SLOT : L_AX + xs_ * AX_SLOT);
      glds4(xp, o0, o1, o2, o3, d + pr * 8192); glds4(xp + 64 * 1280, o0, o1, o2, o3, d + pr * 8192 + 4096);
      if (isB) glds(xp - (rt ? 16 * 1280 : 0), o0 ^ (pr << 6), d + 16384 + pr * 1024);
      stw(lds0 + L_TOK, u);
      waitv<0>(); stw(lds0 + L_CNT + u * 4, 4);
    }
  }
  auto argp = [&](int n) { return (char*)(((u64)ka[2 * n + 1] << 32) | ka[2 * n]); };
  const char* const X = argp(0); const char* const WA = argp(1); const char* const WoA = argp(2); const char* const WB = argp(3);
  const char* const WoB = argp(4); const char* const GW = argp(5); char* const out = argp(6); char* const ws = argp(7);
  const rsrc_t wsr = __builtin_amdgcn_make_buffer_rsrc(ws, 0, WS_END, 0x00020000);
  const u32 aA0 = i16 * 128 + ((g ^ ((i16 >> 1) & 7)) << 4);
  const u32 rB0 = (8 * g + (q4 ^ (4 * (g & 1)))) * 160, rB1 = (8 * g + ((4 + q4) ^ (4 * (g & 1)))) * 160;
  const int rg = (xcd >> 1) * 8 + (jj & 7), ctile = (xcd & 1) * 4 + (jj >> 3), shr = jj & 7;
  auto wreg = [&](int gr) -> u32 { return lds0 + (isB ? (gr == 3 ? 0 : gr) : (gr < 2 ? 2 : gr - 2)) * (2 * CHUNK); };
  auto wo_issue = [&](int s0, int s1, int st) {
#pragma nounroll
    for (int s = s0; s < s1; s += st) {
      const int gr = (s * 26) >> 8;
      glds5p(ws + WS_WOP + (ctile * 40 + s) * 5120, lane * 16, lane * 16 + 4096, wreg(gr) + (s - gr * 10) * 5120);
    }
  };
  auto out_touch = [&](int w) { if (w < 2) touch(out, ((rg >> 1) * 128 + (rg & 1) + 2 * lane) * 1280 + ctile * 160 + w * 156, lds0 + L_SCR); };
  auto wo_post = [&](int s0, int s1, int st) {
    waitv<0>();
    const int s = s0 + lane * st;
    if (s < s1) __hip_atomic_fetch_add((LAS u32*)(size_t)(lds0 + L_WC + ((s * 52) >> 8) * 4), 1u, __ATOMIC_RELAXED, __HIP_MEMORY_SCOPE_WORKGROUP);
  };
  u64 tag;

  struct Att { u32 bk, bv, by, ya, mk, mv, my, ob, fo, b0; };
  auto att_prep = [&](int par, int hp, int half) {
    Att t;
    const u32 own = lds0 + L_HO + (hp * 2 + par) * HO_STRIDE + (hp ? 7 : 12) * 512, pi = 2 * (hg & 1) + hp;
    t.ya = lds0 + L_YX + par * 2048 + hp * 64;
    t.bk = half ? own : lds0 + L_HO + hp * 2 * HO_STRIDE + 17 * 512 + par * 64;
    t.bv = half ? own : lds0 + L_HO + (hp * 2 + 1) * HO_STRIDE + 14 * 512 + par * 256;
    t.by = half ? t.ya + 512 : t.bv + 7 * 512 + hp * 64;
    t.ya += half * 1024;
    t.mk = half ? 0x3F : 0x37; t.mv = half ? 0x3F : 0x1F; t.my = half ? 0x37 : 0x17;
    t.ob = WS_O + (rt * 8 + par * 4 + half * 2) * 40960 + (hg >> 1) * 5120 + pi * 256;
    t.fo = WS_BFLAG + (((rt * 2 + par) * 2 + half) * 2 + (hg >> 2)) * FSTR + ((hg & 3) * 2 + hp) * 8 + (pi << 24);
    t.b0 = rt == 0 && !half ? 0xF149F2CAu : 0u;
    asm volatile("" : "+s"(t.bk), "+s"(t.bv), "+s"(t.by), "+s"(t.ya), "+s"(t.mk), "+s"(t.mv), "+s"(t.my), "+s"(t.ob), "+s"(t.fo), "+s"(t.b0));
    return t;
  };
  auto attention = [&](u32x2 (&P)[2][8], const Att& t) {
    u32x2 Q[2][8], K4[2];
    {
      const u32 ya = t.ya + (lane & 0x37) * 8, ak = t.bk + (lane & t.mk) * 8, av = t.bv + (lane & t.mv) * 8;
      Q[0][2] = ld64(ak + 1024); Q[0][3] = ld64(ak + 1536); Q[0][4] = ld64(t.bk + ((lane | 16) & t.mk) * 8 + 2048);
      P[0][7] = ld64(ya); P[1][7] = ld64(ya + 512);
      Q[0][5] = ld64(av + 2560); Q[0][6] = ld64(av + 3072); Q[0][7] = ld64(t.by + (lane & t.my) * 8);
    }
    const u32x2 tg = ld64(lds0 + L_CTL);
    tag = __builtin_bit_cast(u64, tg) + TAG_STEP;
#pragma unroll
    for (int r = 0; r < 2; ++r) K4[r] = u32x2{odd_rows(P[r][4][0]), odd_rows(P[r][4][1])};
#pragma unroll
    for (int f = 2; f < 8; ++f) Q[1][f] = f == 4 ? K4[0] : P[0][f];
    const bool pv[4] = {LM(4 * g >= i + 9), LM(4 * g + 1 >= i + 9), LM(4 * g + 2 >= i + 9), LM(4 * g + 3 >= i + 9)};
    const f32x4 bc = {LM(4 * g <= i && 4 * g >= i - 7) ? 0.f : -1e30f, LM(4 * g + 1 <= i && 4 * g + 1 >= i - 7) ? 0.f : -1e30f,
                      LM(4 * g + 2 <= i && 4 * g + 2 >= i - 7) ? 0.f : -1e30f, LM(4 * g + 3 <= i && 4 * g + 3 >= i - 7) ? 0.f : -1e30f};
    const u32x2 mp = {pv[0] ? 0xFFFFFFFFu : (pv[1] ? 0xFFFF0000u : 0u), pv[2] ? 0xFFFFFFFFu : (pv[3] ? 0xFFFF0000u : 0u)};
    const bool godd = LM(g & 1), glow = LM(g < 2), gq[2] = {LM(g == 0), LM(g == 2)};
    const float cs = 0.22360679775f * 1.44269504089f, b0 = asf(t.b0);
    const u32x2 zero2 = {0u, 0u};
    const f32x4 z4 = splat(0.f);
    const u32x2 ones2 = {0x3F803F80u, 0x3F803F80u};
    f32x4 m[2][2];
    float mx[2][2];
#pragma unroll
    for (int r = 1; r >= 0; --r) {
#pragma unroll
      for (int h = 0; h < 2; ++h) {
        const u32x2 q4m = gq[h] ? P[r][4] : zero2;
        const f32x4 sc = mma16(K4[r], q4m, mma16(P[r][2 + h], P[r][h], bc));
        const f32x4 sp = mma16(Q[r][4], q4m, mma16(Q[r][2 + h], P[r][h], r ? z4 : splat(b0)));
#pragma unroll
        for (int j = 0; j < 4; ++j) m[r][h][j] = pv[j] ? sp[j] : sc[j];
        mx[r][h] = fmx(fmx(m[r][h][0], m[r][h][1]), fmx(m[r][h][2], m[r][h][3]));
      }
    }
    xmax4(mx[1][0], mx[1][1], mx[0][0], mx[0][1], -cs);
    u32x2 y[2];
#pragma unroll
    for (int r = 1; r >= 0; --r) {
      float rs[2];
      f32x4 o[2], yy[2];
#pragma unroll
      for (int h = 0; h < 2; ++h) {
        f32x4 e;
#pragma unroll
        for (int j = 0; j < 4; ++j) e[j] = __builtin_amdgcn_exp2f(__builtin_fmaf(m[r][h][j], cs, mx[r][h]));
        const u32x2 pp = pk4(e);
        const u32x2 p0 = pp & mp, p1 = pp & ~mp;
        rs[h] = __builtin_amdgcn_rcpf(mma16(ones2, pp, z4)[0]);
        o[h] = mma16(Q[r][5 + h], p0, mma16(P[r][5 + h], p1, z4)) * rs[h];
        yy[h] = mma16(Q[r][7], p0, mma16(P[r][7], p1, z4));
      }
      __builtin_amdgcn_raw_buffer_store_b128(cat(pk4(o[0]), pk4(o[1])), wsr, lane * 16, t.ob + (t.fo >> 24) * 768 + r * 40960, 16);
      y[r] = pk4((godd ? yy[1] : yy[0]) * (godd ? rs[1] : rs[0]));
    }
    __builtin_amdgcn_raw_buffer_store_b128(pair_rows(glow ? y[0] : y[1]), wsr, (godd ? OOB : (glow ? 0 : 40960)) + (lane & 15) * 16, t.ob + 4096, 16);
    STAMP(3);
    publish(ws, t.fo & 0xFFFFFF, tag, lane);
    fence_c(); lds_arrive(lds0 + L_PUB, lane);
    STAMP(4);
  };

  if (wave >= 4) {
    const int lw = wave - 4;
    __builtin_amdgcn_s_setprio(3);
    const int xpar = lw & 1;
    const u32 xo = isB && rt ? 16 * 1280 : 0;
    const char* const X0 = X + rt * (128 * 1280) - xo;
    u32 vx[9], vw[15];
    int xst = (lw >> 1) + 1;
    auto rot = [&](int st) { const int k = st + r0; return k >= 10 ? k - 10 : k; };
    auto issue_xp = [&](u32 slot, bool halo, int st, int pr) {
      const char* p = X0 + (rot(st) * 128 + pr * 1280);
      glds4(p, vx[0], vx[1], vx[2], vx[3], slot + pr * 8192); glds4(p, vx[4], vx[5], vx[6], vx[7], slot + pr * 8192 + 4096);
      if (halo) glds(p, vx[8] ^ (pr << 6), slot + 16384 + pr * 1024);
    };
    auto issue_x = [&](u32 slot, bool halo) { issue_xp(slot, halo, xst, xpar); };
    auto setup = [&](bool forB) {
      relane();
      const u32 ox0 = (lane >> 3) * 2560 + (((lane & 7) ^ (lane >> 4)) << 4);
      vx[0] = ox0 + xo; vx[1] = (ox0 ^ 64) + xo + 20480 - 1024; vx[2] = vx[0] + 2 * (20480 - 1024); vx[3] = vx[1] + 2 * (20480 - 1024);
#pragma unroll
      for (int p = 0; p < 4; ++p) vx[4 + p] = vx[p] + 64 * 1280;
      vx[8] = ox0;
      u32 ow[5];
      w_offsets(lane, ow);
#pragma unroll
      for (int p = 0; p < 15; ++p) { if (forB || p < 10) { vw[p] = ow[p % 5] + (p / 5) * 1280 - (wpos(p) - wpos(p & ~3)); PIN(vw[p]); } }
    };
    const char* Wt = (isB ? WB : WA) + hg * 160;
    auto wsrc = [&](int u) { return Wt + (rot(u >> 1) * 64 + (u & 1) * 32) * 3840; };
    const u32 cw = lds0 + L_CNT + (lw + 1) * 4;
    const bool qjob = lw == 2, pjob = qjob ? b >= 96 : (lw == 3 && b < 200);
    auto pack_store = [&]() {
      int jn = b;
      asm volatile("" : "+s"(jn));
      u32x4 fr[8];
      if (qjob) {
        lds_spin<1>(lds0 + L_CNT + 16, 4);
        jn -= 96;
        const u32 a = lds0 + 8192 + (8 * g + q4) * 256 + ((((jn * 205) >> 12) * 160) & 127), a0 = a + p4 * 8, a4 = a + 32 + p4 * 40;
#pragma unroll
        for (int f = 0; f < 5; ++f) fr[f] = f < 4 ? ldB(a0 + f * 40, a0 + f * 40 + 1024) : ldB(a4, a4 + 1024);
#pragma unroll
        for (int f = 0; f < 5; ++f) __builtin_amdgcn_raw_buffer_store_b128(fr[f], wsr, lane * 16 + (f & 3) * 1024, WS_WQP + jn * 5120 + (f >> 2) * 4096, 16);
      } else {
        const int cg = (jn * 205) >> 13, ks = jn - cg * 40, k20 = ks >= 20 ? ks - 20 : ks, sg = (k20 * 52) >> 8;
        const u32 a = lds0 + (4 * g + q4) * 256 + p4 * 8;
#pragma unroll
        for (int f = 0; f < 8; ++f) fr[f] = ldB(a + f * 32, a + f * 32 + 4096);
        const u32 d = WS_WOP + (((ks < 20 ? 0 : 2) + (sg & 1)) * 10 + (sg >> 1) * 5 + k20 - sg * 5) * 5120 + cg * 8192;
#pragma unroll
        for (int f = 0; f < 8; ++f) __builtin_amdgcn_raw_buffer_store_b128(fr[f], wsr, lane * 16, d + f * 1024 + (((cg * 8 + f) * 52) >> 8) * (39 * 5120), 16);
      }
    };
    auto pack_flag = [&]() {
      const u64 t = *(LAS u64*)(size_t)(lds0 + L_CTL) + TAG_STEP;
      if (lane == 0) __hip_atomic_store((GAS u64*)(ws + WS_PFLAG + ((qjob ? 104 : 0) + b) * 8), t, RLX_AGENT);
    };
    auto first_done = [&]() {
      if (OT == 2 || (OT == 3 && !isB)) out_touch(lw);
      if (lw == 3) { waitv<0>(); stw(cw, 4); }
      if (pjob && PACK_R == 0) pack_store();
    };
    auto pack_wait = [&]() {
      const u64 t = *(LAS u64*)(size_t)(lds0 + L_CTL) + TAG_STEP;
      const u32 f0 = WS_PFLAG + (lane < 40 ? ((ctile * 5) >> 3) * 40 + lane : 200 + hg * 20 + (lane < 60 ? lane - 40 : 0)) * 8;
      const u32 f1 = WS_PFLAG + (((ctile * 5 + 4) >> 3) * 40 + (lane < 40 ? lane : 0)) * 8;
#pragma nounroll
      for (u32 spins = 0; spins < SPIN_CAP; ++spins) {
        const u64 v0 = __hip_atomic_load((GAS u64*)(ws + f0), RLX_AGENT), v1 = __hip_atomic_load((GAS u64*)(ws + f1), RLX_AGENT);
        if (__all(v0 == t && v1 == t)) break;
        __builtin_amdgcn_s_sleep(POLL_SLEEP);
      }
      fence_c();
    };
    if (isB) {
      setup(true); STAMP(1); STAMP(2);
      const u32 wd = lds0 + L_BW + ((lw + 1) & 3) * BW_SLOT, da = lds0 + L_DONE + (lane & 3) * 4;
      u32 xs = lds0 + L_BX1 + (lw >> 1) * BX_SLOT;
      auto unit = [&](int u) {
        lds_spin<1>(da, u - 3);
        const char* wp = wsrc(u); gldsw<0>(wp, vw, wd); gldsw<4>(wp, vw, wd); gldsw<8>(wp, vw, wd); gldsw<12>(wp, vw, wd);
        if (u < 19) issue_x(xs, true);
      };
      if (lw == 3) unit(4);
      STAMP(9);
      first_done();
      STAMP(10);
#pragma nounroll
      for (int u = lw + 5; u < 20; u += 4) {
        xst += 2; xs = lds0 + (xs == lds0 + L_BX0 ? L_BX2 : (xs == lds0 + L_BX2 ? L_BX1 : L_BX0));
        unit(u);
        waitv<0>(); stw(lds0 + L_CNT + u * 4, 4);
        if (pjob && u == lw + 1 + 4 * PACK_R) pack_store();
        if (pjob && u == lw + 5 + 4 * PACK_R) pack_flag();
      }
      STAMP(11);
      if (lw == 3) { pack_wait(); stw(lds0 + L_CNT + 23 * 4, 4); }
      lds_spin<1>(lds0 + L_CNT + ((lane & 1) ? TAILG : 23) * 4, 4);
      wo_issue(3 - lw, 7, 4);
      const Att at = att_prep(lw & 1, lw >> 1, (lw >> 1) ^ 1);
      u32x4 park[25];
      lds_spin<1>(da, 18);
      wo_issue(7 + lw, 20, 4);
#pragma unroll
      for (int n = 0; n < 25; ++n)
        park[n] = __builtin_amdgcn_raw_buffer_load_b128(wsr, lane * 16 + (n & 3) * 1024, WS_WOP + (ctile * 8 + 4 + lw) * CHUNK + (n >> 2) * 4096, 16);
      wo_post(3 - lw, 7, 4); wo_post(7 + lw, 20, 4);
      out_touch(lw);
      __builtin_amdgcn_s_setprio(0);
      bar();
      u32x2 P[2][8];
      const u32 hl = lds0 + L_HO + lw * HO_STRIDE + lane * 8;
#pragma unroll
      for (int f = 0; f < 7; ++f) { P[0][f] = ld64(hl + f * 512); P[1][f] = ld64(hl + (7 + f) * 512); }
      attention(P, at);
      lds_spin<1>(lds0 + L_PUB, 8);
      lds_spin<1>(lds0 + L_D2 + (lane & 3) * 4, 1);
      {
        const u32 a = wreg(2 + (lw >> 1)) + (lw & 1) * CHUNK + lane * 16;
#pragma unroll
        for (int n = 0; n < 25; ++n) *(LAS u32x4*)(size_t)(a + n * 1024) = park[n];
        fence_c(); stw(lds0 + L_WC + (4 + lw) * 4, 5);
      }
    } else {
      setup(false);
      const u32 wd = lds0 + L_AW + ((lw + 1) & 3) * AW_SLOT, da = lds0 + L_DONE + (lane & 7) * 4;
      u32x4 xr[20][2];
      f32x4 gl[2] = {};
      u32 xa0 = lds0 + L_AX + lw * 4096 + i16 * 128 + ((g ^ ((i16 >> 1) & 7)) << 4), xa1 = xa0 ^ 64;
      PIN(xa0); PIN(xa1);
      auto take = [&](int st) {
        lds_spin<1>(lds0 + L_CNT + (st ? 2 * st - (lane & 1) : 0) * 4, 4);
        const u32 o = (st & 3) * AX_SLOT;
        xr[2 * st][0] = ld128(xa0 + o); xr[2 * st][1] = ld128(xa0 + o + 2048);
        xr[2 * st + 1][0] = ld128(xa1 + o); xr[2 * st + 1][1] = ld128(xa1 + o + 2048);
        fence_c(); stw(lds0 + L_DONE + wave * 4, st == 9 ? 30 : (st > 6 ? 19 : 2 * st + 5));
      };
      auto gate_mm = [&](int st) {
        const u32 go = lds0 + L_GATEW + rot(st) * 128 + g * 16;
        const u32x4 g0 = ld128(go), g1 = ld128(go + 64);
        gl[0] = mma(g1, xr[2 * st + 1][0], mma(g0, xr[2 * st][0], gl[0])); gl[1] = mma(g1, xr[2 * st + 1][1], mma(g0, xr[2 * st][1], gl[1]));
      };
      auto wq = [&](int n) { glds5p(ws + WS_WQP + (hg * 20 + rot(n >> 1) * 2 + (n & 1)) * 5120, lane * 16, lane * 16 + 4096, lds0 + n * 5120); };
      stw(lds0 + L_DONE + wave * 4, 3);
#pragma unroll
      for (int j = 0; j < 5; ++j) {
        const int u = 4 * j + lw + 1;
        if (j >= 1) take(2 * j - 2);
        if (j == 0 ? lw == 3 : (j < 4 || lw < 3)) {
          lds_spin<1>(da, u - 3);
          const char* wp = wsrc(u) + 1280; gldsw<0>(wp, vw, wd); gldsw<4>(wp, vw, wd);
          asm volatile("s_add_u32 m0, %3, %4\n\ts_nop 0\n\tglobal_load_lds_dwordx4 %0, %2\n\tglobal_load_lds_dwordx4 %1, %2 offset:1024"
                       ::"v"(vw[8]), "v"(vw[9]), "s"(wp), "s"(wd), "i"(wpos(8)) : "scc");
          if (u < 19) issue_x(lds0 + L_AX + (xst & 3) * AX_SLOT, false);
        }
        xst += 2;
        if (j >= 1) take(2 * j - 1);
        if (j == 4 && lw == 3) { lds_spin<1>(lds0 + L_CNT + A_PW * 4, 4); pack_wait(); }
        if (j == 0) STAMP(9);
        if (j == 0) first_done(); else { waitv<0>(); stw(cw + j * 16, 4); }
        if (j == 0) STAMP(10); if (j == 4) STAMP(11);
        if (j > 0 && j == PACK_R && pjob) pack_store();
        if (j == PACK_R + 1 && pjob) pack_flag();
      }
      take(8); take(9);
      lds_spin<1>(lds0 + L_CNT + 20 * 4, 4);
      wq(lw); wq(lw + 4);
      out_touch(lw);
      lds_spin<1>(da, 20);
      STAMP(12);
#pragma nounroll
      for (int n = 8 + lw; n < 20; n += 4) wq(n);
      wo_issue(3 - lw, 10, 4);
      __builtin_amdgcn_s_setprio(0);
      STAMP(1);
      relane();
#pragma unroll
      for (int st = 0; st < 8; ++st) gate_mm(st);
      gate_mm(8); gate_mm(9);
      float gate[2];
#pragma unroll
      for (int r = 0; r < 2; ++r) { gate[r] = __builtin_amdgcn_rcpf(1.f + __builtin_amdgcn_exp2f(gl[r][0] * -1.44269504089f)); PIN(gate[r]); }
      wo_post(3 - lw, 10, 4); lds_arrive(lds0 + L_CNT + 21 * 4, lane);
      u32x2 pq[2][5];
      {
        f32x4 qa[2][5];
        float pe[4], pm[4];
        auto phi_step = [&](auto F, auto Y) {
          constexpr int y = Y.v, r = y / 18, k = y % 18, j = k & 3;
          if constexpr (y >= 0 && y < 36) {
            if constexpr (k < 4) { pe[j] = __builtin_fmaf(qa[r][F.v][j], 1.44269504089f, -1.44269504089e-6f); PIN(pe[j]); }
            else if constexpr (k < 8) { pe[j] = __builtin_amdgcn_fmed3f(__builtin_amdgcn_exp2f(pe[j]), 0.f, 1.f); PIN(pe[j]); }
            else if constexpr (k < 12) { pm[j] = fmx(qa[r][F.v][j], 1e-6f); PIN(pm[j]); }
            else if constexpr (k < 16) { pm[j] += pe[j]; PIN(pm[j]); }
            else { u32 x = pk2(pm[2 * j], pm[2 * j + 1]); PIN(x); pq[r][F.v][j] = x; }
          }
        };
        u32 q0 = lds0 + lane * 16, q1 = q0 + 61440;
        PIN(q0); PIN(q1);
        constexpr int D = QD;
        u32x4 w[D + 1];
        __builtin_amdgcn_s_setprio(QPRIO);
        sfor<-D, 100>([&](auto T) {
          constexpr int t = T.v, n = t + D, f = t / 20, pf = f - 1;
          if constexpr (n == 0) lds_spin<1>(lds0 + L_CNT + 21 * 4, 8);
          if constexpr (n == 0) { STAMP(6); CSTAMP(14); }
          if constexpr (n % 20 == 0 && n < 100) { qa[0][n / 20] = qa[1][n / 20] = splat(1e-6f); PIN(qa[0][n / 20]); PIN(qa[1][n / 20]); }
          SB();
          if constexpr (t >= 0) mmai(qa[0][f], w[t % (D + 1)], xr[t % 20][0]);
          SB();
          if constexpr (n < 100) w[n % (D + 1)] = ld128((n % 20 >= 12 ? q1 + (n % 20 - 12) * 5120 : q0 + (n % 20) * 5120) + (n / 20) * 1024);
          if constexpr (t >= 20) phi_step(IC<pf>{}, IC<2 * (t % 20) - 4>{});
          SB();
          if constexpr (t >= 0) mmai(qa[1][f], w[t % (D + 1)], xr[t % 20][1]);
          SB();
          if constexpr (t >= 20) phi_step(IC<pf>{}, IC<2 * (t % 20) - 3>{});
        });
        mma_done();
        __builtin_amdgcn_s_setprio(1);
        STAMP(2); CSTAMP(15);
        pq[0][4] = pk4(phi4(qa[0][4])); pq[1][4] = pk4(phi4(qa[1][4])); PIN(pq[0][4]); PIN(pq[1][4]);
      }
      relane();
      u32 ta[4][4];
      {
        const u32 tb = lds0 + L_TAB, tz = tb + 105 * 8, t0 = tb + (64 + i16) * 8, t1 = tb + (80 + p4 + 4 * g) * 8, t2 = tb + (100 + p4) * 8;
#pragma unroll
        for (int h = 0; h < 4; ++h) {
          const bool gh = __builtin_amdgcn_inverse_ballot_w64(0xFFFFull << (16 * h)), eh = __builtin_amdgcn_inverse_ballot_w64(0x000F000F000F000Full << (4 * (h ^ 1)));
          ta[h][0] = gh ? t0 : tz; ta[h][1] = eh ? t1 : tz; ta[h][2] = eh && gh ? t2 : tz; ta[h][3] = gh ? tz - 8 : tz;
#pragma unroll
          for (int i = 0; i < 4; ++i) PIN(ta[h][i]);
        }
      }
      __builtin_amdgcn_s_setprio(3);
      waitl0(); bar();
      STAMP(3);
      tag = *(LAS u64*)(size_t)(lds0 + L_CTL) + TAG_STEP;
      f32x4 n5[2] = {};
      u32x2 ob[2][4];
      float sc5[2] = {0.f, 0.f};
#pragma unroll
      for (int h = 0; h < 4; ++h) {
        const u32 tb = lds0 + L_TAB + h * TAB_STRIDE;
        const u32x2 a0 = ld64(tb + lane * 8), a1 = ld64(ta[h][0] + h * TAB_STRIDE), a5 = ld64(ta[h][1] + h * TAB_STRIDE), a6 = ld64(ta[h][2] + h * TAB_STRIDE);
        const u32x2 z0 = ld64(tb + (96 + g) * 8), z1 = ld64(ta[h][3] + h * TAB_STRIDE);
#pragma unroll
        for (int r = 0; r < 2; ++r) {
          const f32x4 den = mma16(z1, pq[r][4], mma16(z0, pq[r][h], splat(1e-6f))), num = mma16(a1, pq[r][4], mma16(a0, pq[r][h], splat(0.f)));
          n5[r] = mma16(a6, pq[r][4], mma16(a5, pq[r][h], n5[r]));
          const float sc = gate[r] * __builtin_amdgcn_rcpf(den[0]);
          ob[r][h] = pk4(num * sc);
          sc5[r] = __builtin_amdgcn_inverse_ballot_w64(0xFFFFull << (16 * (h ^ 1))) ? sc : sc5[r];
        }
      }
#pragma unroll
      for (int r = 0; r < 2; ++r) {
        const u32 blk = rt * 8 + lw * 2 + r;
        const u32 base = WS_O + blk * 40960 + 20480 + (hg >> 1) * 5120;
        gst128(wsr, base + (hg & 1) * 2048 + lane * 16, cat(ob[r][0], ob[r][1]));
        gst128(wsr, base + (hg & 1) * 2048 + 1024 + lane * 16, cat(ob[r][2], ob[r][3]));
        const u32x4 o5 = pair_rows(pk4(n5[r] * sc5[r]));
        gst128(wsr, (g & 1) ? OOB : base + 4096 + ((2 * (hg & 1) + (g >> 1)) * 16 + i16) * 16, u32x4{o5[2], o5[3], o5[0], o5[1]});
      }
      gst32(wsr, lane < 32 ? WS_GATE + (hg * 128 + rt * 8 + lw * 2) * 64 + lane * 4 : OOB, asu(lane < 16 ? gate[0] : gate[1]));
      STAMP(5);
      fence_c(); lds_arrive(lds0 + L_PUB, lane);
      publish(ws, WS_AFLAG + ((rt * 4 + lw) * 2 + (hg >> 2)) * FSTR + (hg & 3) * 8, tag, lane);
      STAMP(4);
    }
    if (!isB)
    {
      lds_spin<2>(lds0 + L_ISS, 4);
      wo_issue(30 + lw, 40, 4);
      wo_post(30 + lw, 40, 4);
    }
    STAMP(8);
    if (b == 0 && lw == 0) {
      const u32 fo = ((lane >> 3) * 8 + ((lane >> 2) & 1)) * FSTR;
#pragma nounroll
      for (u32 spins = 0; spins < SPIN_CAP; ++spins) {
        bool ok = true;
#pragma unroll
        for (int r = 0; r < 2; ++r) {
          ok &= __hip_atomic_load((GAS u64*)(ws + WS_BFLAG + fo + r * 64 * FSTR + (lane & 3) * 16), RLX_AGENT) == tag;
          ok &= __hip_atomic_load((GAS u64*)(ws + WS_AFLAG + fo + r * 64 * FSTR + (lane & 3) * 8), RLX_AGENT) == tag;
        }
        if (__all(ok)) break;
        __builtin_amdgcn_s_sleep(8);
      }
      if (lane == 0) __hip_atomic_store((GAS u64*)(ws + WS_EPOCH), tag, RLX_AGENT);
    }
    STAMP(13); STAMP_OUT;
    asm volatile(".Lpf_end:");
    return;
  }

  {
  }
  auto get_tag = [&]() {
    const u32x2 t = ld64(lds0 + L_CTL);
    tag = ((u64)(u32)__builtin_amdgcn_readfirstlane(t[1]) << 32) | (u32)__builtin_amdgcn_readfirstlane(t[0]);
    tag += TAG_STEP;
  };
  u32x4 g0[25];
  auto take_g0 = [&]() {
    lds_spin<1>(lds0 + L_WC + (wave >> 1) * 4, 5);
    const u32 a = wreg(0) + (wave >> 1) * CHUNK + lane * 16;
#pragma unroll
    for (int n = 0; n < 25; ++n) g0[n] = ld128(a + n * 1024);
    fence_c(); stw(lds0 + L_D2 + wave * 4, 1);
  };
  u32 ca = lds0 + L_CNT, dcount = 1;
  u32 dn = lds0 + L_DONE + wave * 4;
  PIN(dn);
  if (isB) {
    const int par = wave & 1, hp = wave >> 1;
    u32 bM0 = lds0 + L_BW + rB0 + hp * 80 + p4 * 8, bM1 = bM0 - rB0 + rB1;
    u32 bX0 = lds0 + L_BW + rB0 + hp * 80 + 32 + (p4 & 1) * SEG + (p4 >> 1) * 40, bX1 = bX0 - rB0 + rB1;
    u32 bY0 = lds0 + L_BW + rB0 + 2 * SEG + 32 + p4 * 40, bY1 = bY0 - rB0 + rB1;
    u32 alo = aA0 + par * 8192 + hp * 4096, ahi = aA0 + par * 8192 + (hp ^ 1) * 4096;
    PIN(bM0); PIN(bM1); PIN(bX0); PIN(bX1); PIN(bY0); PIN(bY1); PIN(alo); PIN(ahi); PIN(ca); PIN(dcount);
    const Att at = att_prep(par, hp, hp);
    f32x4 acc[4][7], accy[2], acch[3];
#pragma unroll
    for (int f = 0; f < 3; ++f) { acch[f] = splat(0.f); PIN(acch[f]); }
#pragma unroll
    for (int r = 0; r < 4; ++r)
#pragma unroll
      for (int f = 0; f < 7; ++f) { acc[r][f] = splat(0.f); PIN(acc[r][f]); }
    accy[0] = accy[1] = splat(0.f); PIN(accy[0]); PIN(accy[1]);
    struct Fr { u32x4 a[4]; u32x2 w[8][2]; };
    u32 xs = lds0 + L_BX0, x0 = xs + alo, x1 = xs + ahi;
    const u32 hoff = 16384 - par * 8192 - hp * 4096;
    u32x4 ah[2];
    auto rd1 = [&](Fr& F, int sl, int n) {
      const u32 o = sl * BW_SLOT;
      if (n < 4) { F.a[n] = ld128((n < 2 ? x0 : x1) + (n & 1) * 2048); return; }
      const int f = (n - 4) >> 1, t = n & 1;
      if (f == 4) F.w[4][t] = ldtr((t ? bX1 : bX0) + o);
      else if (f == 5) F.w[7][t] = ldtr((t ? bY1 : bY0) + o);
      else { const int ff = f < 4 ? f : f - 2; F.w[f < 4 ? f : f - 1][t] = ldtr((t ? bM1 : bM0) + o + (ff >> 1) * SEG + (ff & 1) * 40); }
    };
    auto mm1 = [&](Fr& F, int n) {
      if (n >= 20 && n < 22) { mmai(accy[n - 20], F.a[n - 20], cat(F.w[7][0], F.w[7][1])); return; }
      const int m = n < 20 ? n : n - 2, f = m >> 2, r = m & 3;
      const u32x4 w = cat(F.w[f][0], F.w[f][1]);
      if (f < 5) mmai(acc[r][f], w, F.a[r]); else mmai(acc[r][f], F.a[r], w);
    };
    Fr F0, F1;
    waitv<0>(); lds_arrive(lds0 + L_CNT, lane);
    lds_spin<0>(ca, 4);
    STAMP(1); CSTAMP(14);
#pragma unroll
    for (int n = 0; n < 20; ++n) rd1(F0, 0, n);
    ah[0] = ld128(x0 + hoff);
    fence_c(); stw(dn, dcount);
    bool ok = false;
    x0 = xs + (alo ^ 64); x1 = xs + (ahi ^ 64);
    auto unit = [&](Fr& Fc, Fr& Fn, int k, bool last) {
      u32 cv;
#pragma unroll
      for (int n = 0; n < 30; ++n) {
        SB(); mm1(Fc, n); SB();
        if (n == 3) {
          asm volatile("s_cmp_lg_u32 %10, 0\n\ts_cbranch_scc1 .Lhv%=\n\t"
                       "v_mfma_f32_16x16x32_bf16 %0, %4, %3, %0\n\tv_mfma_f32_16x16x32_bf16 %1, %5, %3, %1\n\tv_mfma_f32_16x16x32_bf16 %2, %6, %3, %2\n\t"
                       "s_branch .Lhe%=\n.Lhv%=:\n\t"
                       "v_mfma_f32_16x16x32_bf16 %0, %3, %7, %0\n\tv_mfma_f32_16x16x32_bf16 %1, %3, %8, %1\n\tv_mfma_f32_16x16x32_bf16 %2, %3, %9, %2\n.Lhe%=:"
                       : "+v"(acch[0]), "+v"(acch[1]), "+v"(acch[2])
                       : "v"(ah[k & 1]), "v"(cat(Fc.w[2][0], Fc.w[2][1])), "v"(cat(Fc.w[3][0], Fc.w[3][1])), "v"(cat(Fc.w[4][0], Fc.w[4][1])),
                         "v"(cat(Fc.w[5][0], Fc.w[5][1])), "v"(cat(Fc.w[6][0], Fc.w[6][1])), "v"(cat(Fc.w[7][0], Fc.w[7][1])), "s"(par) : "scc");
          SB();
          if (!last) lds_await<0>(ok, ca + (k + 1) * 4, 4);
          cv = ldw(ca + (k + 2) * 4);
        }
        if (n >= 4 && n < 24) rd1(Fn, (k + 1) & 3, n - 4);
        if (n == 10) ++dcount;
        if (n == 24) ah[(k + 1) & 1] = ld128(x0 + hoff);
        if (n == 25) { fence_c(); stw(dn, dcount); }
        if (n == 28) ok = __all(cv >= 4);
        if (n == 26) { if (!(k & 1)) xs = lds0 + (xs == lds0 + L_BX0 ? L_BX1 : (xs == lds0 + L_BX1 ? L_BX2 : L_BX0)); x0 = (k & 1) ? x0 ^ 64 : xs + alo; PIN(x0); }
        if (n == 27) { x1 = (k & 1) ? x1 ^ 64 : xs + ahi; PIN(x1); }
      }
      SB();
    };
#pragma nounroll
    for (int it = 0; it < 5; ++it) {
      unit(F0, F1, 0, false); unit(F1, F0, 1, false); unit(F0, F1, 2, false); unit(F1, F0, 3, it == 4);
      ca += 16;
    }
    mma_done();
    STAMP(2); CSTAMP(15);
    relane();
    u32x2 P[2][8];
    {
      const u32 dd = lds0 + L_DONE + (lane & 3) * 4;
      const int dv = ldw(dd);
      u32x2 H[2][7];
#pragma unroll
      for (int f = 0; f < 7; ++f) { H[0][f] = pk4(acc[2][f]); H[1][f] = pk4(acc[3][f]); PIN(H[0][f]); PIN(H[1][f]); }
      lds_await<0>(__all(dv >= 20), dd, 20);
      const u32 hl = lds0 + L_HO + wave * HO_STRIDE + lane * 8, yl = lds0 + L_YX + (par * 4 + 2 * hp) * 512 + lane * 8;
#pragma unroll
      for (int f = 0; f < 7; ++f) { st64(hl + f * 512, H[0][f]); st64(hl + (7 + f) * 512, H[1][f]); }
#pragma unroll
      for (int f = 2; f < 7; ++f) { P[1][f] = pk4(acc[1][f]); st64(hl + (12 + f) * 512, P[1][f]); }
#pragma unroll
      for (int f = 0; f < 3; ++f) st64(hl + (19 + f) * 512, pk4(acch[f]));
      st64(yl, pk4(accy[0])); st64(yl + 512, pk4(accy[1]));
    }
    STAMP(5);
    waitl0(); bar();
    STAMP(6);
#pragma unroll
    for (int f = 0; f < 7; ++f) P[0][f] = pk4(acc[0][f]);
    P[1][0] = pk4(acc[1][0]); P[1][1] = pk4(acc[1][1]);
    attention(P, at);
    take_g0();
    lds_spin<1>(lds0 + L_PUB, 8);
  } else {
    const int h = wave;
    u32 bK0 = lds0 + L_AW + rB0 + h * 40 + p4 * 8, bK1 = bK0 - rB0 + rB1;
    u32 bZ0 = lds0 + L_AW + rB0 + h * 40 + 32 + (p4 == (h ^ 1) ? SEG : 0), bZ1 = bZ0 - rB0 + rB1;
    PIN(bK0); PIN(bK1); PIN(bZ0); PIN(bZ1); PIN(ca); PIN(dcount);
    f32x4 acc[8][3];
#pragma unroll
    for (int r = 0; r < 8; ++r)
#pragma unroll
      for (int f = 0; f < 3; ++f) { acc[r][f] = splat(f == 0 ? 1e-6f : (f == 1 || q4 == (h ^ 1) ? 0.f : 1e-6f)); PIN(acc[r][f]); }
    struct Fr { u32x4 a[8]; u32x2 w[3][2]; };
    u32 xs = 0, xa = lds0 + L_AX + aA0, x0 = xa;
    PIN(xa);
    auto rd1 = [&](Fr& F, int sl, int n) {
      const u32 o = sl * AW_SLOT;
      if (n >= 6) { F.a[n - 6] = ld128(x0 + (n - 6) * 2048); return; }
      const int f = n >> 1, t = n & 1;
      F.w[f][t] = f < 2 ? ldtr((t ? bK1 : bK0) + o + f * SEG) : ldtr((t ? bZ1 : bZ0) + o);
    };
    Fr F0, F1;
    waitv<0>(); lds_arrive(lds0 + L_CNT, lane);
    lds_spin<0>(ca, 4);
    STAMP(1); CSTAMP(14);
#pragma unroll
    for (int n = 0; n < 14; ++n) rd1(F0, 0, n);
    fence_c(); stw(dn, dcount);
    bool ok = false;
    x0 ^= 64;
    auto unit = [&](Fr& Fc, Fr& Fn, int k, bool last) {
      u32 cv;
#pragma unroll
      for (int n = 0; n < 24; ++n) {
        SB(); mmai(acc[n / 3][n % 3], Fc.a[n / 3], cat(Fc.w[n % 3][0], Fc.w[n % 3][1])); SB();
        if (n == 2 && !last) lds_await<0>(ok, ca + (k + 1) * 4, 4);
        if (n == 2) cv = ldw(ca + (k + 2) * 4);
        if (n >= 3 && n < 17) rd1(Fn, (k + 1) & 3, n - 3);
        if (n == 10) ++dcount;
        if (n == 17) { fence_c(); stw(dn, dcount); }
        if (n == 19) ok = __all(cv >= 4);
        if (n == 20) { if (!(k & 1)) xs = (xs + AX_SLOT) & (4 * AX_SLOT - 1); x0 = (k & 1) ? x0 ^ 64 : xs + xa; PIN(x0); }
      }
      SB();
    };
#pragma nounroll
    for (int it = 0; it < 5; ++it) {
      unit(F0, F1, 0, false); unit(F1, F0, 1, false); unit(F0, F1, 2, false); unit(F1, F0, 3, it == 4);
      ca += 16;
    }
    mma_done();
    STAMP(2); CSTAMP(15);
    relane(); get_tag();
    __builtin_amdgcn_s_setprio(2);
    const bool eh = q4 == (h ^ 1), gh = g == h, zc = i16 == 4 * h;
    const u32x4 ones = {0x3F803F80u, 0x3F803F80u, 0x3F803F80u, 0x3F803F80u};
    f32x4 t00 = {}, t01 = {}, t10 = {}, t11 = {};
#pragma unroll
    for (int k = 0; k < 4; ++k) {
      const u32x4 ek = cat(pk4(exp4(acc[2 * k][0])), pk4(exp4(acc[2 * k + 1][0]))), ez = cat(pk4(exp4(acc[2 * k][2])), pk4(exp4(acc[2 * k + 1][2])));
      const u32x4 zz = cat(pk4(acc[2 * k][2]), pk4(acc[2 * k + 1][2]));
      const u32x4 rk = relu8(cat(pk4(acc[2 * k][0]), pk4(acc[2 * k + 1][0]))), rz = relu8(zz);
      const u32x4 vv = cat(pk4(acc[2 * k][1]), pk4(acc[2 * k + 1][1])), zb = eh ? zz : ones;
      t00 = mma(ek, vv, mma(rk, vv, t00)); t01 = mma(ek, zb, mma(rk, zb, t01));
      t10 = mma(ez, vv, mma(rz, vv, t10)); t11 = mma(ez, zb, mma(rz, zb, t11));
    }
    const u32 pb = WS_KVP + (hg * 4 + h) * 16 * KVP_STRIDE, tb = lds0 + L_TAB + h * TAB_STRIDE;
    st64(tb + lane * 8, pk4(t00));
    st64(tb + (gh ? 64 + i16 : 106) * 8, pk4(t10));
    st64(tb + (eh ? 80 + p4 + 4 * g : (zc ? 96 + g : 106)) * 8, pk4(t01));
    st64(tb + (gh && eh ? 100 + p4 : (gh && zc ? 104 : 105)) * 8, pk4(t11));
    const u32 po = lane < 53 ? pb + lane * 16 : OOB;
    gst128(wsr, (lane < 56 ? pb + lane * 16 : OOB) + rt * KVP_STRIDE, ld128(tb + lane * 16));
    lds_arrive(lds0 + L_CNT + 21 * 4, lane);
    STAMP(3);
    publish(ws, WS_KVFLAG + (hg * 4 + h) * FSTR + rt * 8, tag, lane);
    STAMP(4);
    __builtin_amdgcn_s_setprio(0);
    take_g0();
    lds_spin<1>(lds0 + L_D2 + (lane & 3) * 4, 1);
    wo_issue(10 + h, 20, 4);
    await(ws, WS_KVFLAG + (hg * 4 + h) * FSTR + i16 * 8, tag);
    __builtin_amdgcn_s_setprio(2);
    STAMP(5);
    {
      u32x4 vm[16];
#pragma unroll
      for (int r = 0; r < 16; ++r) vm[r] = __builtin_amdgcn_raw_buffer_load_b128(wsr, po, r * KVP_STRIDE, 16);
      float sm[8] = {};
      u32 sel0 = 0x00003F80u, sel1 = 0x3F800000u;
      PIN(sel0); PIN(sel1);
#pragma unroll
      for (int r = 0; r < 16; ++r)
#pragma unroll
        for (int e = 0; e < 8; ++e)
          sm[e] = __builtin_amdgcn_fdot2_f32_bf16(__builtin_bit_cast(bf16x2, (u32)(vm[r][e >> 1] + 0u)), __builtin_bit_cast(bf16x2, (e & 1) ? sel1 : sel0), sm[e], false);
      const bool tail = lane == 52;
      *(LAS u32x4*)(size_t)(tb + lane * 16) = u32x4{pk2(sm[0], sm[1]), pk2(sm[2], sm[3]), tail ? 0u : pk2(sm[4], sm[5]), tail ? 0u : pk2(sm[6], sm[7])};
    }
    waitl0(); bar();
    __builtin_amdgcn_s_setprio(0);
    STAMP(6);
    wo_post(10 + h, 20, 4);
    wo_issue(20 + h, 30, 4);
    lds_spin<2>(lds0 + L_PUB, 4);
  }

  {
    const int rp = wave & 1, kh = wave >> 1;
    const int rt2 = rg >> 1, par2 = rg & 1;
    const u32 blk0 = rg * 4 + rp * 2;
    f32x4 acc[2][5];
#pragma unroll
    for (int r = 0; r < 2; ++r)
#pragma unroll
      for (int c = 0; c < 5; ++c) { acc[r][c] = splat(0.f); PIN(acc[r][c]); }
    u32x4 ofb[10][2], ofa[10][2];
    float gt[2];
    const u32 ob[2] = {WS_O + (blk0 + kh) * 40960 + kh * 10240, WS_O + (blk0 + (kh ^ 1)) * 40960 + kh * 10240};
    const u32 gb[2] = {WS_GATE + (kh * 4 * 128 + blk0 + kh) * 64, WS_GATE + (kh * 4 * 128 + blk0 + (kh ^ 1)) * 64};
    auto ldo = [&](u32 v, int r, int n) { return __builtin_amdgcn_raw_buffer_load_b128(wsr, v + (n & 3) * 1024, ob[r] + (n >> 2) * 4096, 16); };
    auto ldg = [&](u32 v, int r) { return asf(__builtin_amdgcn_raw_buffer_load_b32(wsr, v, gb[r], 16)); };
    const u32 l16 = lane * 16;
    auto load_a = [&]() {
#pragma unroll
      for (int r = 0; r < 2; ++r) gt[r] = ldg(i16 * 4, r);
#pragma unroll
      for (int n = 0; n < 10; ++n)
#pragma unroll
        for (int r = 0; r < 2; ++r) ofa[n][r] = ldo(l16, r, 20 + n);
    };
    const u32 fa = WS_AFLAG + ((rt2 * 4 + par2 * 2 + rp) * 2 + kh) * FSTR + (lane & 3) * 8;
    await(ws, WS_BFLAG + (((rt2 * 2 + par2) * 2 + rp) * 2 + kh) * FSTR + (lane & 7) * 8, tag);
    STAMP(7);
    if (!isB) wo_post(20 + wave, 30, 4);
    u64 av;
#pragma unroll
    for (int n = 0; n < 10; ++n) {
#pragma unroll
      for (int r = 0; r < 2; ++r) ofb[n][r] = ldo(l16, r, n);
      if (n == AVN) { SB(); av = __hip_atomic_load((GAS u64*)(ws + fa), RLX_AGENT); SB(); }
    }
    fence_c(); lds_arrive(lds0 + L_ISS, lane);
    bool early = false;
    u32 oe = OOB, og = OOB, so[3], xw, xr_, tw, trd;
    const u32 xb = wreg(1);
    {
      constexpr int D = 6;
      u32x4 w[D + 1];
      u32 am = 0;
      sfor<-D, 100>([&](auto T) {
        constexpr int t = T.v, n = t + D, gr = n / 25;
        if constexpr (n == 0) STAMP(8);
        if constexpr (n < 100 && n > 0 && n % 25 == 0) {
          if constexpr (n == 50) { fence_c(); stw(lds0 + L_D2 + wave * 4, 5); }
          lds_spin<1>(n == 75 && lane < 4 ? lds0 + L_D2 + lane * 4 : lds0 + L_WC + (2 * gr + kh) * 4, 5);
          am = wreg(gr) + kh * CHUNK + lane * 16; PIN(am);
        }
        if constexpr (t == 4) { early = __all(av == tag); fence_c(); oe = early ? l16 : OOB; og = early ? i16 * 4 : OOB; }
        if constexpr (t == 4 || t == 5) gt[t & 1] = ldg(og, t & 1);
        if constexpr (t >= 6 && t < 46 && !(t & 1)) ofa[(t - 6) >> 2][(t >> 1 & 1) ^ 1] = ldo(oe, (t >> 1 & 1) ^ 1, 20 + ((t - 6) >> 2));
        if constexpr (t >= 80 && t < 83) { const u32 id = (t - 80) * 64 + lane; so[t - 80] = id * 16 + mul24(mul24(id, 6554u) >> 16, 2400u) + (t == 82 ? (lane & 32) << 26 : 0); PIN(so[t - 80]); }
        if constexpr (t == 84) { xw = xb + wave * 5120 + l16; xr_ = xb + (wave ^ 2) * 5120 + l16; PIN(xw); PIN(xr_); }
        if constexpr (t == 85) { trd = xb + 20480 + wave * 2560 + l16; tw = xb + 20480 + wave * 2560 + i16 * 160 + g * 8; PIN(trd); PIN(tw); }
        if constexpr (t == 50) {
          STAMP(9);
          if (!early) { await(ws, fa, tag); load_a(); }
          STAMP(10);
          mma_done();
#pragma unroll
          for (int r = 0; r < 2; ++r)
#pragma unroll
            for (int cc = 0; cc < 5; ++cc) { acc[r][cc] *= 1.f - gt[r]; PIN(acc[r][cc]); }
          asm volatile("s_nop 1");
        }
        SB();
        if constexpr (t >= 0) mmai(acc[0][t % 5], t < 25 ? g0[t % 25] : w[t % (D + 1)], (t < 50 ? ofb : ofa)[t / 5 % 10][0]);
        SB();
        if constexpr (n >= 25 && n < 100) w[n % (D + 1)] = ld128(am + (n % 25) * 1024);
        SB();
        if constexpr (t >= 0) mmai(acc[1][t % 5], t < 25 ? g0[t % 25] : w[t % (D + 1)], (t < 50 ? ofb : ofa)[t / 5 % 10][1]);
        SB();
      });
      mma_done();
    }
    STAMP(11);
#pragma unroll
    for (int c = 0; c < 5; ++c) *(LAS f32x4*)(size_t)(xw + c * 1024) = acc[1][c];
    fence_c(); stw(lds0 + L_D2 + wave * 4, 6);
    const u32 blk = blk0 + kh;
    const rsrc_t outr = __builtin_amdgcn_make_buffer_rsrc(out, 0, 2048 * 1280, 0x00020000);
    u32 obase = ((blk >> 3) * 128 + (blk & 3) * 32 + ((blk >> 2) & 1)) * 1280 + ctile * 160;
    asm volatile("" : "+s"(obase));
    lds_spin<0>(lds0 + L_D2 + (wave ^ 2) * 4, 6);
    f32x4 pv[5];
#pragma unroll
    for (int c = 0; c < 5; ++c) pv[c] = *(LAS f32x4*)(size_t)(xr_ + c * 1024);
    SB();
#pragma unroll
    for (int c = 0; c < 5; ++c) st64(tw + c * 32, pk4(acc[0][c] + pv[c]));
#pragma unroll
    for (int i = 0; i < 3; ++i) __builtin_amdgcn_raw_buffer_store_b128(ld128(trd + i * 1024), outr, so[i], obase, OUT_AUX);
    STAMP(12);
    waitv<0>();
    STAMP(13); STAMP_OUT;
  }
}

extern "C" void kernel_launch(void* const* d_in, const int* in_sizes, int n_in, void* d_out, int out_size, void* d_ws, size_t ws_size,
                              hipStream_t stream) {
  (void)hipFuncSetAttribute((const void*)fused_kernel, hipFuncAttributeMaxDynamicSharedMemorySize, LDS_BYTES);
  fused_kernel<<<256, 512, LDS_BYTES, stream>>>((const char*)d_in[0], (const char*)d_in[1], (const char*)d_in[2], (const char*)d_in[4],
                                                (const char*)d_in[5], (const char*)d_in[6], (char*)d_out, (char*)d_ws);
}
```

```cpp
#include <hip/hip_runtime.h>
#include <hip/hip_bf16.h>

typedef unsigned long long u64;
typedef unsigned u32;
typedef u32 u32x2 __attribute__((ext_vector_type(2)));
typedef u32 u32x4 __attribute__((ext_vector_type(4)));
typedef float f32x2 __attribute__((ext_vector_type(2)));
typedef float f32x4 __attribute__((ext_vector_type(4)));
typedef short s16x4 __attribute__((ext_vector_type(4)));
typedef __bf16 bf16x2 __attribute__((ext_vector_type(2)));
typedef __bf16 bf16x8 __attribute__((ext_vector_type(8)));
#define LAS __attribute__((address_space(3)))
#define GAS __attribute__((address_space(1)))
#define DEV __device__ __forceinline__
#define SB() __builtin_amdgcn_sched_barrier(0)
#define PIN(x) asm volatile("" : "+v"(x))
#define RLX_AGENT __ATOMIC_RELAXED, __HIP_MEMORY_SCOPE_AGENT

#define STAMP_DECL
#define CSTAMP(i)
#define STAMP(i)
#define STAMP_OUT
#define ROT 5
#define TAILG 18
#define A_G0W 10
#define PACK_AT 0
#define A_PW 17
#define TOKS 0
#define AVN 4
#define QGATE 8
#define OUT_AUX 16
#define FB 24
#define FA 18
#define ST_SLEEP 0
#define PACK_TOK 3
#define PACK_R 0
#define QD 6
#define QPRIO 3
#define NOPHI 0
#define OT 1
#define ACV 14
#define AOK 22
#define BCV 19
#define B_R1 19
#define POLL_SLEEP 2

constexpr u32 WS_EPOCH = 0;
#define FSTR 128
constexpr u32 WS_KVFLAG = 4096;
constexpr u32 WS_BFLAG = WS_KVFLAG + 32 * FSTR;
constexpr u32 WS_AFLAG = WS_BFLAG + 128 * FSTR;
constexpr u32 WS_PFLAG = WS_AFLAG + 128 * FSTR;
constexpr u32 WS_GATE = WS_PFLAG + 4096;
constexpr u32 WS_KVP = WS_GATE + 8 * 128 * 64;
constexpr u32 KVP_STRIDE = 896;
constexpr u32 WS_O = WS_KVP + 512 * KVP_STRIDE;
constexpr u32 WS_WOP = WS_O + 128 * 40960;
constexpr u32 WS_WQP = WS_WOP + 1600 * 1024;
constexpr u32 WS_END = WS_WQP + 800 * 1024;
constexpr u64 TAG_STEP = 0x9E3779B97F4A7C15ull;
constexpr u32 SPIN_CAP = 1u << 18;
constexpr u32 OOB = 0x80000000u;

constexpr u32 SEG = 5136;
constexpr u32 BX_SLOT = 18432, BW_SLOT = 15424, L_BX1 = 36864, L_BX2 = L_BX1 + BX_SLOT, L_BW = L_BX2 + BX_SLOT, L_BX0 = L_BW + 4 * BW_SLOT;
constexpr u32 AX_SLOT = 16384, AW_SLOT = 2 * SEG, L_AX = 26624, L_AW = L_AX + 4 * AX_SLOT;
constexpr u32 L_MISC = L_BX0 + BX_SLOT;
constexpr u32 L_HO = L_BW + 2 * BW_SLOT;
constexpr u32 HO_STRIDE = 11264, L_YX = L_MISC;
constexpr u32 L_GATEW = L_MISC, L_TAB = L_MISC + 1280, TAB_STRIDE = 1024;
constexpr u32 L_CTL = L_MISC + 9216;
constexpr u32 L_DONE = L_CTL + 16;
constexpr u32 L_CNT = L_CTL + 64;
constexpr u32 L_WC = L_CTL + 192;
constexpr u32 L_D2 = L_CTL + 224;
constexpr u32 L_ISS = L_CTL + 244;
constexpr u32 L_TOK = L_CTL + 248;
constexpr u32 L_PUB = L_CTL + 240;
constexpr u32 L_SCR = L_CTL + 256;
constexpr u32 CHUNK = 25600;
constexpr u32 LDS_BYTES = 163840;
static_assert(L_BX0 % 128 == 0 && L_HO >= 4 * CHUNK && L_HO + 4 * 11264 <= L_MISC && L_MISC >= 6 * CHUNK && L_AW + 6 * AW_SLOT <= L_MISC && L_SCR + 256 <= LDS_BYTES, "lds map");

template <int V> struct IC { static constexpr int v = V; };
template <int I, int N, class F> DEV void sfor(F&& f) { if constexpr (I < N) { f(IC<I>{}); sfor<I + 1, N>(f); } }
template <int N> DEV void waitv() { asm volatile("s_waitcnt vmcnt(%0)" ::"i"(N) : "memory"); }
DEV void waitl0() { asm volatile("s_waitcnt lgkmcnt(0)" ::: "memory"); }
DEV void bar() { asm volatile("s_barrier" ::: "memory"); }
DEV void fence_c() { asm volatile("" ::: "memory"); }
DEV void glds(const char* sbase, u32 voff, u32 lds) {
  asm volatile("s_mov_b32 m0, %2\n\ts_nop 0\n\tglobal_load_lds_dwordx4 %0, %1" ::"v"(voff), "s"(sbase), "s"(lds));
}
DEV void touch(const char* sbase, u32 voff, u32 lds) {
  asm volatile("s_mov_b32 m0, %2\n\ts_nop 0\n\tglobal_load_lds_dword %0, %1 sc1" ::"v"(voff), "s"(sbase), "s"(lds));
}
DEV void glds4(const char* sbase, u32 v0, u32 v1, u32 v2, u32 v3, u32 lds) {
  asm volatile("s_mov_b32 m0, %5\n\ts_nop 0\n\tglobal_load_lds_dwordx4 %0, %4\n\tglobal_load_lds_dwordx4 %1, %4 offset:1024\n\t"
               "global_load_lds_dwordx4 %2, %4 offset:2048\n\tglobal_load_lds_dwordx4 %3, %4 offset:3072"
               ::"v"(v0), "v"(v1), "v"(v2), "v"(v3), "s"(sbase), "s"(lds));
}
DEV void glds5p(const char* sbase, u32 v, u32 v4, u32 lds) {
  asm volatile("s_mov_b32 m0, %3\n\ts_nop 0\n\tglobal_load_lds_dwordx4 %0, %2 sc1\n\tglobal_load_lds_dwordx4 %0, %2 offset:1024 sc1\n\t"
               "global_load_lds_dwordx4 %0, %2 offset:2048 sc1\n\tglobal_load_lds_dwordx4 %0, %2 offset:3072 sc1\n\t"
               "s_add_u32 m0, %3, 4096\n\ts_nop 0\n\tglobal_load_lds_dwordx4 %1, %2 sc1" ::"v"(v), "v"(v4), "s"(sbase), "s"(lds) : "scc");
}
constexpr int wpos(int p) { return (p / 5) * (int)SEG + (p % 5) * 1024; }
template <int P> DEV void gldsw(const char* sbase, const u32 (&v)[15], u32 lds) {
  if constexpr (P < 12)
    asm volatile("s_add_u32 m0, %5, %6\n\ts_nop 0\n\tglobal_load_lds_dwordx4 %0, %4\n\tglobal_load_lds_dwordx4 %1, %4 offset:%7\n\t"
                 "global_load_lds_dwordx4 %2, %4 offset:%8\n\tglobal_load_lds_dwordx4 %3, %4 offset:%9"
                 ::"v"(v[P]), "v"(v[P + 1]), "v"(v[P + 2]), "v"(v[P + 3]), "s"(sbase), "s"(lds), "i"(wpos(P)), "i"(wpos(P + 1) - wpos(P)),
                   "i"(wpos(P + 2) - wpos(P)), "i"(wpos(P + 3) - wpos(P)) : "scc");
  else
    asm volatile("s_add_u32 m0, %4, %5\n\ts_nop 0\n\tglobal_load_lds_dwordx4 %0, %3\n\tglobal_load_lds_dwordx4 %1, %3 offset:%6\n\t"
                 "global_load_lds_dwordx4 %2, %3 offset:%7"
                 ::"v"(v[P]), "v"(v[P + 1]), "v"(v[P + 2]), "s"(sbase), "s"(lds), "i"(wpos(P)), "i"(wpos(P + 1) - wpos(P)),
                   "i"(wpos(P + 2) - wpos(P)) : "scc");
}
DEV u32x4 ld128(u32 a) { return *(LAS u32x4*)(size_t)a; }
DEV u32x2 ld64(u32 a) { return *(LAS u32x2*)(size_t)a; }
DEV void st64(u32 a, u32x2 v) { *(LAS u32x2*)(size_t)a = v; }
DEV u32x2 ldtr(u32 a) { return __builtin_bit_cast(u32x2, __builtin_amdgcn_ds_read_tr16_b64_v4i16((LAS s16x4*)(size_t)a)); }
DEV u32x4 cat(u32x2 a, u32x2 b) { return __builtin_shufflevector(a, b, 0, 1, 2, 3); }
DEV u32x4 ldB(u32 a0, u32 a1) { return cat(ldtr(a0), ldtr(a1)); }
DEV f32x4 mma(u32x4 a, u32x4 b, f32x4 c) {
  return __builtin_amdgcn_mfma_f32_16x16x32_bf16(__builtin_bit_cast(bf16x8, a), __builtin_bit_cast(bf16x8, b), c, 0, 0, 0);
}
DEV f32x4 mma16(u32x2 a, u32x2 b, f32x4 c) {
  return __builtin_amdgcn_mfma_f32_16x16x16bf16_1k(__builtin_bit_cast(s16x4, a), __builtin_bit_cast(s16x4, b), c, 0, 0, 0);
}
DEV void mmai(f32x4& c, const u32x4& a, const u32x4& b) { asm volatile("v_mfma_f32_16x16x32_bf16 %0, %1, %2, %0" : "+v"(c) : "v"(a), "v"(b)); }
DEV void mma_done() { asm volatile("s_nop 7\n\ts_nop 3"); SB(); }
DEV u32 pk2(float a, float b) { f32x2 v = {a, b}; return __builtin_bit_cast(u32, __builtin_convertvector(v, bf16x2)); }
DEV u32x2 pk4(f32x4 v) { return u32x2{pk2(v[0], v[1]), pk2(v[2], v[3])}; }
DEV f32x4 splat(float v) { return f32x4{v, v, v, v}; }
DEV float fmx(float a, float b) { return __builtin_elementwise_maximum(a, b); }
DEV float phi1(float u) {
  float e = __builtin_amdgcn_fmed3f(__builtin_amdgcn_exp2f(__builtin_fmaf(u, 1.44269504089f, -1.44269504089e-6f)), 0.f, 1.f);
  return fmx(u, 1e-6f) + e;
}
DEV float phi1b(float u, float one) {
  float e = __builtin_amdgcn_fmed3f(__builtin_amdgcn_exp2f(__builtin_fmaf(u, 1.44269504089f, -1.44269504089e-6f)), 0.f, 1.f);
  return __builtin_fmaf(e, one, fmx(u, 1e-6f));
}
DEV f32x4 phi4(f32x4 v) {
  float one = 1.f; asm("" : "+s"(one));
  return f32x4{phi1(v[0]), phi1b(v[1], one), phi1(v[2]), phi1b(v[3], one)};
}
DEV f32x4 exp4(f32x4 u) {
  const f32x4 a = u * 1.44269504089f - 1.44269504089e-6f;
  f32x4 e;
#pragma unroll
  for (int j = 0; j < 4; ++j) e[j] = __builtin_amdgcn_fmed3f(__builtin_amdgcn_exp2f(a[j]), 0.f, 1.f);
  return e;
}
typedef short s16x8 __attribute__((ext_vector_type(8)));
DEV u32x4 relu8(u32x4 v) {
  return __builtin_bit_cast(u32x4, __builtin_elementwise_max(__builtin_bit_cast(s16x8, v), (s16x8)(short)0x3586));
}
DEV float asf(u32 u) { return __builtin_bit_cast(float, u); }
DEV u32 asu(float f) { return __builtin_bit_cast(u32, f); }
DEV void xmax4(float& a, float& b, float& c, float& d, float scale) {
  auto r = __builtin_amdgcn_permlane16_swap(asu(a), asu(b), false, false);
  auto t = __builtin_amdgcn_permlane16_swap(asu(c), asu(d), false, false);
  auto w = __builtin_amdgcn_permlane32_swap(asu(fmx(asf(r[0]), asf(r[1]))), asu(fmx(asf(t[0]), asf(t[1]))), false, false);
  const u32 z = asu(fmx(asf(w[0]), asf(w[1])) * scale);
  auto x = __builtin_amdgcn_permlane32_swap(z, z, false, false);
  auto y0 = __builtin_amdgcn_permlane16_swap(x[0], x[0], false, false);
  auto y1 = __builtin_amdgcn_permlane16_swap(x[1], x[1], false, false);
  a = asf(y0[0]); b = asf(y0[1]); c = asf(y1[0]); d = asf(y1[1]);
}
DEV u32x4 pair_rows(u32x2 v) {
  auto a = __builtin_amdgcn_permlane16_swap(v[0], v[0], false, false);
  auto b = __builtin_amdgcn_permlane16_swap(v[1], v[1], false, false);
  return u32x4{a[0], b[0], a[1], b[1]};
}
DEV u32 odd_rows(u32 x) { auto r = __builtin_amdgcn_permlane16_swap(x, x, false, false); return r[1]; }

template <class F> constexpr u64 lane_mask(F f) { u64 m = 0; for (int l = 0; l < 64; ++l) if (f(l >> 4, l & 15)) m |= 1ull << l; return m; }
#define LM(expr) ({ constexpr u64 m_ = lane_mask([](int g, int i) { return (bool)(expr); }); (bool)__builtin_amdgcn_inverse_ballot_w64(m_); })
typedef __amdgpu_buffer_rsrc_t rsrc_t;
DEV u32x4 gld128(rsrc_t r, u32 off) { return __builtin_amdgcn_raw_buffer_load_b128(r, off, 0, 16); }
DEV u32 gld32(rsrc_t r, u32 off) { return __builtin_amdgcn_raw_buffer_load_b32(r, off, 0, 16); }
DEV void gst128(rsrc_t r, u32 off, u32x4 v) { __builtin_amdgcn_raw_buffer_store_b128(v, r, off, 0, 16); }
DEV void gst64(rsrc_t r, u32 off, u32x2 v) { __builtin_amdgcn_raw_buffer_store_b64(v, r, off, 0, 16); }
DEV void gst32(rsrc_t r, u32 off, u32 v) { __builtin_amdgcn_raw_buffer_store_b32(v, r, off, 0, 16); }
DEV void publish(char* ws, u32 off, u64 tag, int lane) {
  waitv<0>();
  if (lane == 0) __hip_atomic_store((GAS u64*)(ws + off), tag, RLX_AGENT);
}
DEV void await(char* ws, u32 off, u64 tag) {
#pragma nounroll
  for (u32 spins = 0; spins < SPIN_CAP; ++spins) {
    u64 v = __hip_atomic_load((GAS u64*)(ws + off), RLX_AGENT);
    if (__all(v == tag)) break;
    __builtin_amdgcn_s_sleep(POLL_SLEEP);
  }
  fence_c();
}
DEV u32 ldw(u32 a) { return __hip_atomic_load((LAS u32*)(size_t)a, __ATOMIC_RELAXED, __HIP_MEMORY_SCOPE_WORKGROUP); }
DEV void stw(u32 a, u32 v) { __hip_atomic_store((LAS u32*)(size_t)a, v, __ATOMIC_RELAXED, __HIP_MEMORY_SCOPE_WORKGROUP); }
DEV void lds_arrive(u32 a, int lane, u32 n = 1) {
  if (lane == 0) __hip_atomic_fetch_add((LAS u32*)(size_t)a, n, __ATOMIC_RELAXED, __HIP_MEMORY_SCOPE_WORKGROUP);
}
template <int SLEEP> DEV void lds_spin(u32 a, int want) {
#pragma nounroll
  for (u32 spins = 0; spins < (SPIN_CAP << 4); ++spins) {
    if (__all((int)ldw(a) >= want)) break;
    if (SLEEP) __builtin_amdgcn_s_sleep(SLEEP);
  }
  fence_c();
}
template <int SLEEP> DEV void lds_await(bool ok, u32 a, int want) {
  if (__builtin_expect(!ok, 0)) lds_spin<SLEEP>(a, want);
  fence_c();
}
template <int NM> DEV void ilv() {
#pragma unroll
  for (int n = 0; n < NM; ++n) { __builtin_amdgcn_sched_group_barrier(0x8, 1, 0); __builtin_amdgcn_sched_group_barrier(0x80, 1, 0); }
}

typedef u32 u32x16 __attribute__((ext_vector_type(16)));
DEV u32 mul24(u32 a, u32 b) { return __umul24(a, b); }
DEV void w_offsets(int lane, u32 (&ow)[5]) {
#pragma unroll
  for (int i = 0; i < 5; ++i) {
    const u32 c = i * 64 + lane, pos = mul24(c, 6554u) >> 16;
    ow[i] = mul24(pos ^ ((pos >> 1) & 4), 3840u) + c * 16 - mul24(pos, 160u);
  }
}

__global__ __launch_bounds__(512, 2) void fused_kernel(const char*, const char*, const char*, const char*, const char*, const char*, char*, char*) {
  extern __shared__ __attribute__((aligned(16))) char smem[];
  u32x16 ka;
  asm volatile("s_load_dwordx16 %0, %1, 0x0" : "=s"(ka) : "s"(__builtin_amdgcn_kernarg_segment_ptr()));
  STAMP_DECL
  const u32 lds0 = (u32)(size_t)(LAS char*)smem;
  const int b = blockIdx.x, xcd = b & 7, jj = b >> 3;
  const int rt = (xcd & 1) * 8 + (jj & 7), ct = (xcd >> 1) * 4 + (jj >> 3);
  const int r0t = (((jj & 7) * 5 + (jj >> 3) * 10 + (xcd >> 1) * 3 + (xcd & 1) * 2) >> 2), r0 = r0t >= 10 ? r0t - 10 : r0t;
  const bool isB = ct >= 8;
  const int hg = ct & 7;
  const int wave = __builtin_amdgcn_readfirstlane(threadIdx.x >> 6);
  int lane = threadIdx.x & 63, g = lane >> 4, i16 = lane & 15, q4 = (lane >> 2) & 3, p4 = lane & 3;
  auto relane = [&]() {
    asm volatile("v_mbcnt_lo_u32_b32 %0, -1, 0\n\tv_mbcnt_hi_u32_b32 %0, -1, %0" : "=v"(lane));
    g = lane >> 4; i16 = lane & 15; q4 = (lane >> 2) & 3; p4 = lane & 3;
  };
  if (wave == 7) {
    const char* pc; u32 len;
    asm volatile("s_getpc_b64 %0\n.Lpf_here:\n\ts_mov_b32 %1, .Lpf_end-.Lpf_here" : "=s"(pc), "=s"(len));
    const u32 o = (lane * 32 + jj) * 128;
    if (o < len) touch(pc, o, lds0 + L_SCR);
  }
  {
    u32 ow[5];
    u32 o0 = mul24(lane >> 3, 2560u) + (((lane & 7) ^ (lane >> 4)) << 4), o1 = (o0 ^ 64) + 19456, o2 = o0 + 2 * 19456, o3 = o1 + 2 * 19456;
    w_offsets(lane, ow);
    ow[1] -= 1024; ow[2] -= 2048; ow[3] -= 3072;
    PIN(o0); PIN(o1); PIN(o2); PIN(o3); PIN(ow[0]); PIN(ow[1]); PIN(ow[2]); PIN(ow[3]); PIN(ow[4]);
    if (wave == 4 && lane < 60) stw(lds0 + L_DONE + lane * 4, 0);
    const u32 xoff = (128 * rt + 64 * (wave & 1) + (wave >> 1)) * 1280 + r0 * 128, woff = r0 * (64 * 3840) + hg * 160 + (wave + !isB) * 1280;
    asm volatile("s_waitcnt lgkmcnt(0)" : "+s"(ka)::"memory");
    if (wave < 4) {
      const char* xp = (const char*)(((u64)ka[1] << 32) | ka[0]);
      glds4(xp + xoff, o0, o1, o2, o3, lds0 + (isB ? L_BX0 : L_AX) + wave * 4096);
      if (wave < (isB ? 3 : 2)) {
        const char* wp = (const char*)(((u64)(isB ? ka[7] : ka[3]) << 32) | (isB ? ka[6] : ka[2])) + woff;
        const u32 d = lds0 + (isB ? L_BW : L_AW) + wave * SEG;
        glds4(wp, ow[0], ow[1], ow[2], ow[3], d); glds(wp, ow[4], d + 4096);
      } else if (!isB) {
        if (wave == 2 || lane < 16) glds((const char*)(((u64)ka[11] << 32) | ka[10]) + (wave - 2) * 1024, lane * 16, lds0 + L_GATEW + (wave - 2) * 1024);
      } else {
        const char* hp_ = xp + (rt ? 128 * rt - 16 : 0) * 1280 + r0 * 128;
        glds(hp_, o0, lds0 + L_BX0 + 16384); glds(hp_ + 1280, o0 ^ 64, lds0 + L_BX0 + 17408);
      }
    }
    bar();
    if (wave == 7 && lane < 2) touch((const char*)(((u64)ka[15] << 32) | ka[14]), WS_KVFLAG + (b * 2 + lane) * 128, lds0 + L_SCR);
    if (wave == 7) {
      const int cg = (b * 205) >> 13, ks = b - cg * 40, k20 = ks >= 20 ? ks - 20 : ks, sg = (k20 * 52) >> 8, st = k20 - sg * 5;
      const int qn = b - 96, qh = (qn * 205) >> 12;
      const char* so = (const char*)(((u64)(ks < 20 ? ka[9] : ka[5]) << 32) | (ks < 20 ? ka[8] : ka[4])) + ((sg * 160 + (st < 4 ? 40 * st : 16)) * 1280 + cg * 256);
      const char* sq = (const char*)(((u64)ka[3] << 32) | ka[2]) + ((qn - qh * 20) * (32 * 3840) + ((qh * 160) & ~127));
      const u32 r1 = (st < 4 ? 4 : 40) * 1280 - 1024, vo = g * 1280 + i16 * 16, vq = g * 3840 + i16 * 16;
      if (lane < 2) touch((const char*)(((u64)ka[15] << 32) | ka[14]) + WS_EPOCH, lane * 4, lds0 + L_CTL);
      lds_spin<0>(lds0 + L_TOK, PACK_TOK);
      if (b < 200) { glds4(so, vo, vo + r1, vo + 2 * r1, vo + 3 * r1, lds0); glds4(so + 20 * 1280, vo, vo + r1, vo + 2 * r1, vo + 3 * r1, lds0 + 4096); }
      if (b >= 96) {
        glds4(sq, vq, vq + 14336, vq + 2 * 14336, vq + 3 * 14336, lds0 + 8192);
        glds4(sq + 16 * 3840, vq, vq + 14336, vq + 2 * 14336, vq + 3 * 14336, lds0 + 12288);
      }
    }
    if (wave >= 4 && wave < 7) {
      const int u = wave - 3, xs_ = (u + 1) >> 1, pr = (u + 1) & 1;
      const int kw = r0 + (u >> 1) >= 10 ? r0 + (u >> 1) - 10 : r0 + (u >> 1), kx = r0 + xs_ >= 10 ? r0 + xs_ - 10 : r0 + xs_;
      const char* wp = (const char*)(((u64)(isB ? ka[7] : ka[3]) << 32) | (isB ? ka[6] : ka[2])) + ((kw * 64 + (u & 1) * 32) * 3840 + hg * 160);
      u32 d = lds0 + (isB ? L_BW + u * BW_SLOT : L_AW + u * AW_SLOT - SEG);
      asm volatile("" :: "s"(wp), "s"(d));
      if (u > 1) lds_spin<0>(lds0 + L_TOK, u - 1);
#pragma nounroll
      for (int sg = !isB; sg < 3; ++sg) { glds4(wp + sg * 1280, ow[0], ow[1], ow[2], ow[3], d + sg * SEG); glds(wp + sg * 1280, ow[4], d + sg * SEG + 4096); }
      const char* xp = (const char*)(((u64)ka[1] << 32) | ka[0]) + ((128 * rt + pr) * 1280 + kx * 128);
      d = lds0 + (isB ? L_BX1 + (xs_ - 1) * BX_SLOT : L_AX + xs_ * AX_SLOT);
      glds4(xp, o0, o1, o2, o3, d + pr * 8192); glds4(xp + 64 * 1280, o0, o1, o2, o3, d + pr * 8192 + 4096);
      if (isB) glds(xp - (rt ? 16 * 1280 : 0), o0 ^ (pr << 6), d + 16384 + pr * 1024);
      stw(lds0 + L_TOK, u);
      waitv<0>(); stw(lds0 + L_CNT + u * 4, 4);
    }
  }
  auto argp = [&](int n) { return (char*)(((u64)ka[2 * n + 1] << 32) | ka[2 * n]); };
  const char* const X = argp(0); const char* const WA = argp(1); const char* const WoA = argp(2); const char* const WB = argp(3);
  const char* const WoB = argp(4); const char* const GW = argp(5); char* const out = argp(6); char* const ws = argp(7);
  const rsrc_t wsr = __builtin_amdgcn_make_buffer_rsrc(ws, 0, WS_END, 0x00020000);
  const u32 aA0 = i16 * 128 + ((g ^ ((i16 >> 1) & 7)) << 4);
  const u32 rB0 = (8 * g + (q4 ^ (4 * (g & 1)))) * 160, rB1 = (8 * g + ((4 + q4) ^ (4 * (g & 1)))) * 160;
  const int rg = (xcd >> 1) * 8 + (jj & 7), ctile = (xcd & 1) * 4 + (jj >> 3), shr = jj & 7;
  auto wreg = [&](int gr) -> u32 { return lds0 + (isB ? (gr == 3 ? 0 : gr) : (gr < 2 ? 2 : gr - 2)) * (2 * CHUNK); };
  auto wo_issue = [&](int s0, int s1, int st) {
#pragma nounroll
    for (int s = s0; s < s1; s += st) {
      const int gr = (s * 26) >> 8;
      glds5p(ws + WS_WOP + (ctile * 40 + s) * 5120, lane * 16, lane * 16 + 4096, wreg(gr) + (s - gr * 10) * 5120);
    }
  };
  auto out_touch = [&](int w) { if (w < 2) touch(out, ((rg >> 1) * 128 + (rg & 1) + 2 * lane) * 1280 + ctile * 160 + w * 156, lds0 + L_SCR); };
  auto wo_post = [&](int s0, int s1, int st, bool drained = false) {
    if (!drained) waitv<0>();
    const int s = s0 + lane * st;
    if (s < s1) __hip_atomic_fetch_add((LAS u32*)(size_t)(lds0 + L_WC + ((s * 52) >> 8) * 4), 1u, __ATOMIC_RELAXED, __HIP_MEMORY_SCOPE_WORKGROUP);
  };
  u64 tag;

  struct Att { u32 bk, bv, by, ya, mk, mv, my, ob, fo, b0; };
  auto att_prep = [&](int par, int hp, int half) {
    Att t;
    const u32 own = lds0 + L_HO + (hp * 2 + par) * HO_STRIDE + (hp ? 7 : 12) * 512, pi = 2 * (hg & 1) + hp;
    t.ya = lds0 + L_YX + par * 2048 + hp * 64;
    t.bk = half ? own : lds0 + L_HO + hp * 2 * HO_STRIDE + 17 * 512 + par * 64;
    t.bv = half ? own : lds0 + L_HO + (hp * 2 + 1) * HO_STRIDE + 14 * 512 + par * 256;
    t.by = half ? t.ya + 512 : t.bv + 7 * 512 + hp * 64;
    t.ya += half * 1024;
    t.mk = half ? 0x3F : 0x37; t.mv = half ? 0x3F : 0x1F; t.my = half ? 0x37 : 0x17;
    t.ob = WS_O + (rt * 8 + par * 4 + half * 2) * 40960 + (hg >> 1) * 5120 + pi * 256;
    t.fo = WS_BFLAG + (((rt * 2 + par) * 2 + half) * 2 + (hg >> 2)) * FSTR + ((hg & 3) * 2 + hp) * 8 + (pi << 24);
    t.b0 = rt == 0 && !half ? 0xF149F2CAu : 0u;
    asm volatile("" : "+s"(t.bk), "+s"(t.bv), "+s"(t.by), "+s"(t.ya), "+s"(t.mk), "+s"(t.mv), "+s"(t.my), "+s"(t.ob), "+s"(t.fo), "+s"(t.b0));
    return t;
  };
  auto attention = [&](u32x2 (&P)[2][8], const Att& t) {
    u32x2 Q[2][8], K4[2];
    {
      const u32 ya = t.ya + (lane & 0x37) * 8, ak = t.bk + (lane & t.mk) * 8, av = t.bv + (lane & t.mv) * 8;
      Q[0][2] = ld64(ak + 1024); Q[0][3] = ld64(ak + 1536); Q[0][4] = ld64(t.bk + ((lane | 16) & t.mk) * 8 + 2048);
      P[0][7] = ld64(ya); P[1][7] = ld64(ya + 512);
      Q[0][5] = ld64(av + 2560); Q[0][6] = ld64(av + 3072); Q[0][7] = ld64(t.by + (lane & t.my) * 8);
    }
    const u32x2 tg = ld64(lds0 + L_CTL);
    tag = __builtin_bit_cast(u64, tg) + TAG_STEP;
#pragma unroll
    for (int r = 0; r < 2; ++r) K4[r] = u32x2{odd_rows(P[r][4][0]), odd_rows(P[r][4][1])};
#pragma unroll
    for (int f = 2; f < 8; ++f) Q[1][f] = f == 4 ? K4[0] : P[0][f];
    const bool pv[4] = {LM(4 * g >= i + 9), LM(4 * g + 1 >= i + 9), LM(4 * g + 2 >= i + 9), LM(4 * g + 3 >= i + 9)};
    const f32x4 bc = {LM(4 * g <= i && 4 * g >= i - 7) ? 0.f : -1e30f, LM(4 * g + 1 <= i && 4 * g + 1 >= i - 7) ? 0.f : -1e30f,
                      LM(4 * g + 2 <= i && 4 * g + 2 >= i - 7) ? 0.f : -1e30f, LM(4 * g + 3 <= i && 4 * g + 3 >= i - 7) ? 0.f : -1e30f};
    const u32x2 mp = {pv[0] ? 0xFFFFFFFFu : (pv[1] ? 0xFFFF0000u : 0u), pv[2] ? 0xFFFFFFFFu : (pv[3] ? 0xFFFF0000u : 0u)};
    const bool godd = LM(g & 1), glow = LM(g < 2), gq[2] = {LM(g == 0), LM(g == 2)};
    const float cs = 0.22360679775f * 1.44269504089f, b0 = asf(t.b0);
    const u32x2 zero2 = {0u, 0u};
    const f32x4 z4 = splat(0.f);
    const u32x2 ones2 = {0x3F803F80u, 0x3F803F80u};
    f32x4 m[2][2];
    float mx[2][2];
#pragma unroll
    for (int r = 1; r >= 0; --r) {
#pragma unroll
      for (int h = 0; h < 2; ++h) {
        const u32x2 q4m = gq[h] ? P[r][4] : zero2;
        const f32x4 sc = mma16(K4[r], q4m, mma16(P[r][2 + h], P[r][h], bc));
        const f32x4 sp = mma16(Q[r][4], q4m, mma16(Q[r][2 + h], P[r][h], r ? z4 : splat(b0)));
#pragma unroll
        for (int j = 0; j < 4; ++j) m[r][h][j] = pv[j] ? sp[j] : sc[j];
        mx[r][h] = fmx(fmx(m[r][h][0], m[r][h][1]), fmx(m[r][h][2], m[r][h][3]));
      }
    }
    xmax4(mx[1][0], mx[1][1], mx[0][0], mx[0][1], -cs);
    u32x2 y[2];
#pragma unroll
    for (int r = 1; r >= 0; --r) {
      float rs[2];
      f32x4 o[2], yy[2];
#pragma unroll
      for (int h = 0; h < 2; ++h) {
        f32x4 e;
#pragma unroll
        for (int j = 0; j < 4; ++j) e[j] = __builtin_amdgcn_exp2f(__builtin_fmaf(m[r][h][j], cs, mx[r][h]));
        const u32x2 pp = pk4(e);
        const u32x2 p0 = pp & mp, p1 = pp & ~mp;
        rs[h] = __builtin_amdgcn_rcpf(mma16(ones2, pp, z4)[0]);
        o[h] = mma16(Q[r][5 + h], p0, mma16(P[r][5 + h], p1, z4)) * rs[h];
        yy[h] = mma16(Q[r][7], p0, mma16(P[r][7], p1, z4));
      }
      __builtin_amdgcn_raw_buffer_store_b128(cat(pk4(o[0]), pk4(o[1])), wsr, lane * 16, t.ob + (t.fo >> 24) * 768 + r * 40960, 16);
      y[r] = pk4((godd ? yy[1] : yy[0]) * (godd ? rs[1] : rs[0]));
    }
    __builtin_amdgcn_raw_buffer_store_b128(pair_rows(glow ? y[0] : y[1]), wsr, (godd ? OOB : (glow ? 0 : 40960)) + (lane & 15) * 16, t.ob + 4096, 16);
    STAMP(3);
    publish(ws, t.fo & 0xFFFFFF, tag, lane);
    fence_c(); lds_arrive(lds0 + L_PUB, lane);
    STAMP(4);
  };

  if (wave >= 4) {
    const int lw = wave - 4;
    __builtin_amdgcn_s_setprio(3);
    const int xpar = lw & 1;
    const u32 xo = isB && rt ? 16 * 1280 : 0;
    const char* const X0 = X + rt * (128 * 1280) - xo;
    u32 vx[9], vw[15];
    int xst = (lw >> 1) + 1;
    auto rot = [&](int st) { const int k = st + r0; return k >= 10 ? k - 10 : k; };
    auto issue_xp = [&](u32 slot, bool halo, int st, int pr) {
      const char* p = X0 + (rot(st) * 128 + pr * 1280);
      glds4(p, vx[0], vx[1], vx[2], vx[3], slot + pr * 8192); glds4(p, vx[4], vx[5], vx[6], vx[7], slot + pr * 8192 + 4096);
      if (halo) glds(p, vx[8] ^ (pr << 6), slot + 16384 + pr * 1024);
    };
    auto issue_x = [&](u32 slot, bool halo) { issue_xp(slot, halo, xst, xpar); };
    auto setup = [&](bool forB) {
      relane();
      const u32 ox0 = (lane >> 3) * 2560 + (((lane & 7) ^ (lane >> 4)) << 4);
      vx[0] = ox0 + xo; vx[1] = (ox0 ^ 64) + xo + 20480 - 1024; vx[2] = vx[0] + 2 * (20480 - 1024); vx[3] = vx[1] + 2 * (20480 - 1024);
#pragma unroll
      for (int p = 0; p < 4; ++p) vx[4 + p] = vx[p] + 64 * 1280;
      vx[8] = ox0;
      u32 ow[5];
      w_offsets(lane, ow);
#pragma unroll
      for (int p = 0; p < 15; ++p) { if (forB || p < 10) { vw[p] = ow[p % 5] + (p / 5) * 1280 - (wpos(p) - wpos(p & ~3)); PIN(vw[p]); } }
    };
    const char* Wt = (isB ? WB : WA) + hg * 160;
    auto wsrc = [&](int u) { return Wt + (rot(u >> 1) * 64 + (u & 1) * 32) * 3840; };
    const u32 cw = lds0 + L_CNT + (lw + 1) * 4;
    const bool qjob = lw == 2, pjob = qjob ? b >= 96 : (lw == 3 && b < 200);
    auto pack_store = [&]() {
      int jn = b;
      asm volatile("" : "+s"(jn));
      u32x4 fr[8];
      if (qjob) {
        lds_spin<1>(lds0 + L_CNT + 16, 4);
        jn -= 96;
        const u32 a = lds0 + 8192 + (8 * g + q4) * 256 + ((((jn * 205) >> 12) * 160) & 127), a0 = a + p4 * 8, a4 = a + 32 + p4 * 40;
#pragma unroll
        for (int f = 0; f < 5; ++f) fr[f] = f < 4 ? ldB(a0 + f * 40, a0 + f * 40 + 1024) : ldB(a4, a4 + 1024);
#pragma unroll
        for (int f = 0; f < 5; ++f) __builtin_amdgcn_raw_buffer_store_b128(fr[f], wsr, lane * 16 + (f & 3) * 1024, WS_WQP + jn * 5120 + (f >> 2) * 4096, 16);
      } else {
        const int cg = (jn * 205) >> 13, ks = jn - cg * 40, k20 = ks >= 20 ? ks - 20 : ks, sg = (k20 * 52) >> 8;
        const u32 a = lds0 + (4 * g + q4) * 256 + p4 * 8;
#pragma unroll
        for (int f = 0; f < 8; ++f) fr[f] = ldB(a + f * 32, a + f * 32 + 4096);
        const u32 d = WS_WOP + (((ks < 20 ? 0 : 2) + (sg & 1)) * 10 + (sg >> 1) * 5 + k20 - sg * 5) * 5120 + cg * 8192;
#pragma unroll
        for (int f = 0; f < 8; ++f) __builtin_amdgcn_raw_buffer_store_b128(fr[f], wsr, lane * 16, d + f * 1024 + (((cg * 8 + f) * 52) >> 8) * (39 * 5120), 16);
      }
    };
    auto pack_flag = [&]() {
      const u64 t = *(LAS u64*)(size_t)(lds0 + L_CTL) + TAG_STEP;
      if (lane == 0) __hip_atomic_store((GAS u64*)(ws + WS_PFLAG + ((qjob ? 104 : 0) + b) * 8), t, RLX_AGENT);
    };
    auto first_done = [&]() {
      if (OT == 2 || (OT == 3 && !isB)) out_touch(lw);
      if (lw == 3) { waitv<0>(); stw(cw, 4); }
      if (pjob && PACK_R == 0) pack_store();
    };
    auto pack_wait = [&]() {
      const u64 t = *(LAS u64*)(size_t)(lds0 + L_CTL) + TAG_STEP;
      const u32 f0 = WS_PFLAG + (lane < 40 ? ((ctile * 5) >> 3) * 40 + lane : 200 + hg * 20 + (lane < 60 ? lane - 40 : 0)) * 8;
      const u32 f1 = WS_PFLAG + (((ctile * 5 + 4) >> 3) * 40 + (lane < 40 ? lane : 0)) * 8;
#pragma nounroll
      for (u32 spins = 0; spins < SPIN_CAP; ++spins) {
        const u64 v0 = __hip_atomic_load((GAS u64*)(ws + f0), RLX_AGENT), v1 = __hip_atomic_load((GAS u64*)(ws + f1), RLX_AGENT);
        if (__all(v0 == t && v1 == t)) break;
        __builtin_amdgcn_s_sleep(POLL_SLEEP);
      }
      fence_c();
    };
    if (isB) {
      setup(true); STAMP(1); STAMP(2);
      const u32 wd = lds0 + L_BW + ((lw + 1) & 3) * BW_SLOT, da = lds0 + L_DONE + (lane & 3) * 4;
      u32 xs = lds0 + L_BX1 + (lw >> 1) * BX_SLOT;
      auto unit = [&](int u) {
        lds_spin<1>(da, u - 3);
        const char* wp = wsrc(u); gldsw<0>(wp, vw, wd); gldsw<4>(wp, vw, wd); gldsw<8>(wp, vw, wd); gldsw<12>(wp, vw, wd);
        if (u < 19) issue_x(xs, true);
      };
      if (lw == 3) unit(4);
      STAMP(9);
      first_done();
      STAMP(10);
#pragma nounroll
      for (int u = lw + 5; u < 20; u += 4) {
        xst += 2; xs = lds0 + (xs == lds0 + L_BX0 ? L_BX2 : (xs == lds0 + L_BX2 ? L_BX1 : L_BX0));
        unit(u);
        waitv<0>(); stw(lds0 + L_CNT + u * 4, 4);
        if (pjob && u == lw + 1 + 4 * PACK_R) pack_store();
        if (pjob && u == lw + 5 + 4 * PACK_R) pack_flag();
      }
      STAMP(11);
      if (lw == 3) { pack_wait(); stw(lds0 + L_CNT + 23 * 4, 4); }
      lds_spin<1>(lds0 + L_CNT + ((lane & 1) ? TAILG : 23) * 4, 4);
      wo_issue(3 - lw, 7, 4);
      const Att at = att_prep(lw & 1, lw >> 1, (lw >> 1) ^ 1);
      u32x4 park[25];
#pragma unroll
      for (int n = 0; n < 25; ++n)
        park[n] = __builtin_amdgcn_raw_buffer_load_b128(wsr, lane * 16 + (n & 3) * 1024, WS_WOP + (ctile * 8 + 4 + lw) * CHUNK + (n >> 2) * 4096, 16);
      lds_spin<1>(da, 18);
      wo_issue(7 + lw, 20, 4);
      out_touch(lw);
      __builtin_amdgcn_s_setprio(0);
      bar();
      u32x2 P[2][8];
      const u32 hl = lds0 + L_HO + lw * HO_STRIDE + lane * 8;
#pragma unroll
      for (int f = 0; f < 7; ++f) { P[0][f] = ld64(hl + f * 512); P[1][f] = ld64(hl + (7 + f) * 512); }
      attention(P, at);
      wo_post(3 - lw, 7, 4, true); wo_post(7 + lw, 20, 4, true);
      lds_spin<1>(lds0 + L_PUB, 8);
      lds_spin<1>(lds0 + L_D2 + (lane & 3) * 4, 1);
      {
        const u32 a = wreg(2 + (lw >> 1)) + (lw & 1) * CHUNK + lane * 16;
#pragma unroll
        for (int n = 0; n < 25; ++n) *(LAS u32x4*)(size_t)(a + n * 1024) = park[n];
        fence_c(); stw(lds0 + L_WC + (4 + lw) * 4, 5);
      }
    } else {
      setup(false);
      u32 wd = lds0 + L_AW + (lw + 1) * AW_SLOT;
      const u32 da = lds0 + L_DONE + (lane & 7) * 4;
      u32x4 xr[20][2];
      f32x4 gl[2] = {};
      u32 xa0 = lds0 + L_AX + lw * 4096 + i16 * 128 + ((g ^ ((i16 >> 1) & 7)) << 4), xa1 = xa0 ^ 64;
      PIN(xa0); PIN(xa1);
      auto take = [&](int st) {
        lds_spin<1>(lds0 + L_CNT + (st ? 2 * st - (lane & 1) : 0) * 4, 4);
        const u32 o = (st & 3) * AX_SLOT;
        xr[2 * st][0] = ld128(xa0 + o); xr[2 * st][1] = ld128(xa0 + o + 2048);
        xr[2 * st + 1][0] = ld128(xa1 + o); xr[2 * st + 1][1] = ld128(xa1 + o + 2048);
        fence_c(); stw(lds0 + L_DONE + wave * 4, st == 9 ? 30 : 2 * st + 3);
      };
      auto gate_mm = [&](int st) {
        const u32 go = lds0 + L_GATEW + rot(st) * 128 + g * 16;
        const u32x4 g0 = ld128(go), g1 = ld128(go + 64);
        gl[0] = mma(g1, xr[2 * st + 1][0], mma(g0, xr[2 * st][0], gl[0])); gl[1] = mma(g1, xr[2 * st + 1][1], mma(g0, xr[2 * st][1], gl[1]));
      };
      auto wq = [&](int n) { glds5p(ws + WS_WQP + (hg * 20 + rot(n >> 1) * 2 + (n & 1)) * 5120, lane * 16, lane * 16 + 4096, lds0 + n * 5120); };
      stw(lds0 + L_DONE + wave * 4, 1);
#pragma unroll
      for (int j = 0; j < 5; ++j) {
        const int u = 4 * j + lw + 1;
        if (j >= 1) take(2 * j - 2);
        if (j == 0 ? lw == 3 : (j < 4 || lw < 3)) {
          const char* wp = wsrc(u) + 1280;
          asm volatile("" : "+s"(wp));
          if (j) lds_spin<1>(da, u - 5);
          gldsw<0>(wp, vw, wd); gldsw<4>(wp, vw, wd);
          asm volatile("s_add_u32 m0, %3, %4\n\ts_nop 0\n\tglobal_load_lds_dwordx4 %0, %2\n\tglobal_load_lds_dwordx4 %1, %2 offset:1024"
                       ::"v"(vw[8]), "v"(vw[9]), "s"(wp), "s"(wd), "i"(wpos(8)) : "scc");
          if (u < 19) issue_x(lds0 + L_AX + (xst & 3) * AX_SLOT, false);
        }
        xst += 2; wd = wd >= lds0 + L_AW + 2 * AW_SLOT ? wd - 2 * AW_SLOT : wd + 4 * AW_SLOT;
        if (j >= 1) take(2 * j - 1);
        if (j == 4 && lw == 3) { lds_spin<1>(lds0 + L_CNT + A_PW * 4, 4); pack_wait(); }
        if (j == 0) STAMP(9);
        if (j == 0) first_done(); else { waitv<0>(); stw(cw + j * 16, 4); }
        if (j == 0) STAMP(10); if (j == 4) STAMP(11);
        if (j > 0 && j == PACK_R && pjob) pack_store();
        if (j == PACK_R + 1 && pjob) pack_flag();
      }
      take(8); take(9);
      lds_spin<1>(lds0 + L_CNT + 20 * 4, 4);
      wq(lw);
      out_touch(lw);
      lds_spin<1>(da, 20);
      STAMP(12);
#pragma nounroll
      for (int n = 4 + lw; n < 20; n += 4) wq(n);
      wo_issue(3 - lw, 10, 4);
      __builtin_amdgcn_s_setprio(0);
      STAMP(1);
      relane();
#pragma unroll
      for (int st = 0; st < 8; ++st) gate_mm(st);
      gate_mm(8); gate_mm(9);
      float gate[2];
#pragma unroll
      for (int r = 0; r < 2; ++r) { gate[r] = __builtin_amdgcn_rcpf(1.f + __builtin_amdgcn_exp2f(gl[r][0] * -1.44269504089f)); PIN(gate[r]); }
      wo_post(3 - lw, 10, 4); lds_arrive(lds0 + L_CNT + 21 * 4, lane);
      u32x2 pq[2][5];
      {
        f32x4 qa[2][5];
        float pe[4], pm[4];
        auto phi_step = [&](auto F, auto Y) {
          constexpr int y = Y.v, r = y / 18, k = y % 18, j = k & 3;
          if constexpr (y >= 0 && y < 36) {
            if constexpr (k < 4) { pe[j] = __builtin_fmaf(qa[r][F.v][j], 1.44269504089f, -1.44269504089e-6f); PIN(pe[j]); }
            else if constexpr (k < 8) { pe[j] = __builtin_amdgcn_fmed3f(__builtin_amdgcn_exp2f(pe[j]), 0.f, 1.f); PIN(pe[j]); }
            else if constexpr (k < 12) { pm[j] = fmx(qa[r][F.v][j], 1e-6f); PIN(pm[j]); }
            else if constexpr (k < 16) { pm[j] += pe[j]; PIN(pm[j]); }
            else { u32 x = pk2(pm[2 * j], pm[2 * j + 1]); PIN(x); pq[r][F.v][j] = x; }
          }
        };
        u32 q0 = lds0 + lane * 16, q1 = q0 + 61440;
        PIN(q0); PIN(q1);
        constexpr int D = QD;
        u32x4 w[D + 1];
        __builtin_amdgcn_s_setprio(QPRIO);
        sfor<-D, 100>([&](auto T) {
          constexpr int t = T.v, n = t + D, f = t / 20, pf = f - 1;
          if constexpr (n == 0) lds_spin<1>(lds0 + L_CNT + 21 * 4, 8);
          if constexpr (n == 0) { STAMP(6); CSTAMP(14); }
          if constexpr (n % 20 == 0 && n < 100) { qa[0][n / 20] = qa[1][n / 20] = splat(1e-6f); PIN(qa[0][n / 20]); PIN(qa[1][n / 20]); }
          SB();
          if constexpr (t >= 0) mmai(qa[0][f], w[t % (D + 1)], xr[t % 20][0]);
          SB();
          if constexpr (n < 100) w[n % (D + 1)] = ld128((n % 20 >= 12 ? q1 + (n % 20 - 12) * 5120 : q0 + (n % 20) * 5120) + (n / 20) * 1024);
          if constexpr (t >= 20) phi_step(IC<pf>{}, IC<2 * (t % 20) - 4>{});
          SB();
          if constexpr (t >= 0) mmai(qa[1][f], w[t % (D + 1)], xr[t % 20][1]);
          SB();
          if constexpr (t >= 20) phi_step(IC<pf>{}, IC<2 * (t % 20) - 3>{});
        });
        mma_done();
        __builtin_amdgcn_s_setprio(1);
        STAMP(2); CSTAMP(15);
        pq[0][4] = pk4(phi4(qa[0][4])); pq[1][4] = pk4(phi4(qa[1][4])); PIN(pq[0][4]); PIN(pq[1][4]);
      }
      relane();
      u32 ta[4][4];
      {
        const u32 tb = lds0 + L_TAB, tz = tb + 105 * 8, t0 = tb + (64 + i16) * 8, t1 = tb + (80 + p4 + 4 * g) * 8, t2 = tb + (100 + p4) * 8;
#pragma unroll
        for (int h = 0; h < 4; ++h) {
          const bool gh = __builtin_amdgcn_inverse_ballot_w64(0xFFFFull << (16 * h)), eh = __builtin_amdgcn_inverse_ballot_w64(0x000F000F000F000Full << (4 * (h ^ 1)));
          ta[h][0] = gh ? t0 : tz; ta[h][1] = eh ? t1 : tz; ta[h][2] = eh && gh ? t2 : tz; ta[h][3] = gh ? tz - 8 : tz;
#pragma unroll
          for (int i = 0; i < 4; ++i) PIN(ta[h][i]);
        }
      }
      __builtin_amdgcn_s_setprio(3);
      waitl0(); bar();
      STAMP(3);
      tag = *(LAS u64*)(size_t)(lds0 + L_CTL) + TAG_STEP;
      f32x4 n5[2] = {};
      u32x2 ob[2][4];
      float sc5[2] = {0.f, 0.f};
#pragma unroll
      for (int h = 0; h < 4; ++h) {
        const u32 tb = lds0 + L_TAB + h * TAB_STRIDE;
        const u32x2 a0 = ld64(tb + lane * 8), a1 = ld64(ta[h][0] + h * TAB_STRIDE), a5 = ld64(ta[h][1] + h * TAB_STRIDE), a6 = ld64(ta[h][2] + h * TAB_STRIDE);
        const u32x2 z0 = ld64(tb + (96 + g) * 8), z1 = ld64(ta[h][3] + h * TAB_STRIDE);
#pragma unroll
        for (int r = 0; r < 2; ++r) {
          const f32x4 den = mma16(z1, pq[r][4], mma16(z0, pq[r][h], splat(1e-6f))), num = mma16(a1, pq[r][4], mma16(a0, pq[r][h], splat(0.f)));
          n5[r] = mma16(a6, pq[r][4], mma16(a5, pq[r][h], n5[r]));
          const float sc = gate[r] * __builtin_amdgcn_rcpf(den[0]);
          ob[r][h] = pk4(num * sc);
          sc5[r] = __builtin_amdgcn_inverse_ballot_w64(0xFFFFull << (16 * (h ^ 1))) ? sc : sc5[r];
        }
      }
#pragma unroll
      for (int r = 0; r < 2; ++r) {
        const u32 blk = rt * 8 + lw * 2 + r;
        const u32 base = WS_O + blk * 40960 + 20480 + (hg >> 1) * 5120;
        gst128(wsr, base + (hg & 1) * 2048 + lane * 16, cat(ob[r][0], ob[r][1]));
        gst128(wsr, base + (hg & 1) * 2048 + 1024 + lane * 16, cat(ob[r][2], ob[r][3]));
        const u32x4 o5 = pair_rows(pk4(n5[r] * sc5[r]));
        gst128(wsr, (g & 1) ? OOB : base + 4096 + ((2 * (hg & 1) + (g >> 1)) * 16 + i16) * 16, u32x4{o5[2], o5[3], o5[0], o5[1]});
      }
      gst32(wsr, lane < 32 ? WS_GATE + (hg * 128 + rt * 8 + lw * 2) * 64 + lane * 4 : OOB, asu(lane < 16 ? gate[0] : gate[1]));
      STAMP(5);
      fence_c(); lds_arrive(lds0 + L_PUB, lane);
      publish(ws, WS_AFLAG + ((rt * 4 + lw) * 2 + (hg >> 2)) * FSTR + (hg & 3) * 8, tag, lane);
      STAMP(4);
    }
    if (!isB)
    {
      lds_spin<2>(lds0 + L_ISS, 4);
      wo_issue(30 + lw, 40, 4);
      wo_post(30 + lw, 40, 4);
    }
    STAMP(8);
    if (b == 0 && lw == 0) {
      const u32 fo = ((lane >> 3) * 8 + ((lane >> 2) & 1)) * FSTR;
#pragma nounroll
      for (u32 spins = 0; spins < SPIN_CAP; ++spins) {
        bool ok = true;
#pragma unroll
        for (int r = 0; r < 2; ++r) {
          ok &= __hip_atomic_load((GAS u64*)(ws + WS_BFLAG + fo + r * 64 * FSTR + (lane & 3) * 16), RLX_AGENT) == tag;
          ok &= __hip_atomic_load((GAS u64*)(ws + WS_AFLAG + fo + r * 64 * FSTR + (lane & 3) * 8), RLX_AGENT) == tag;
        }
        if (__all(ok)) break;
        __builtin_amdgcn_s_sleep(8);
      }
      if (lane == 0) __hip_atomic_store((GAS u64*)(ws + WS_EPOCH), tag, RLX_AGENT);
    }
    STAMP(13); STAMP_OUT;
    asm volatile(".Lpf_end:");
    return;
  }

  {
  }
  auto get_tag = [&]() {
    const u32x2 t = ld64(lds0 + L_CTL);
    tag = ((u64)(u32)__builtin_amdgcn_readfirstlane(t[1]) << 32) | (u32)__builtin_amdgcn_readfirstlane(t[0]);
    tag += TAG_STEP;
  };
  u32x4 g0[25];
  auto take_g0 = [&]() {
    lds_spin<1>(lds0 + L_WC + (wave >> 1) * 4, 5);
    const u32 a = wreg(0) + (wave >> 1) * CHUNK + lane * 16;
#pragma unroll
    for (int n = 0; n < 25; ++n) g0[n] = ld128(a + n * 1024);
    fence_c(); stw(lds0 + L_D2 + wave * 4, 1);
  };
  u32 ca = lds0 + L_CNT, dcount = 1;
  u32 dn = lds0 + L_DONE + wave * 4;
  PIN(dn);
  if (isB) {
    const int par = wave & 1, hp = wave >> 1;
    u32 bM0 = lds0 + L_BW + rB0 + hp * 80 + p4 * 8, bM1 = bM0 - rB0 + rB1;
    u32 bX0 = lds0 + L_BW + rB0 + hp * 80 + 32 + (p4 & 1) * SEG + (p4 >> 1) * 40, bX1 = bX0 - rB0 + rB1;
    u32 bY0 = lds0 + L_BW + rB0 + 2 * SEG + 32 + p4 * 40, bY1 = bY0 - rB0 + rB1;
    u32 alo = aA0 + par * 8192 + hp * 4096, ahi = aA0 + par * 8192 + (hp ^ 1) * 4096;
    PIN(bM0); PIN(bM1); PIN(bX0); PIN(bX1); PIN(bY0); PIN(bY1); PIN(alo); PIN(ahi); PIN(ca); PIN(dcount);
    const Att at = att_prep(par, hp, hp);
    f32x4 acc[4][7], accy[2], acch[3];
#pragma unroll
    for (int f = 0; f < 3; ++f) { acch[f] = splat(0.f); PIN(acch[f]); }
#pragma unroll
    for (int r = 0; r < 4; ++r)
#pragma unroll
      for (int f = 0; f < 7; ++f) { acc[r][f] = splat(0.f); PIN(acc[r][f]); }
    accy[0] = accy[1] = splat(0.f); PIN(accy[0]); PIN(accy[1]);
    struct Fr { u32x4 a[4]; u32x2 w[8][2]; };
    u32 xs = lds0 + L_BX0, x0 = xs + alo, x1 = xs + ahi;
    const u32 hoff = 16384 - par * 8192 - hp * 4096;
    u32x4 ah[2];
    auto rd1 = [&](Fr& F, int sl, int n) {
      const u32 o = sl * BW_SLOT;
      if (n < 4) { F.a[n] = ld128((n < 2 ? x0 : x1) + (n & 1) * 2048); return; }
      const int f = (n - 4) >> 1, t = n & 1;
      if (f == 4) F.w[4][t] = ldtr((t ? bX1 : bX0) + o);
      else if (f == 5) F.w[7][t] = ldtr((t ? bY1 : bY0) + o);
      else { const int ff = f < 4 ? f : f - 2; F.w[f < 4 ? f : f - 1][t] = ldtr((t ? bM1 : bM0) + o + (ff >> 1) * SEG + (ff & 1) * 40); }
    };
    auto mm1 = [&](Fr& F, int n) {
      if (n >= 20 && n < 22) { mmai(accy[n - 20], F.a[n - 20], cat(F.w[7][0], F.w[7][1])); return; }
      const int m = n < 20 ? n : n - 2, f = m >> 2, r = m & 3;
      const u32x4 w = cat(F.w[f][0], F.w[f][1]);
      if (f < 5) mmai(acc[r][f], w, F.a[r]); else mmai(acc[r][f], F.a[r], w);
    };
    Fr F0, F1;
    waitv<0>(); lds_arrive(lds0 + L_CNT, lane);
    lds_spin<0>(ca, 4);
    STAMP(1); CSTAMP(14);
#pragma unroll
    for (int n = 0; n < 20; ++n) rd1(F0, 0, n);
    ah[0] = ld128(x0 + hoff);
    fence_c(); stw(dn, dcount);
    bool ok = false;
    x0 = xs + (alo ^ 64); x1 = xs + (ahi ^ 64);
    auto unit = [&](Fr& Fc, Fr& Fn, int k, bool last) {
      u32 cv;
#pragma unroll
      for (int n = 0; n < 30; ++n) {
        SB(); mm1(Fc, n); SB();
        if (n == 3) {
          asm volatile("s_cmp_lg_u32 %10, 0\n\ts_cbranch_scc1 .Lhv%=\n\t"
                       "v_mfma_f32_16x16x32_bf16 %0, %4, %3, %0\n\tv_mfma_f32_16x16x32_bf16 %1, %5, %3, %1\n\tv_mfma_f32_16x16x32_bf16 %2, %6, %3, %2\n\t"
                       "s_branch .Lhe%=\n.Lhv%=:\n\t"
                       "v_mfma_f32_16x16x32_bf16 %0, %3, %7, %0\n\tv_mfma_f32_16x16x32_bf16 %1, %3, %8, %1\n\tv_mfma_f32_16x16x32_bf16 %2, %3, %9, %2\n.Lhe%=:"
                       : "+v"(acch[0]), "+v"(acch[1]), "+v"(acch[2])
                       : "v"(ah[k & 1]), "v"(cat(Fc.w[2][0], Fc.w[2][1])), "v"(cat(Fc.w[3][0], Fc.w[3][1])), "v"(cat(Fc.w[4][0], Fc.w[4][1])),
                         "v"(cat(Fc.w[5][0], Fc.w[5][1])), "v"(cat(Fc.w[6][0], Fc.w[6][1])), "v"(cat(Fc.w[7][0], Fc.w[7][1])), "s"(par) : "scc");
          SB();
          if (!last) lds_await<0>(ok, ca + (k + 1) * 4, 4);
        }
        if (n == BCV) cv = ldw(ca + (k + 2) * 4);
        if (n >= 4 && n < 24) rd1(Fn, (k + 1) & 3, n - 4);
        if (n == 10) ++dcount;
        if (n == 24) ah[(k + 1) & 1] = ld128(x0 + hoff);
        if (n == 25) { fence_c(); stw(dn, dcount); }
        if (n == 28) ok = __all(cv >= 4);
        if (n == 26) { if (!(k & 1)) xs = lds0 + (xs == lds0 + L_BX0 ? L_BX1 : (xs == lds0 + L_BX1 ? L_BX2 : L_BX0)); x0 = (k & 1) ? x0 ^ 64 : xs + alo; PIN(x0); }
        if (n == 27) { x1 = (k & 1) ? x1 ^ 64 : xs + ahi; PIN(x1); }
      }
      SB();
    };
#pragma nounroll
    for (int it = 0; it < 5; ++it) {
      unit(F0, F1, 0, false); unit(F1, F0, 1, false); unit(F0, F1, 2, false); unit(F1, F0, 3, it == 4);
      ca += 16;
    }
    mma_done();
    STAMP(2); CSTAMP(15);
    relane();
    u32x2 P[2][8];
    {
      const u32 dd = lds0 + L_DONE + (lane & 3) * 4;
      const int dv = ldw(dd);
      u32x2 H[2][7];
#pragma unroll
      for (int f = 0; f < 7; ++f) { H[0][f] = pk4(acc[2][f]); H[1][f] = pk4(acc[3][f]); PIN(H[0][f]); PIN(H[1][f]); }
      lds_await<0>(__all(dv >= 20), dd, 20);
      const u32 hl = lds0 + L_HO + wave * HO_STRIDE + lane * 8, yl = lds0 + L_YX + (par * 4 + 2 * hp) * 512 + lane * 8;
#pragma unroll
      for (int f = 0; f < 7; ++f) { st64(hl + f * 512, H[0][f]); st64(hl + (7 + f) * 512, H[1][f]); }
#pragma unroll
      for (int f = 2; f < 7; ++f) { P[1][f] = pk4(acc[1][f]); st64(hl + (12 + f) * 512, P[1][f]); }
#pragma unroll
      for (int f = 0; f < 3; ++f) st64(hl + (19 + f) * 512, pk4(acch[f]));
      st64(yl, pk4(accy[0])); st64(yl + 512, pk4(accy[1]));
    }
    STAMP(5);
    waitl0(); bar();
    STAMP(6);
#pragma unroll
    for (int f = 0; f < 7; ++f) P[0][f] = pk4(acc[0][f]);
    P[1][0] = pk4(acc[1][0]); P[1][1] = pk4(acc[1][1]);
    attention(P, at);
    take_g0();
    lds_spin<1>(lds0 + L_PUB, 8);
  } else {
    const int h = wave;
    u32 bK0 = lds0 + L_AW + rB0 + h * 40 + p4 * 8, bK1 = bK0 - rB0 + rB1;
    u32 bZ0 = lds0 + L_AW + rB0 + h * 40 + 32 + (p4 == (h ^ 1) ? SEG : 0), bZ1 = bZ0 - rB0 + rB1;
    PIN(bK0); PIN(bK1); PIN(bZ0); PIN(bZ1); PIN(ca); PIN(dcount);
    f32x4 acc[8][3];
#pragma unroll
    for (int r = 0; r < 8; ++r)
#pragma unroll
      for (int f = 0; f < 3; ++f) { acc[r][f] = splat(f == 0 ? 1e-6f : (f == 1 || q4 == (h ^ 1) ? 0.f : 1e-6f)); PIN(acc[r][f]); }
    struct Fr { u32x4 a[8]; u32x2 w[3][2]; };
    u32 xs = 0, xa = lds0 + L_AX + aA0, x0 = xa, wo_ = 0, k0 = bK0, k1 = bK1, z0_ = bZ0, z1_ = bZ1;
    PIN(xa);
    auto rd1 = [&](Fr& F, int sl, int n) {
      if (n >= 6) { F.a[n - 6] = ld128(x0 + (n - 6) * 2048); return; }
      const int f = n >> 1, t = n & 1;
      F.w[f][t] = f < 2 ? ldtr((t ? k1 : k0) + f * SEG) : ldtr(t ? z1_ : z0_);
    };
    Fr F0, F1;
    waitv<0>(); lds_arrive(lds0 + L_CNT, lane);
    lds_spin<0>(ca, 4);
    STAMP(1); CSTAMP(14);
#pragma unroll
    for (int n = 0; n < 14; ++n) rd1(F0, 0, n);
    fence_c(); stw(dn, dcount);
    bool ok = false;
    x0 ^= 64; wo_ = AW_SLOT; k0 += AW_SLOT; k1 += AW_SLOT; z0_ += AW_SLOT; z1_ += AW_SLOT;
    auto unit = [&](Fr& Fc, Fr& Fn, int k, bool last) {
      u32 cv;
#pragma unroll
      for (int n = 0; n < 24; ++n) {
        SB(); mmai(acc[n / 3][n % 3], Fc.a[n / 3], cat(Fc.w[n % 3][0], Fc.w[n % 3][1])); SB();
        if (n == 2 && !last) lds_await<0>(ok, ca + (k + 1) * 4, 4);
        if (n == ACV) cv = ldw(ca + (k + 2) * 4);
        if (n >= 2 && n < 14) rd1(Fn, 0, (n & 1) ? 6 + (n - 3) / 2 : (n - 2) / 2);
        if (n == 15 || n == 17) rd1(Fn, 0, 12 + (n - 15) / 2);
        if (n == 10) ++dcount;
        if (n == 18) { fence_c(); stw(dn, dcount); }
        if (n == AOK) ok = __all(cv >= 4);
        if (n == 20) { if (!(k & 1)) xs = (xs + AX_SLOT) & (4 * AX_SLOT - 1); x0 = (k & 1) ? x0 ^ 64 : xs + xa; PIN(x0); }
        if (n == 18) { wo_ = wo_ == 5 * AW_SLOT ? 0 : wo_ + AW_SLOT; k0 = bK0 + wo_; PIN(k0); }
        if (n == 21) { k1 = bK1 + wo_; PIN(k1); }
        if (n == 22) { z0_ = bZ0 + wo_; PIN(z0_); }
        if (n == 23) { z1_ = bZ1 + wo_; PIN(z1_); }
      }
      SB();
    };
#pragma nounroll
    for (int it = 0; it < 5; ++it) {
      unit(F0, F1, 0, false); unit(F1, F0, 1, false); unit(F0, F1, 2, false); unit(F1, F0, 3, it == 4);
      ca += 16;
    }
    mma_done();
    STAMP(2); CSTAMP(15);
    relane(); get_tag();
    __builtin_amdgcn_s_setprio(2);
    const bool eh = q4 == (h ^ 1), gh = g == h, zc = i16 == 4 * h;
    const u32x4 ones = {0x3F803F80u, 0x3F803F80u, 0x3F803F80u, 0x3F803F80u};
    f32x4 t00 = {}, t01 = {}, t10 = {}, t11 = {};
#pragma unroll
    for (int k = 0; k < 4; ++k) {
      const u32x4 ek = cat(pk4(exp4(acc[2 * k][0])), pk4(exp4(acc[2 * k + 1][0]))), ez = cat(pk4(exp4(acc[2 * k][2])), pk4(exp4(acc[2 * k + 1][2])));
      const u32x4 zz = cat(pk4(acc[2 * k][2]), pk4(acc[2 * k + 1][2]));
      const u32x4 rk = relu8(cat(pk4(acc[2 * k][0]), pk4(acc[2 * k + 1][0]))), rz = relu8(zz);
      const u32x4 vv = cat(pk4(acc[2 * k][1]), pk4(acc[2 * k + 1][1])), zb = eh ? zz : ones;
      t00 = mma(ek, vv, mma(rk, vv, t00)); t01 = mma(ek, zb, mma(rk, zb, t01));
      t10 = mma(ez, vv, mma(rz, vv, t10)); t11 = mma(ez, zb, mma(rz, zb, t11));
    }
    const u32 pb = WS_KVP + (hg * 4 + h) * 16 * KVP_STRIDE, tb = lds0 + L_TAB + h * TAB_STRIDE;
    st64(tb + lane * 8, pk4(t00));
    st64(tb + (gh ? 64 + i16 : 106) * 8, pk4(t10));
    st64(tb + (eh ? 80 + p4 + 4 * g : (zc ? 96 + g : 106)) * 8, pk4(t01));
    st64(tb + (gh && eh ? 100 + p4 : (gh && zc ? 104 : 105)) * 8, pk4(t11));
    const u32 po = lane < 53 ? pb + lane * 16 : OOB;
    gst128(wsr, (lane < 56 ? pb + lane * 16 : OOB) + rt * KVP_STRIDE, ld128(tb + lane * 16));
    lds_arrive(lds0 + L_CNT + 21 * 4, lane);
    STAMP(3);
    publish(ws, WS_KVFLAG + (hg * 4 + h) * FSTR + rt * 8, tag, lane);
    STAMP(4);
    __builtin_amdgcn_s_setprio(0);
    take_g0();
    lds_spin<1>(lds0 + L_D2 + (lane & 3) * 4, 1);
    wo_issue(10 + h, 20, 4);
    await(ws, WS_KVFLAG + (hg * 4 + h) * FSTR + i16 * 8, tag);
    __builtin_amdgcn_s_setprio(2);
    STAMP(5);
    {
      u32x4 vm[16];
#pragma unroll
      for (int r = 0; r < 16; ++r) vm[r] = __builtin_amdgcn_raw_buffer_load_b128(wsr, po, r * KVP_STRIDE, 16);
      float sm[8] = {};
      u32 sel0 = 0x00003F80u, sel1 = 0x3F800000u;
      PIN(sel0); PIN(sel1);
#pragma unroll
      for (int r = 0; r < 16; ++r)
#pragma unroll
        for (int e = 0; e < 8; ++e)
          sm[e] = __builtin_amdgcn_fdot2_f32_bf16(__builtin_bit_cast(bf16x2, (u32)(vm[r][e >> 1] + 0u)), __builtin_bit_cast(bf16x2, (e & 1) ? sel1 : sel0), sm[e], false);
      const bool tail = lane == 52;
      *(LAS u32x4*)(size_t)(tb + lane * 16) = u32x4{pk2(sm[0], sm[1]), pk2(sm[2], sm[3]), tail ? 0u : pk2(sm[4], sm[5]), tail ? 0u : pk2(sm[6], sm[7])};
    }
    waitl0(); bar();
    __builtin_amdgcn_s_setprio(0);
    STAMP(6);
    wo_post(10 + h, 20, 4);
    wo_issue(20 + h, 30, 4);
    lds_spin<2>(lds0 + L_PUB, 4);
  }

  {
    const int rp = wave & 1, kh = wave >> 1;
    const int rt2 = rg >> 1, par2 = rg & 1;
    const u32 blk0 = rg * 4 + rp * 2;
    f32x4 acc[2][5];
#pragma unroll
    for (int r = 0; r < 2; ++r)
#pragma unroll
      for (int c = 0; c < 5; ++c) { acc[r][c] = splat(0.f); PIN(acc[r][c]); }
    u32x4 ofb[10][2], ofa[10][2];
    float gt[2];
    const u32 ob[2] = {WS_O + (blk0 + kh) * 40960 + kh * 10240, WS_O + (blk0 + (kh ^ 1)) * 40960 + kh * 10240};
    const u32 gb[2] = {WS_GATE + (kh * 4 * 128 + blk0 + kh) * 64, WS_GATE + (kh * 4 * 128 + blk0 + (kh ^ 1)) * 64};
    auto ldo = [&](u32 v, int r, int n) { return __builtin_amdgcn_raw_buffer_load_b128(wsr, v + (n & 3) * 1024, ob[r] + (n >> 2) * 4096, 16); };
    auto ldg = [&](u32 v, int r) { return asf(__builtin_amdgcn_raw_buffer_load_b32(wsr, v, gb[r], 16)); };
    const u32 l16 = lane * 16;
    auto load_a = [&]() {
#pragma unroll
      for (int r = 0; r < 2; ++r) gt[r] = ldg(i16 * 4, r);
#pragma unroll
      for (int n = 0; n < 10; ++n)
#pragma unroll
        for (int r = 0; r < 2; ++r) ofa[n][r] = ldo(l16, r, 20 + n);
    };
    const u32 fa = WS_AFLAG + ((rt2 * 4 + par2 * 2 + rp) * 2 + kh) * FSTR + (lane & 3) * 8;
    await(ws, WS_BFLAG + (((rt2 * 2 + par2) * 2 + rp) * 2 + kh) * FSTR + (lane & 7) * 8, tag);
    STAMP(7);
    if (!isB) wo_post(20 + wave, 30, 4);
    u64 av;
#pragma unroll
    for (int n = 0; n < 10; ++n) {
#pragma unroll
      for (int r = 0; r < 2; ++r) ofb[n][r] = ldo(l16, r, n);
      if (n == AVN) { SB(); av = __hip_atomic_load((GAS u64*)(ws + fa), RLX_AGENT); SB(); }
    }
    fence_c(); lds_arrive(lds0 + L_ISS, lane);
    bool early = false;
    u32 oe = OOB, og = OOB, so[3], xw, xr_, tw, trd;
    const u32 xb = wreg(1);
    {
      constexpr int D = 6;
      u32x4 w[D + 1];
      u32 am = 0;
      sfor<-D, 100>([&](auto T) {
        constexpr int t = T.v, n = t + D, gr = n / 25;
        if constexpr (n == 0) STAMP(8);
        if constexpr (n < 100 && n > 0 && n % 25 == 0) {
          if constexpr (n == 50) { fence_c(); stw(lds0 + L_D2 + wave * 4, 5); }
          lds_spin<1>(n == 75 && lane < 4 ? lds0 + L_D2 + lane * 4 : lds0 + L_WC + (2 * gr + kh) * 4, 5);
          am = wreg(gr) + kh * CHUNK + lane * 16; PIN(am);
        }
        if constexpr (t == 4) { early = __all(av == tag); fence_c(); oe = early ? l16 : OOB; og = early ? i16 * 4 : OOB; }
        if constexpr (t == 4 || t == 5) gt[t & 1] = ldg(og, t & 1);
        if constexpr (t >= 6 && t < 46 && !(t & 1)) ofa[(t - 6) >> 2][(t >> 1 & 1) ^ 1] = ldo(oe, (t >> 1 & 1) ^ 1, 20 + ((t - 6) >> 2));
        if constexpr (t >= 80 && t < 83) { const u32 id = (t - 80) * 64 + lane; so[t - 80] = id * 16 + mul24(mul24(id, 6554u) >> 16, 2400u) + (t == 82 ? (lane & 32) << 26 : 0); PIN(so[t - 80]); }
        if constexpr (t == 84) { xw = xb + wave * 5120 + l16; xr_ = xb + (wave ^ 2) * 5120 + l16; PIN(xw); PIN(xr_); }
        if constexpr (t == 85) { trd = xb + 20480 + wave * 2560 + l16; tw = xb + 20480 + wave * 2560 + i16 * 160 + g * 8; PIN(trd); PIN(tw); }
        if constexpr (t == 50) {
          STAMP(9);
          if (!early) { await(ws, fa, tag); load_a(); }
          STAMP(10);
          mma_done();
#pragma unroll
          for (int r = 0; r < 2; ++r)
#pragma unroll
            for (int cc = 0; cc < 5; ++cc) { acc[r][cc] *= 1.f - gt[r]; PIN(acc[r][cc]); }
          asm volatile("s_nop 1");
        }
        SB();
        if constexpr (t >= 0) mmai(acc[0][t % 5], t < 25 ? g0[t % 25] : w[t % (D + 1)], (t < 50 ? ofb : ofa)[t / 5 % 10][0]);
        SB();
        if constexpr (n >= 25 && n < 100) w[n % (D + 1)] = ld128(am + (n % 25) * 1024);
        SB();
        if constexpr (t >= 0) mmai(acc[1][t % 5], t < 25 ? g0[t % 25] : w[t % (D + 1)], (t < 50 ? ofb : ofa)[t / 5 % 10][1]);
        SB();
      });
      mma_done();
    }
    STAMP(11);
#pragma unroll
    for (int c = 0; c < 5; ++c) *(LAS f32x4*)(size_t)(xw + c * 1024) = acc[1][c];
    fence_c(); stw(lds0 + L_D2 + wave * 4, 6);
    const u32 blk = blk0 + kh;
    const rsrc_t outr = __builtin_amdgcn_make_buffer_rsrc(out, 0, 2048 * 1280, 0x00020000);
    u32 obase = ((blk >> 3) * 128 + (blk & 3) * 32 + ((blk >> 2) & 1)) * 1280 + ctile * 160;
    asm volatile("" : "+s"(obase));
    lds_spin<0>(lds0 + L_D2 + (wave ^ 2) * 4, 6);
    f32x4 pv[5];
#pragma unroll
    for (int c = 0; c < 5; ++c) pv[c] = *(LAS f32x4*)(size_t)(xr_ + c * 1024);
    SB();
#pragma unroll
    for (int c = 0; c < 5; ++c) st64(tw + c * 32, pk4(acc[0][c] + pv[c]));
#pragma unroll
    for (int i = 0; i < 3; ++i) __builtin_amdgcn_raw_buffer_store_b128(ld128(trd + i * 1024), outr, so[i], obase, OUT_AUX);
    STAMP(12);
    STAMP(13); STAMP_OUT;
  }
}

extern "C" void kernel_launch(void* const* d_in, const int* in_sizes, int n_in, void* d_out, int out_size, void* d_ws, size_t ws_size,
                              hipStream_t stream) {
  (void)hipFuncSetAttribute((const void*)fused_kernel, hipFuncAttributeMaxDynamicSharedMemorySize, LDS_BYTES);
  fused_kernel<<<256, 512, LDS_BYTES, stream>>>((const char*)d_in[0], (const char*)d_in[1], (const char*)d_in[2], (const char*)d_in[4],
                                                (const char*)d_in[5], (const char*)d_in[6], (char*)d_out, (char*)d_ws);
}
```

```cpp
#include <hip/hip_runtime.h>
#include <hip/hip_bf16.h>

typedef unsigned long long u64;
typedef unsigned u32;
typedef u32 u32x2 __attribute__((ext_vector_type(2)));
typedef u32 u32x4 __attribute__((ext_vector_type(4)));
typedef float f32x2 __attribute__((ext_vector_type(2)));
typedef float f32x4 __attribute__((ext_vector_type(4)));
typedef short s16x4 __attribute__((ext_vector_type(4)));
typedef __bf16 bf16x2 __attribute__((ext_vector_type(2)));
typedef __bf16 bf16x8 __attribute__((ext_vector_type(8)));
#define LAS __attribute__((address_space(3)))
#define GAS __attribute__((address_space(1)))
#define DEV __device__ __forceinline__
#define SB() __builtin_amdgcn_sched_barrier(0)
#define PIN(x) asm volatile("" : "+v"(x))
#define RLX_AGENT __ATOMIC_RELAXED, __HIP_MEMORY_SCOPE_AGENT

constexpr u32 WS_EPOCH = 0;
constexpr u32 WS_KVFLAG = 4096;
constexpr u32 WS_BFLAG = WS_KVFLAG + 32 * 128;
constexpr u32 WS_AFLAG = WS_BFLAG + 128 * 128;
constexpr u32 WS_PFLAG = WS_AFLAG + 128 * 128;
constexpr u32 WS_GATE = WS_PFLAG + 4096;
constexpr u32 WS_KVP = WS_GATE + 8 * 128 * 64;
constexpr u32 KVP_STRIDE = 896;
constexpr u32 WS_O = WS_KVP + 512 * KVP_STRIDE;
constexpr u32 WS_WOP = WS_O + 128 * 40960;
constexpr u32 WS_END = WS_WOP + 1600 * 1024;
constexpr u64 TAG_STEP = 0x9E3779B97F4A7C15ull;
constexpr u32 SPIN_CAP = 1u << 18;
constexpr u32 OOB = 0x80000000u;

constexpr u32 SEG = 5136;
constexpr u32 BX_SLOT = 18432, BW_SLOT = 15424, L_BX1 = 36864, L_BX2 = L_BX1 + BX_SLOT, L_BW = L_BX2 + BX_SLOT, L_BX0 = L_BW + 4 * BW_SLOT;
constexpr u32 AX_SLOT = 16384, AW_SLOT = 2 * SEG, L_AX = 26624, L_AW = L_AX + 4 * AX_SLOT;
constexpr u32 L_MISC = L_BX0 + BX_SLOT;
constexpr u32 L_HO = L_BW + 2 * BW_SLOT, HO_STRIDE = 4096;
constexpr u32 L_YX = L_MISC;
constexpr u32 L_GATEW = L_MISC, L_TAB = L_MISC + 2560, TAB_STRIDE = 1024;
constexpr u32 L_GL = L_TAB + 5 * TAB_STRIDE;
constexpr u32 L_CTL = L_MISC + 9216;
constexpr u32 L_DONE = L_CTL + 16;
constexpr u32 L_CNT = L_CTL + 64;
constexpr u32 L_WC = L_CTL + 192;
constexpr u32 L_D2 = L_CTL + 224;
constexpr u32 L_TOK = L_CTL + 248;
constexpr u32 L_PUB = L_CTL + 240;
constexpr u32 L_SCR = L_CTL + 256;
constexpr u32 CHUNK = 25600;
constexpr u32 LDS_BYTES = 163840;
static_assert(L_BX0 % 128 == 0 && L_HO >= 4 * CHUNK && L_HO + 4 * HO_STRIDE <= L_MISC && L_MISC >= 6 * CHUNK && L_AW + 6 * AW_SLOT <= L_MISC && L_SCR + 256 <= LDS_BYTES, "lds map");

template <int V> struct IC { static constexpr int v = V; };
template <int I, int N, class F> DEV void sfor(F&& f) { if constexpr (I < N) { f(IC<I>{}); sfor<I + 1, N>(f); } }
template <int N> DEV void waitv() { asm volatile("s_waitcnt vmcnt(%0)" ::"i"(N) : "memory"); }
DEV void bar() { asm volatile("s_barrier" ::: "memory"); }
DEV void fence_c() { asm volatile("" ::: "memory"); }
DEV void glds(const char* sbase, u32 voff, u32 lds) {
  asm volatile("s_mov_b32 m0, %2\n\ts_nop 0\n\tglobal_load_lds_dwordx4 %0, %1" ::"v"(voff), "s"(sbase), "s"(lds));
}
DEV void touch(const char* sbase, u32 voff, u32 lds) {
  asm volatile("s_mov_b32 m0, %2\n\ts_nop 0\n\tglobal_load_lds_dword %0, %1 sc1" ::"v"(voff), "s"(sbase), "s"(lds));
}
DEV void glds4(const char* sbase, u32 v0, u32 v1, u32 v2, u32 v3, u32 lds) {
  asm volatile("s_mov_b32 m0, %5\n\ts_nop 0\n\tglobal_load_lds_dwordx4 %0, %4\n\tglobal_load_lds_dwordx4 %1, %4 offset:1024\n\t"
               "global_load_lds_dwordx4 %2, %4 offset:2048\n\tglobal_load_lds_dwordx4 %3, %4 offset:3072"
               ::"v"(v0), "v"(v1), "v"(v2), "v"(v3), "s"(sbase), "s"(lds));
}
DEV void glds5p(const char* sbase, u32 v, u32 v4, u32 lds) {
  asm volatile("s_mov_b32 m0, %3\n\ts_nop 0\n\tglobal_load_lds_dwordx4 %0, %2 sc1\n\tglobal_load_lds_dwordx4 %0, %2 offset:1024 sc1\n\t"
               "global_load_lds_dwordx4 %0, %2 offset:2048 sc1\n\tglobal_load_lds_dwordx4 %0, %2 offset:3072 sc1\n\t"
               "s_add_u32 m0, %3, 4096\n\ts_nop 0\n\tglobal_load_lds_dwordx4 %1, %2 sc1" ::"v"(v), "v"(v4), "s"(sbase), "s"(lds) : "scc");
}
constexpr int wpos(int p) { return (p / 5) * (int)SEG + (p % 5) * 1024; }
template <int P> DEV void gldsw(const char* sbase, const u32 (&v)[15], u32 lds) {
  if constexpr (P < 12)
    asm volatile("s_add_u32 m0, %5, %6\n\ts_nop 0\n\tglobal_load_lds_dwordx4 %0, %4\n\tglobal_load_lds_dwordx4 %1, %4 offset:%7\n\t"
                 "global_load_lds_dwordx4 %2, %4 offset:%8\n\tglobal_load_lds_dwordx4 %3, %4 offset:%9"
                 ::"v"(v[P]), "v"(v[P + 1]), "v"(v[P + 2]), "v"(v[P + 3]), "s"(sbase), "s"(lds), "i"(wpos(P)), "i"(wpos(P + 1) - wpos(P)),
                   "i"(wpos(P + 2) - wpos(P)), "i"(wpos(P + 3) - wpos(P)) : "scc");
  else
    asm volatile("s_add_u32 m0, %4, %5\n\ts_nop 0\n\tglobal_load_lds_dwordx4 %0, %3\n\tglobal_load_lds_dwordx4 %1, %3 offset:%6\n\t"
                 "global_load_lds_dwordx4 %2, %3 offset:%7"
                 ::"v"(v[P]), "v"(v[P + 1]), "v"(v[P + 2]), "s"(sbase), "s"(lds), "i"(wpos(P)), "i"(wpos(P + 1) - wpos(P)),
                   "i"(wpos(P + 2) - wpos(P)) : "scc");
}
DEV u32x4 ld128(u32 a) { return *(LAS u32x4*)(size_t)a; }
DEV u32x2 ld64(u32 a) { return *(LAS u32x2*)(size_t)a; }
DEV u32x2 ld64v(u32 a) { return *(volatile LAS u32x2*)(size_t)a; }
DEV void st64(u32 a, u32x2 v) { *(LAS u32x2*)(size_t)a = v; }
DEV u32x2 ldtr(u32 a) { return __builtin_bit_cast(u32x2, __builtin_amdgcn_ds_read_tr16_b64_v4i16((LAS s16x4*)(size_t)a)); }
DEV u32x4 cat(u32x2 a, u32x2 b) { return __builtin_shufflevector(a, b, 0, 1, 2, 3); }
DEV u32x4 ldB(u32 a0, u32 a1) { return cat(ldtr(a0), ldtr(a1)); }
DEV f32x4 mma(u32x4 a, u32x4 b, f32x4 c) {
  return __builtin_amdgcn_mfma_f32_16x16x32_bf16(__builtin_bit_cast(bf16x8, a), __builtin_bit_cast(bf16x8, b), c, 0, 0, 0);
}
DEV f32x4 mma16(u32x2 a, u32x2 b, f32x4 c) {
  return __builtin_amdgcn_mfma_f32_16x16x16bf16_1k(__builtin_bit_cast(s16x4, a), __builtin_bit_cast(s16x4, b), c, 0, 0, 0);
}
DEV void mmai(f32x4& c, const u32x4& a, const u32x4& b) { asm volatile("v_mfma_f32_16x16x32_bf16 %0, %1, %2, %0" : "+v"(c) : "v"(a), "v"(b)); }
DEV void mma_done() { asm volatile("s_nop 7\n\ts_nop 3"); SB(); }
DEV u32 pk2(float a, float b) { f32x2 v = {a, b}; return __builtin_bit_cast(u32, __builtin_convertvector(v, bf16x2)); }
DEV u32x2 pk4(f32x4 v) { return u32x2{pk2(v[0], v[1]), pk2(v[2], v[3])}; }
DEV f32x4 splat(float v) { return f32x4{v, v, v, v}; }
DEV float fmx(float a, float b) { return __builtin_elementwise_maximum(a, b); }
DEV float phi1(float u) {
  float e = __builtin_amdgcn_fmed3f(__builtin_amdgcn_exp2f(__builtin_fmaf(u, 1.44269504089f, -1.44269504089e-6f)), 0.f, 1.f);
  return fmx(u, 1e-6f) + e;
}
DEV f32x4 phi4(f32x4 v) {
  return f32x4{phi1(v[0]), phi1(v[1]), phi1(v[2]), phi1(v[3])};
}
DEV f32x4 exp4(f32x4 u) {
  const f32x4 a = u * 1.44269504089f - 1.44269504089e-6f;
  f32x4 e;
#pragma unroll
  for (int j = 0; j < 4; ++j) e[j] = __builtin_amdgcn_fmed3f(__builtin_amdgcn_exp2f(a[j]), 0.f, 1.f);
  return e;
}
typedef short s16x8 __attribute__((ext_vector_type(8)));
DEV u32x4 relu8(u32x4 v) {
  return __builtin_bit_cast(u32x4, __builtin_elementwise_max(__builtin_bit_cast(s16x8, v), (s16x8)(short)0x3586));
}
DEV float asf(u32 u) { return __builtin_bit_cast(float, u); }
DEV u32 asu(float f) { return __builtin_bit_cast(u32, f); }
DEV void xmax4(float& a, float& b, float& c, float& d, float scale) {
  auto r = __builtin_amdgcn_permlane16_swap(asu(a), asu(b), false, false);
  auto t = __builtin_amdgcn_permlane16_swap(asu(c), asu(d), false, false);
  auto w = __builtin_amdgcn_permlane32_swap(asu(fmx(asf(r[0]), asf(r[1]))), asu(fmx(asf(t[0]), asf(t[1]))), false, false);
  const u32 z = asu(fmx(asf(w[0]), asf(w[1])) * scale);
  auto x = __builtin_amdgcn_permlane32_swap(z, z, false, false);
  auto y0 = __builtin_amdgcn_permlane16_swap(x[0], x[0], false, false);
  auto y1 = __builtin_amdgcn_permlane16_swap(x[1], x[1], false, false);
  a = asf(y0[0]); b = asf(y0[1]); c = asf(y1[0]); d = asf(y1[1]);
}
DEV u32x4 pair_rows(u32x2 v) {
  auto a = __builtin_amdgcn_permlane16_swap(v[0], v[0], false, false);
  auto b = __builtin_amdgcn_permlane16_swap(v[1], v[1], false, false);
  return u32x4{a[0], b[0], a[1], b[1]};
}
DEV u32 odd_rows(u32 x) { auto r = __builtin_amdgcn_permlane16_swap(x, x, false, false); return r[1]; }

template <class F> constexpr u64 lane_mask(F f) { u64 m = 0; for (int l = 0; l < 64; ++l) if (f(l >> 4, l & 15)) m |= 1ull << l; return m; }
#define LM(expr) ({ constexpr u64 m_ = lane_mask([](int g, int i) { return (bool)(expr); }); (bool)__builtin_amdgcn_inverse_ballot_w64(m_); })
typedef __amdgpu_buffer_rsrc_t rsrc_t;
DEV void gst128(rsrc_t r, u32 off, u32x4 v) { __builtin_amdgcn_raw_buffer_store_b128(v, r, off, 0, 16); }
DEV void gst32(rsrc_t r, u32 off, u32 v) { __builtin_amdgcn_raw_buffer_store_b32(v, r, off, 0, 16); }
DEV void publish(char* ws, u32 off, u64 tag, int lane) {
  waitv<0>();
  if (lane == 0) __hip_atomic_store((GAS u64*)(ws + off), tag, RLX_AGENT);
}
DEV void await(char* ws, u32 off, u64 tag) {
#pragma nounroll
  for (u32 spins = 0; spins < SPIN_CAP; ++spins) {
    u64 v = __hip_atomic_load((GAS u64*)(ws + off), RLX_AGENT);
    if (__all(v == tag)) break;
    __builtin_amdgcn_s_sleep(2);
  }
  fence_c();
}
DEV u32 ldw(u32 a) { return __hip_atomic_load((LAS u32*)(size_t)a, __ATOMIC_RELAXED, __HIP_MEMORY_SCOPE_WORKGROUP); }
DEV void stw(u32 a, u32 v) { __hip_atomic_store((LAS u32*)(size_t)a, v, __ATOMIC_RELAXED, __HIP_MEMORY_SCOPE_WORKGROUP); }
DEV void lds_arrive(u32 a, int lane, u32 n = 1) {
  if (lane == 0) __hip_atomic_fetch_add((LAS u32*)(size_t)a, n, __ATOMIC_RELAXED, __HIP_MEMORY_SCOPE_WORKGROUP);
}
template <int SLEEP> DEV void lds_spin(u32 a, int want) {
#pragma nounroll
  for (u32 spins = 0; spins < (SPIN_CAP << 4); ++spins) {
    if (__all((int)ldw(a) >= want)) break;
    if (SLEEP) __builtin_amdgcn_s_sleep(SLEEP);
  }
  fence_c();
}
template <int SLEEP> DEV void lds_await(bool ok, u32 a, int want) {
  if (__builtin_expect(!ok, 0)) lds_spin<SLEEP>(a, want);
  fence_c();
}
typedef u32 u32x16 __attribute__((ext_vector_type(16)));
DEV u32 mul24(u32 a, u32 b) { return __umul24(a, b); }
DEV void w_offsets(int lane, u32 (&ow)[5]) {
#pragma unroll
  for (int i = 0; i < 5; ++i) {
    const u32 c = i * 64 + lane, pos = mul24(c, 6554u) >> 16;
    ow[i] = mul24(pos ^ ((pos >> 1) & 4), 3840u) + c * 16 - mul24(pos, 160u);
  }
}

__global__ __launch_bounds__(512, 2) void fused_kernel(const char*, const char*, const char*, const char*, const char*, const char*, char*, char*) {
  extern __shared__ __attribute__((aligned(16))) char smem[];
  u32x16 ka;
  asm volatile("s_load_dwordx16 %0, %1, 0x0" : "=s"(ka) : "s"(__builtin_amdgcn_kernarg_segment_ptr()));
  const u32 lds0 = (u32)(size_t)(LAS char*)smem;
  const int b = blockIdx.x, xcd = b & 7, jj = b >> 3;
  const int rt = (xcd & 1) * 8 + (jj & 7), ct = (xcd >> 1) * 4 + (jj >> 3);
  const int r0t = (((jj & 7) * 5 + (jj >> 3) * 10 + (xcd >> 1) * 3 + (xcd & 1) * 2) >> 2), r0 = r0t >= 10 ? r0t - 10 : r0t;
  const bool isB = ct >= 8;
  const int hg = ct & 7;
  const int wave = __builtin_amdgcn_readfirstlane(threadIdx.x >> 6);
  int lane = threadIdx.x & 63, g = lane >> 4, i16 = lane & 15, q4 = (lane >> 2) & 3, p4 = lane & 3;
  auto relane = [&]() {
    asm volatile("v_mbcnt_lo_u32_b32 %0, -1, 0\n\tv_mbcnt_hi_u32_b32 %0, -1, %0" : "=v"(lane));
    g = lane >> 4; i16 = lane & 15; q4 = (lane >> 2) & 3; p4 = lane & 3;
  };
  const int job = !isB ? -1 : (wave == 7 ? hg * 16 + rt : (wave == 6 && rt < 9 ? 128 + hg * 9 + rt : -1));
  const u32 jl = lds0 + (wave & 1) * 8192;
  if (wave == 7) {
    const char* pc; u32 len;
    asm volatile("s_getpc_b64 %0\n.Lpf_here:\n\ts_mov_b32 %1, .Lpf_end-.Lpf_here" : "=s"(pc), "=s"(len));
    const u32 o = (lane * 32 + jj) * 128;
    if (o < len) touch(pc, o, lds0 + L_SCR);
  }
  {
    u32 ow[5];
    u32 o0 = mul24(lane >> 3, 2560u) + (((lane & 7) ^ (lane >> 4)) << 4), o1 = (o0 ^ 64) + 19456, o2 = o0 + 2 * 19456, o3 = o1 + 2 * 19456;
    w_offsets(lane, ow);
    ow[1] -= 1024; ow[2] -= 2048; ow[3] -= 3072;
    PIN(o0); PIN(o1); PIN(o2); PIN(o3); PIN(ow[0]); PIN(ow[1]); PIN(ow[2]); PIN(ow[3]); PIN(ow[4]);
    if (wave == 4 && lane < 60) stw(lds0 + L_DONE + lane * 4, 0);
    const u32 xoff = (128 * rt + 64 * (wave & 1) + (wave >> 1)) * 1280 + r0 * 128, woff = r0 * (64 * 3840) + hg * 160 + (wave + !isB) * 1280;
    asm volatile("s_waitcnt lgkmcnt(0)" : "+s"(ka)::"memory");
    const char* pWoA = (const char*)(((u64)ka[5] << 32) | ka[4]); const char* pWoB = (const char*)(((u64)ka[9] << 32) | ka[8]);
    asm volatile("" : "+s"(pWoA), "+s"(pWoB));
    if (wave < 4) {
      const char* xp = (const char*)(((u64)ka[1] << 32) | ka[0]);
      glds4(xp + xoff, o0, o1, o2, o3, lds0 + (isB ? L_BX0 : L_AX) + wave * 4096);
      if (wave < (isB ? 3 : 2)) {
        const char* wp = (const char*)(((u64)(isB ? ka[7] : ka[3]) << 32) | (isB ? ka[6] : ka[2])) + woff;
        const u32 d = lds0 + (isB ? L_BW : L_AW) + wave * SEG;
        glds4(wp, ow[0], ow[1], ow[2], ow[3], d); glds(wp, ow[4], d + 4096);
      } else if (!isB) {
        const char* gp = (const char*)(((u64)ka[11] << 32) | ka[10]);
        glds(gp, lane * 16, lds0 + L_GATEW + (wave - 2) * 1280);
        if (lane < 16) glds(gp + 1024, lane * 16, lds0 + L_GATEW + (wave - 2) * 1280 + 1024);
      } else {
        const char* hp_ = xp + (rt ? 128 * rt - 16 : 0) * 1280 + r0 * 128;
        glds(hp_, o0, lds0 + L_BX0 + 16384); glds(hp_ + 1280, o0 ^ 64, lds0 + L_BX0 + 17408);
      }
    }
    bar();
    if (wave >= 4) {
      const int u = wave - 3, xs_ = (u + 1) >> 1, pr = (u + 1) & 1;
      const int kw = r0 + (u >> 1) >= 10 ? r0 + (u >> 1) - 10 : r0 + (u >> 1), kx = r0 + xs_ >= 10 ? r0 + xs_ - 10 : r0 + xs_;
      const char* wp = (const char*)(((u64)(isB ? ka[7] : ka[3]) << 32) | (isB ? ka[6] : ka[2])) + ((kw * 64 + (u & 1) * 32) * 3840 + hg * 160);
      u32 d = lds0 + (isB ? L_BW + (u & 3) * BW_SLOT : L_AW + u * AW_SLOT - SEG);
      asm volatile("" :: "s"(wp), "s"(d));
      if (u > 1) lds_spin<0>(lds0 + L_TOK, u - 1);
      if (u == 4 && isB) lds_spin<0>(lds0 + L_DONE + (lane & 3) * 4, 1);
#pragma nounroll
      for (int sg = !isB; sg < 3; ++sg) { glds4(wp + sg * 1280, ow[0], ow[1], ow[2], ow[3], d + sg * SEG); glds(wp + sg * 1280, ow[4], d + sg * SEG + 4096); }
      const char* xp = (const char*)(((u64)ka[1] << 32) | ka[0]) + ((128 * rt + pr) * 1280 + kx * 128);
      d = lds0 + (isB ? L_BX1 + (xs_ - 1) * BX_SLOT : L_AX + xs_ * AX_SLOT);
      glds4(xp, o0, o1, o2, o3, d + pr * 8192); glds4(xp + 64 * 1280, o0, o1, o2, o3, d + pr * 8192 + 4096);
      if (isB) glds(xp - (rt ? 16 * 1280 : 0), o0 ^ (pr << 6), d + 16384 + pr * 1024);
      stw(lds0 + L_TOK, u);
      {
        if (wave == 4 && lane < 2) touch((const char*)(((u64)ka[15] << 32) | ka[14]) + WS_EPOCH, lane * 4, lds0 + L_CTL);
        if (job >= 0) {
          const int cg = (job * 205) >> 13, ks = job - cg * 40, k20 = ks >= 20 ? ks - 20 : ks, sg = (k20 * 52) >> 8, st = k20 - sg * 5;
          const char* src = (ks < 20 ? pWoB : pWoA) + ((sg * 160 + (st < 4 ? 40 * st : 16)) * 1280 + cg * 256);
          const u32 v = g * 1280 + i16 * 16, r1 = (st < 4 ? 4 : 40) * 1280 - 1024;
          glds4(src, v, v + r1, v + 2 * r1, v + 3 * r1, jl); glds4(src + 20 * 1280, v, v + r1, v + 2 * r1, v + 3 * r1, jl + 4096);
        }
      }
      if (wave == 4) { waitv<1>(); stw(lds0 + L_CNT + 4, 4); }
    }
  }
  auto argp = [&](int n) { return (char*)(((u64)ka[2 * n + 1] << 32) | ka[2 * n]); };
  const char* const X = argp(0); const char* const WA = argp(1); const char* const WB = argp(3);
  char* const out = argp(6); char* const ws = argp(7);
  const rsrc_t wsr = __builtin_amdgcn_make_buffer_rsrc(ws, 0, WS_END, 0x00020000);
  const u32 aA0 = i16 * 128 + ((g ^ ((i16 >> 1) & 7)) << 4);
  const u32 rB0 = (8 * g + (q4 ^ (4 * (g & 1)))) * 160, rB1 = (8 * g + ((4 + q4) ^ (4 * (g & 1)))) * 160;
  const int rg = (xcd >> 1) * 8 + (jj & 7), ctile = (xcd & 1) * 4 + (jj >> 3), shr = jj & 7;
  auto wreg = [&](int gr) -> u32 { return lds0 + (isB ? (gr == 3 ? 0 : gr) : (gr < 2 ? 2 : gr - 2)) * (2 * CHUNK); };
  auto wo_issue = [&](int s0, int s1, int st) {
    const int cnt = (s1 - s0 + 3) >> 2;
    int s = s0 + ((shr * cnt) >> 3) * st;
#pragma nounroll
    for (int it = 0; it < cnt; ++it, s = s + st >= s1 ? s0 : s + st) {
      const int gr = (s * 26) >> 8;
      glds5p(ws + WS_WOP + (ctile * 40 + s) * 5120, lane * 16, lane * 16 + 4096, wreg(gr) + (s - gr * 10) * 5120);
    }
  };
  auto out_touch = [&](int w) { if (w < 2) touch(out, ((rg >> 1) * 128 + (rg & 1) + 2 * lane) * 1280 + ctile * 160 + w * 156, lds0 + L_SCR); };
  auto wo_post = [&](int s0, int s1, int st, bool drained = false) {
    if (!drained) waitv<0>();
    const int s = s0 + lane * st;
    if (s < s1) __hip_atomic_fetch_add((LAS u32*)(size_t)(lds0 + L_WC + ((s * 52) >> 8) * 4), 1u, __ATOMIC_RELAXED, __HIP_MEMORY_SCOPE_WORKGROUP);
  };
  u64 tag;

  auto wo_wait = [&]() {
    const u64 t = *(LAS u64*)(size_t)(lds0 + L_CTL) + TAG_STEP;
    const u32 f0 = WS_PFLAG + (lane < 40 ? lane : 0) * 8;
#pragma nounroll
    for (u32 spins = 0; spins < SPIN_CAP; ++spins) {
      const u64 v0 = __hip_atomic_load((GAS u64*)(ws + (f0 + ((ctile * 5) >> 3) * 320)), RLX_AGENT);
      const u64 v1 = __hip_atomic_load((GAS u64*)(ws + (f0 + ((ctile * 5 + 4) >> 3) * 320)), RLX_AGENT);
      if (__all(v0 == t && v1 == t)) break;
      __builtin_amdgcn_s_sleep(2);
    }
    fence_c(); stw(lds0 + L_CNT + 23 * 4, 4);
  };
  struct Att { u32 bk, bv, by, ya, mk, mv, my, ob, fo, b0; };
  auto att_prep = [&](int par, int hp, int half) {
    Att t;
    const u32 own = lds0 + L_HO + (hp * 2 + par) * HO_STRIDE - 2 * 512, pi = 2 * (hg & 1) + hp;
    t.ya = lds0 + L_YX + par * 2048 + hp * 64;
    t.bk = half ? own : lds0 + L_HO + hp * 2 * HO_STRIDE + 3 * 512 + par * 64;
    t.bv = half ? own : lds0 + L_HO + (hp * 2 + 1) * HO_STRIDE + par * 256;
    t.by = half ? t.ya + 512 : t.bv + 7 * 512 + hp * 64;
    t.ya += half * 1024;
    t.mk = half ? 0x3F : 0x37; t.mv = half ? 0x3F : 0x1F; t.my = half ? 0x37 : 0x17;
    t.ob = WS_O + (rt * 8 + par * 4 + half * 2) * 40960 + (hg >> 1) * 5120 + pi * 256;
    t.fo = WS_BFLAG + (((rt * 2 + par) * 2 + half) * 2 + (hg >> 2)) * 128 + ((hg & 3) * 2 + hp) * 8 + (pi << 24);
    t.b0 = rt == 0 && !half ? 0xF149F2CAu : 0u;
    asm volatile("" : "+s"(t.bk), "+s"(t.bv), "+s"(t.by), "+s"(t.ya), "+s"(t.mk), "+s"(t.mv), "+s"(t.my), "+s"(t.ob), "+s"(t.fo), "+s"(t.b0));
    return t;
  };
  auto attention = [&](u32x2 (&P)[2][8], const Att& t) {
    u32x2 Q[2][8], K4[2];
    {
      const u32 ya = t.ya + (lane & 0x37) * 8, ak = t.bk + (lane & t.mk) * 8, av = t.bv + (lane & t.mv) * 8;
      Q[0][2] = ld64(ak + 1024); Q[0][3] = ld64(ak + 1536); Q[0][4] = ld64(t.bk + ((lane | 16) & t.mk) * 8 + 2048);
      P[0][7] = ld64(ya); P[1][7] = ld64(ya + 512);
      Q[0][5] = ld64(av + 2560); Q[0][6] = ld64(av + 3072); Q[0][7] = ld64(t.by + (lane & t.my) * 8);
    }
    const u32x2 tg = ld64(lds0 + L_CTL);
    tag = __builtin_bit_cast(u64, tg) + TAG_STEP;
#pragma unroll
    for (int r = 0; r < 2; ++r) K4[r] = u32x2{odd_rows(P[r][4][0]), odd_rows(P[r][4][1])};
#pragma unroll
    for (int f = 2; f < 8; ++f) Q[1][f] = f == 4 ? K4[0] : P[0][f];
    const bool pv[4] = {LM(4 * g >= i + 9), LM(4 * g + 1 >= i + 9), LM(4 * g + 2 >= i + 9), LM(4 * g + 3 >= i + 9)};
    const f32x4 bc = {LM(4 * g <= i && 4 * g >= i - 7) ? 0.f : -1e30f, LM(4 * g + 1 <= i && 4 * g + 1 >= i - 7) ? 0.f : -1e30f,
                      LM(4 * g + 2 <= i && 4 * g + 2 >= i - 7) ? 0.f : -1e30f, LM(4 * g + 3 <= i && 4 * g + 3 >= i - 7) ? 0.f : -1e30f};
    const u32x2 mp = {pv[0] ? 0xFFFFFFFFu : (pv[1] ? 0xFFFF0000u : 0u), pv[2] ? 0xFFFFFFFFu : (pv[3] ? 0xFFFF0000u : 0u)};
    const bool godd = LM(g & 1), glow = LM(g < 2), gq[2] = {LM(g == 0), LM(g == 2)};
    const float cs = 0.22360679775f * 1.44269504089f, b0 = asf(t.b0);
    const u32x2 zero2 = {0u, 0u};
    const f32x4 z4 = splat(0.f);
    const u32x2 ones2 = {0x3F803F80u, 0x3F803F80u};
    f32x4 m[2][2];
    float mx[2][2];
#pragma unroll
    for (int r = 1; r >= 0; --r) {
#pragma unroll
      for (int h = 0; h < 2; ++h) {
        const u32x2 q4m = gq[h] ? P[r][4] : zero2;
        const f32x4 sc = mma16(K4[r], q4m, mma16(P[r][2 + h], P[r][h], bc));
        const f32x4 sp = mma16(Q[r][4], q4m, mma16(Q[r][2 + h], P[r][h], r ? z4 : splat(b0)));
#pragma unroll
        for (int j = 0; j < 4; ++j) m[r][h][j] = pv[j] ? sp[j] : sc[j];
        mx[r][h] = fmx(fmx(m[r][h][0], m[r][h][1]), fmx(m[r][h][2], m[r][h][3]));
      }
    }
    xmax4(mx[1][0], mx[1][1], mx[0][0], mx[0][1], -cs);
    u32x2 y[2];
#pragma unroll
    for (int r = 1; r >= 0; --r) {
      float rs[2];
      f32x4 o[2], yy[2];
#pragma unroll
      for (int h = 0; h < 2; ++h) {
        f32x4 e;
#pragma unroll
        for (int j = 0; j < 4; ++j) e[j] = __builtin_amdgcn_exp2f(__builtin_fmaf(m[r][h][j], cs, mx[r][h]));
        const u32x2 pp = pk4(e);
        const u32x2 p0 = pp & mp, p1 = pp & ~mp;
        rs[h] = __builtin_amdgcn_rcpf(mma16(ones2, pp, z4)[0]);
        o[h] = mma16(Q[r][5 + h], p0, mma16(P[r][5 + h], p1, z4)) * rs[h];
        yy[h] = mma16(Q[r][7], p0, mma16(P[r][7], p1, z4));
      }
      gst128(wsr, lane * 16 + (t.ob + (t.fo >> 24) * 768 + r * 40960), cat(pk4(o[0]), pk4(o[1])));
      y[r] = pk4((godd ? yy[1] : yy[0]) * (godd ? rs[1] : rs[0]));
    }
    gst128(wsr, (godd ? OOB : (glow ? 0 : 40960)) + (lane & 15) * 16 + (t.ob + 4096), pair_rows(glow ? y[0] : y[1]));
  };

  if (wave >= 4) {
    const int lw = wave - 4;
    __builtin_amdgcn_s_setprio(3);
    const int xpar = lw & 1;
    const u32 xo = isB && rt ? 16 * 1280 : 0;
    const char* const X0 = X + rt * (128 * 1280) - xo;
    u32 vx[9], vw[15];
    int xst = (lw >> 1) + 1;
    auto rot = [&](int st) { const int k = st + r0; return k >= 10 ? k - 10 : k; };
    auto issue_x = [&](u32 slot, bool halo) {
      const char* p = X0 + (rot(xst) * 128 + xpar * 1280);
      glds4(p, vx[0], vx[1], vx[2], vx[3], slot + xpar * 8192); glds4(p, vx[4], vx[5], vx[6], vx[7], slot + xpar * 8192 + 4096);
      if (halo) glds(p, vx[8], slot + 16384 + xpar * 1024);
    };
    auto setup = [&](bool forB) {
      relane();
      const u32 ox0 = (lane >> 3) * 2560 + (((lane & 7) ^ (lane >> 4)) << 4);
      vx[0] = ox0 + xo; vx[1] = (ox0 ^ 64) + xo + 20480 - 1024; vx[2] = vx[0] + 2 * (20480 - 1024); vx[3] = vx[1] + 2 * (20480 - 1024);
#pragma unroll
      for (int p = 0; p < 4; ++p) vx[4 + p] = vx[p] + 64 * 1280;
      vx[8] = ox0 ^ (xpar << 6);
      u32 ow[5];
      w_offsets(lane, ow);
#pragma unroll
      for (int p = 0; p < 15; ++p) { if (forB || p < 10) { vw[p] = ow[p % 5] + (p / 5) * 1280 - (wpos(p) - wpos(p & ~3)); PIN(vw[p]); } }
    };
    const char* Wt = (isB ? WB : WA) + hg * 160;
    auto wsrc = [&](int u) { return Wt + (rot(u >> 1) * 64 + (u & 1) * 32) * 3840; };
    const u32 cw = lds0 + L_CNT + (lw + 1) * 4;
    const bool pjob = job >= 0;
    auto pack_flag = [&]() {
      lds_spin<1>(lds0 + L_CNT + 20, 4);
      const u64 t = *(LAS u64*)(size_t)(lds0 + L_CTL) + TAG_STEP;
      if (lane == 0) __hip_atomic_store((GAS u64*)(ws + WS_PFLAG + job * 8), t, RLX_AGENT);
    };
    auto first_done = [&]() {
      if (lw) {
        if (pjob) waitv<8>(); else waitv<0>();
        stw(cw, 4);
      }
    };
    auto pack_store = [&]() {
      {
        int jn = job;
        asm volatile("" : "+s"(jn));
        const int cg = (jn * 205) >> 13, ks = jn - cg * 40, k20 = ks >= 20 ? ks - 20 : ks, sg = (k20 * 52) >> 8;
        const u32 a = jl + (4 * g + q4) * 256 + p4 * 8;
        u32x4 fr[8];
#pragma unroll
        for (int f = 0; f < 8; ++f) fr[f] = ldB(a + f * 32, a + f * 32 + 4096);
        const u32 d = WS_WOP + (((ks < 20 ? 0 : 2) + (sg & 1)) * 10 + (sg >> 1) * 5 + k20 - sg * 5) * 5120 + cg * 8192;
#pragma unroll
        for (int f = 0; f < 8; ++f) gst128(wsr, lane * 16 + (d + f * 1024 + (((cg * 8 + f) * 52) >> 8) * (39 * 5120)), fr[f]);
      }
    };
    if (isB) {
      setup(true);
      const u32 wd = lds0 + L_BW + ((lw + 1) & 3) * BW_SLOT, da = lds0 + L_DONE + (lane & 3) * 4;
      u32 xs = lds0 + L_BX1 + (lw >> 1) * BX_SLOT;
      first_done();
#pragma nounroll
      for (int u = lw + 5; u < 20; u += 4) {
        xst += 2; xs = lds0 + (xs == lds0 + L_BX0 ? L_BX2 : (xs == lds0 + L_BX2 ? L_BX1 : L_BX0));
        const char* wp = wsrc(u);
        asm volatile("" : "+s"(wp));
        lds_spin<1>(da, u - 3);
        gldsw<0>(wp, vw, wd); gldsw<4>(wp, vw, wd); gldsw<8>(wp, vw, wd); gldsw<12>(wp, vw, wd);
        if (u < 19) issue_x(xs, true);
        if (pjob && u < 9) {
          waitv<24>(); pack_store(); waitv<8>(); stw(lds0 + L_CNT + u * 4, 4); waitv<0>(); pack_flag();
        } else { waitv<0>(); stw(lds0 + L_CNT + u * 4, 4); }
      }
      if (lw == 3) wo_wait();
      lds_spin<1>(lds0 + L_CNT + ((lane & 1) ? 18 : 23) * 4, 4);
      out_touch(lw);
      wo_issue(3 - lw, 7, 4);
      u32x4 park[25];
#pragma unroll
      for (int n = 0; n < 25; ++n)
        park[n] = __builtin_amdgcn_raw_buffer_load_b128(wsr, lane * 16 + (n & 3) * 1024, WS_WOP + (ctile * 8 + 4 + lw) * CHUNK + (n >> 2) * 4096, 16);
      lds_spin<1>(da, 18);
      wo_issue(7 + lw, 20, 4);
      __builtin_amdgcn_s_setprio(0);
      wo_post(3 - lw, 7, 4); wo_post(7 + lw, 20, 4, true);
      lds_spin<1>(lds0 + L_PUB, 4);
      {
        const u32 a = wreg(2 + (lw >> 1)) + (lw & 1) * CHUNK + lane * 16;
#pragma unroll
        for (int n = 0; n < 25; ++n) *(LAS u32x4*)(size_t)(a + n * 1024) = park[n];
        fence_c(); stw(lds0 + L_WC + (4 + lw) * 4, 5);
      }
    } else {
      setup(false);
      u32 wd = lds0 + L_AW + (lw + 1) * AW_SLOT;
      const u32 da = lds0 + L_DONE + (lane & 7) * 4;
      u32x4 xr[20][2];
      u32 xa0 = lds0 + L_AX + lw * 4096 + i16 * 128 + ((g ^ ((i16 >> 1) & 7)) << 4), xa1 = xa0 ^ 64;
      PIN(xa0); PIN(xa1);
      auto take = [&](int st) {
        lds_spin<1>(lds0 + L_CNT + (st ? 2 * st - (lane & 1) : 0) * 4, 4);
        const u32 o = (st & 3) * AX_SLOT;
        xr[2 * st][0] = ld128(xa0 + o); xr[2 * st][1] = ld128(xa0 + o + 2048);
        xr[2 * st + 1][0] = ld128(xa1 + o); xr[2 * st + 1][1] = ld128(xa1 + o + 2048);
        fence_c(); stw(lds0 + L_DONE + wave * 4, st == 9 ? 30 : 2 * st + 3);
      };
      auto wq = [&](int n) { const char* p = wsrc(n); glds4(p, vw[0], vw[1], vw[2], vw[3], lds0 + n * 5120); glds(p, vw[4], lds0 + n * 5120 + 4096); };
      stw(lds0 + L_DONE + wave * 4, 1);
#pragma unroll
      for (int j = 0; j < 5; ++j) {
        const int u = 4 * j + lw + 1;
        if (j >= 1) take(2 * j - 2);
        if (j > 0 && (j < 4 || lw < 3)) {
          const char* wp = wsrc(u) + 1280;
          asm volatile("" : "+s"(wp));
          lds_spin<1>(da, u - 5);
          gldsw<0>(wp, vw, wd); gldsw<4>(wp, vw, wd);
          asm volatile("s_add_u32 m0, %3, %4\n\ts_nop 0\n\tglobal_load_lds_dwordx4 %0, %2\n\tglobal_load_lds_dwordx4 %1, %2 offset:1024"
                       ::"v"(vw[8]), "v"(vw[9]), "s"(wp), "s"(wd), "i"(wpos(8)) : "scc");
          if (u < 19) issue_x(lds0 + L_AX + (xst & 3) * AX_SLOT, false);
        }
        xst += 2; wd = wd >= lds0 + L_AW + 2 * AW_SLOT ? wd - 2 * AW_SLOT : wd + 4 * AW_SLOT;
        if (j >= 1) take(2 * j - 1);
        if (j == 0) first_done();
        else { waitv<0>(); stw(cw + j * 16, 4); }
      }
      take(8); take(9);
      wq(lw);
      lds_spin<1>(da, 16);
      wq(12 + lw);
      if (lw == 3) wo_wait(); else wq(lw < 2 ? 16 + lw : 4);
      out_touch(lw);
      lds_spin<1>(da, 20);
      wq(5 + lw); wq(lw < 3 ? 9 + lw : 18);
      if (lw == 0) wq(19);
      lds_spin<1>(lds0 + L_CNT + 23 * 4, 4);
      wo_issue(3 - lw, 10, 4);
      __builtin_amdgcn_s_setprio(0);
      relane();
      if (lw < 2) waitv<10>(); else waitv<15>();
      lds_arrive(lds0 + L_CNT + 21 * 4, lane);
      float gate[2];
      u32x2 pq[2][5];
      {
        f32x4 qa[2][5];
        float pe[4], pm[4];
        auto phi_step = [&](auto F, auto Y) {
          constexpr int y = Y.v, r = y / 18, k = y % 18, j = k & 3;
          if constexpr (y >= 0 && y < 36) {
            if constexpr (k < 4) { pe[j] = __builtin_fmaf(qa[r][F.v][j], 1.44269504089f, -1.44269504089e-6f); PIN(pe[j]); }
            else if constexpr (k < 8) { pe[j] = __builtin_amdgcn_fmed3f(__builtin_amdgcn_exp2f(pe[j]), 0.f, 1.f); PIN(pe[j]); }
            else if constexpr (k < 12) { pm[j] = fmx(qa[r][F.v][j], 1e-6f); PIN(pm[j]); }
            else if constexpr (k < 16) { pm[j] += pe[j]; PIN(pm[j]); }
            else { u32 x = pk2(pm[2 * j], pm[2 * j + 1]); PIN(x); pq[r][F.v][j] = x; }
          }
        };
        const u32 c0 = (8 * g + (q4 ^ (4 * (g & 1)))) * 160, c1 = (8 * g + ((4 + q4) ^ (4 * (g & 1)))) * 160;
        u32 q0 = lds0 + c0 + p4 * 8, q1 = q0 + 61440, q2 = lds0 + c1 + p4 * 8, q3 = q2 + 61440, d5 = 32 + p4 * 32;
        PIN(q2); PIN(q3); PIN(d5);
        u32x2 wl;
        PIN(q0); PIN(q1);
        constexpr int D = 6;
        u32x4 w[D + 1];
        __builtin_amdgcn_s_setprio(1);
        sfor<-D, 100>([&](auto T) {
          constexpr int t = T.v, n = t + D, f = t / 20, pf = f - 1;
          if constexpr (n == 0) lds_spin<1>(lds0 + L_CNT + 21 * 4, 8);
          if constexpr (n == 16) wo_post(3 - lw, 10, 4);
          if constexpr (n == 100) { fence_c(); stw(lds0 + L_CNT + (24 + lw) * 4, 4); }
          if constexpr (n % 20 == 0 && n < 100) { qa[0][n / 20] = qa[1][n / 20] = splat(1e-6f); PIN(qa[0][n / 20]); PIN(qa[1][n / 20]); }
          SB();
          if constexpr (t >= 0) mmai(qa[0][f], w[t % (D + 1)], xr[t % 20][0]);
          SB();
          if constexpr (n == 80) { q0 += d5; q1 += d5; q2 += d5; q3 += d5; PIN(q0); PIN(q1); PIN(q2); PIN(q3); }
          if constexpr (n < 100) wl = ldtr((n % 20 >= 12 ? q1 + (n % 20 - 12) * 5120 : q0 + (n % 20) * 5120) + (n < 80 ? n / 20 * 40 : 0));
          if constexpr (t == 2 || t == 3) gate[t & 1] = *(LAS float*)(size_t)(lds0 + L_GL + (lw * 2 + (t & 1)) * 64 + (lane & 15) * 4);
          if constexpr (t == 10 || t == 14) { gate[t >> 2 & 1] = __builtin_amdgcn_rcpf(1.f + __builtin_amdgcn_exp2f(gate[t >> 2 & 1] * -1.44269504089f)); PIN(gate[t >> 2 & 1]); }
          if constexpr (t >= 20) phi_step(IC<pf>{}, IC<2 * (t % 20) - 4>{});
          SB();
          if constexpr (t >= 0) mmai(qa[1][f], w[t % (D + 1)], xr[t % 20][1]);
          SB();
          if constexpr (n < 100) w[n % (D + 1)] = cat(wl, ldtr((n % 20 >= 12 ? q3 + (n % 20 - 12) * 5120 : q2 + (n % 20) * 5120) + (n < 80 ? n / 20 * 40 : 0)));
          if constexpr (t >= 20) phi_step(IC<pf>{}, IC<2 * (t % 20) - 3>{});
        });
        mma_done();
        pq[0][4] = pk4(phi4(qa[0][4])); pq[1][4] = pk4(phi4(qa[1][4]));
      }
      relane();
      u32 ta[4][4];
      {
        const u32 tb = lds0 + L_TAB, tz = tb + 105 * 8, t0 = tb + (64 + i16) * 8, t1 = tb + (80 + p4 + 4 * g) * 8, t2 = tb + (100 + p4) * 8;
#pragma unroll
        for (int h = 0; h < 4; ++h) {
          const bool gh = __builtin_amdgcn_inverse_ballot_w64(0xFFFFull << (16 * h)), eh = __builtin_amdgcn_inverse_ballot_w64(0x000F000F000F000Full << (4 * (h ^ 2)));
          ta[h][0] = gh ? t0 : tz; ta[h][1] = eh ? t1 : tz; ta[h][2] = eh && gh ? t2 : tz; ta[h][3] = gh ? tz - 8 : tz;
#pragma unroll
          for (int i = 0; i < 4; ++i) PIN(ta[h][i]);
        }
      }
      u32x4 bo[2][4];
#pragma unroll
      for (int r = 0; r < 2; ++r)
#pragma unroll
        for (int h = 0; h < 4; ++h) { bo[r][h] = cat(pq[r][h], pq[r][4]); PIN(bo[r][h]); }
      const u32 sb = WS_O + (rt * 8 + lw * 2) * 40960 + 20480 + (hg >> 1) * 5120;
      u32 tm = lds0 + L_TAB + lane * 8, tz = lds0 + L_TAB + (96 + g) * 8, s16[2], s5[2];
      s16[0] = sb + (hg & 1) * 2048 + lane * 16; s16[1] = s16[0] + 40960;
      s5[0] = ((g & 1) << 31) + sb + 4096 + ((2 * (hg & 1) + ((g >> 1) ^ 1)) * 16 + i16) * 16; s5[1] = s5[0] + 40960;
      u32 fo = WS_AFLAG + ((rt * 4 + lw) * 2 + (hg >> 2)) * 128 + (hg & 3) * 8;
      f32x4 eps4 = splat(1e-6f);
      tag = *(LAS u64*)(size_t)(lds0 + L_CTL) + TAG_STEP;
      PIN(tm); PIN(tz); PIN(s16[0]); PIN(s16[1]); PIN(s5[0]); PIN(s5[1]); PIN(eps4); PIN(tag);
      asm volatile("" : "+s"(fo));
      gst32(wsr, lane < 32 ? WS_GATE + (hg * 128 + rt * 8 + lw * 2) * 64 + lane * 4 : OOB, asu(lane < 16 ? gate[0] : gate[1]));
      __builtin_amdgcn_s_setprio(3);
      lds_spin<0>(lds0 + L_CNT + (28 + (lane & 3)) * 4, 4);
      {
        u32x4 az[4], a0[4], a5[4];
#pragma unroll
        for (int h = 0; h < 4; ++h) az[h] = cat(ld64v(tz + h * TAB_STRIDE), ld64(ta[h][3] + h * TAB_STRIDE));
#pragma unroll
        for (int h = 0; h < 4; ++h) a0[h] = cat(ld64v(tm + h * TAB_STRIDE), ld64(ta[h][0] + h * TAB_STRIDE));
#pragma unroll
        for (int h = 0; h < 4; ++h) a5[h] = cat(ld64(ta[h][1] + h * TAB_STRIDE), ld64(ta[h][2] + h * TAB_STRIDE));
        SB();
        f32x4 den[2][4], num[2][4], n5[2] = {};
#pragma unroll
        for (int h = 0; h < 4; ++h) { den[0][h] = mma(az[h], bo[0][h], eps4); den[1][h] = mma(az[h], bo[1][h], eps4); }
        SB();
        float sc[2][4], sc5[2] = {0.f, 0.f};
#pragma unroll
        for (int h = 0; h < 4; ++h)
#pragma unroll
          for (int r = 0; r < 2; ++r) { num[r][h] = mma(a0[h], bo[r][h], splat(0.f)); SB(); sc[r][h] = gate[r] * __builtin_amdgcn_rcpf(den[r][h][0]); SB(); }
        u32x2 ob[2][4];
#pragma unroll
        for (int h = 0; h < 4; ++h)
#pragma unroll
          for (int r = 0; r < 2; ++r) {
            n5[r] = mma(a5[h], bo[r][h], n5[r]); SB();
            ob[r][h] = pk4(num[r][h] * sc[r][h]);
            sc5[r] = __builtin_amdgcn_inverse_ballot_w64(0xFFFFull << (16 * (h ^ 2))) ? sc[r][h] : sc5[r];
            if (h & 1) gst128(wsr, s16[r] + (h >> 1) * 1024, cat(ob[r][h - 1], ob[r][h]));
            SB();
          }
#pragma unroll
        for (int r = 0; r < 2; ++r) {
          gst128(wsr, s5[r], pair_rows(pk4(n5[r] * sc5[r])));
        }
      }
      fence_c(); lds_arrive(lds0 + L_PUB, lane);
      publish(ws, fo, tag, lane);
    }
    if (b == 0 && lw == 0) {
      lds_spin<4>(lds0 + L_D2 + (lane & 3) * 4, 5);
      const u32 fo = ((lane >> 3) * 8 + ((lane >> 2) & 1)) * 128;
#pragma nounroll
      for (u32 spins = 0; spins < SPIN_CAP; ++spins) {
        bool ok = true;
#pragma unroll
        for (int r = 0; r < 2; ++r) {
          ok &= __hip_atomic_load((GAS u64*)(ws + WS_BFLAG + fo + r * 64 * 128 + (lane & 3) * 16), RLX_AGENT) == tag;
          ok &= __hip_atomic_load((GAS u64*)(ws + WS_AFLAG + fo + r * 64 * 128 + (lane & 3) * 8), RLX_AGENT) == tag;
        }
        if (__all(ok)) break;
        __builtin_amdgcn_s_sleep(8);
      }
      if (lane == 0) __hip_atomic_store((GAS u64*)(ws + WS_EPOCH), tag, RLX_AGENT);
    }
    asm volatile(".Lpf_end:");
    return;
  }

  auto get_tag = [&]() {
    const u32x2 t = ld64(lds0 + L_CTL);
    tag = ((u64)(u32)__builtin_amdgcn_readfirstlane(t[1]) << 32) | (u32)__builtin_amdgcn_readfirstlane(t[0]);
    tag += TAG_STEP;
  };
  u32x4 g0[25];
  auto take_g0 = [&]() {
    lds_spin<1>(lds0 + L_WC + (wave >> 1) * 4, 5);
    const u32 a = wreg(0) + (wave >> 1) * CHUNK + lane * 16;
#pragma unroll
    for (int n = 0; n < 25; ++n) g0[n] = ld128(a + n * 1024);
    fence_c(); stw(lds0 + L_D2 + wave * 4, 1);
  };
  auto bflag = [&]() { return WS_BFLAG + ((rg * 2 + (wave & 1)) * 2 + (wave >> 1)) * 128 + (lane & 7) * 8; };
  bool bseen = false;
  u32 ca = lds0 + L_CNT, dcount = 1;
  u32 dn = lds0 + L_DONE + wave * 4;
  PIN(dn);
  if (isB) {
    const int par = wave & 1, hp = wave >> 1;
    u32 bM0 = lds0 + L_BW + rB0 + hp * 80 + p4 * 8, bM1 = bM0 - rB0 + rB1;
    u32 bX0 = lds0 + L_BW + rB0 + hp * 80 + 32 + (p4 & 1) * SEG + (p4 >> 1) * 40, bX1 = bX0 - rB0 + rB1;
    u32 bY0 = lds0 + L_BW + rB0 + 2 * SEG + 32 + p4 * 40, bY1 = bY0 - rB0 + rB1;
    u32 alo = aA0 + par * 8192 + hp * 4096, ahi = aA0 + par * 8192 + (hp ^ 1) * 4096;
    PIN(bM0); PIN(bM1); PIN(bX0); PIN(bX1); PIN(bY0); PIN(bY1); PIN(alo); PIN(ahi); PIN(ca); PIN(dcount);
    const Att at = att_prep(par, hp, hp);
    const Att at2 = att_prep(par, hp, hp ^ 1);
    f32x4 acc[4][7], accy[2], acch[3];
#pragma unroll
    for (int f = 0; f < 3; ++f) { acch[f] = splat(0.f); PIN(acch[f]); }
#pragma unroll
    for (int r = 0; r < 4; ++r)
#pragma unroll
      for (int f = 0; f < 7; ++f) { acc[r][f] = splat(0.f); PIN(acc[r][f]); }
    accy[0] = accy[1] = splat(0.f); PIN(accy[0]); PIN(accy[1]);
    struct Fr { u32x4 a[4]; u32x2 w[8][2]; };
    u32 xs = lds0 + L_BX0, xs1 = lds0 + L_BX1, xs2 = lds0 + L_BX2, x0 = xs + alo, x1 = xs + ahi;
    const u32 hoff = 16384 - par * 8192 - hp * 4096;
    u32x4 ah[2];
    auto rd1 = [&](Fr& F, int sl, int n) {
      const u32 o = sl * BW_SLOT;
      if (n < 4) { F.a[n] = ld128((n < 2 ? x0 : x1) + (n & 1) * 2048); return; }
      const int f = (n - 4) >> 1, t = n & 1;
      if (f == 4) F.w[4][t] = ldtr((t ? bX1 : bX0) + o);
      else if (f == 5) F.w[7][t] = ldtr((t ? bY1 : bY0) + o);
      else { const int ff = f < 4 ? f : f - 2; F.w[f < 4 ? f : f - 1][t] = ldtr((t ? bM1 : bM0) + o + (ff >> 1) * SEG + (ff & 1) * 40); }
    };
    auto mm1 = [&](Fr& F, int n) {
      if (n >= 20 && n < 22) { mmai(accy[n - 20], F.a[n - 20], cat(F.w[7][0], F.w[7][1])); return; }
      const int m = n < 20 ? n : n - 2, f = m >> 2, r = m & 3;
      const u32x4 w = cat(F.w[f][0], F.w[f][1]);
      if (f < 5) mmai(acc[r][f], w, F.a[r]); else mmai(acc[r][f], F.a[r], w);
    };
    Fr F0, F1;
    waitv<0>(); lds_arrive(lds0 + L_CNT, lane);
    lds_spin<0>(ca, 4);
#pragma unroll
    for (int n = 0; n < 20; ++n) rd1(F0, 0, n);
    ah[0] = ld128(x0 + hoff);
    fence_c(); stw(dn, dcount);
    bool ok = false;
    x0 = xs + (alo ^ 64); x1 = xs + (ahi ^ 64);
    auto unit = [&](Fr& Fc, Fr& Fn, int k, bool last) {
      u32 cv;
#pragma unroll
      for (int n = 0; n < 30; ++n) {
        SB(); mm1(Fc, n); SB();
        if (n == 3) {
          asm volatile("s_cmp_lg_u32 %10, 0\n\ts_cbranch_scc1 .Lhv%=\n\t"
                       "v_mfma_f32_16x16x32_bf16 %0, %4, %3, %0\n\tv_mfma_f32_16x16x32_bf16 %1, %5, %3, %1\n\tv_mfma_f32_16x16x32_bf16 %2, %6, %3, %2\n\t"
                       "s_branch .Lhe%=\n.Lhv%=:\n\t"
                       "v_mfma_f32_16x16x32_bf16 %0, %3, %7, %0\n\tv_mfma_f32_16x16x32_bf16 %1, %3, %8, %1\n\tv_mfma_f32_16x16x32_bf16 %2, %3, %9, %2\n.Lhe%=:"
                       : "+v"(acch[0]), "+v"(acch[1]), "+v"(acch[2])
                       : "v"(ah[k & 1]), "v"(cat(Fc.w[2][0], Fc.w[2][1])), "v"(cat(Fc.w[3][0], Fc.w[3][1])), "v"(cat(Fc.w[4][0], Fc.w[4][1])),
                         "v"(cat(Fc.w[5][0], Fc.w[5][1])), "v"(cat(Fc.w[6][0], Fc.w[6][1])), "v"(cat(Fc.w[7][0], Fc.w[7][1])), "s"(par) : "scc");
          SB();
          if (!last) lds_await<0>(ok, ca + (k + 1) * 4, 4);
        }
        if (n == 19) cv = ldw(ca + (k + 2) * 4);
        if (n >= 4 && n < 24) rd1(Fn, (k + 1) & 3, n - 4);
        if (n == 10) ++dcount;
        if (n == 24) ah[(k + 1) & 1] = ld128(x0 + hoff);
        if (n == 25) { fence_c(); stw(dn, dcount); }
        if (n == 28) ok = __all(cv >= 4);
        if (n == 0 && !(k & 1)) { const u32 t_ = xs; xs = xs1; xs1 = xs2; xs2 = t_; asm volatile("" : "+s"(xs), "+s"(xs1), "+s"(xs2)); }
        if (n == 26) { x0 = (k & 1) ? x0 ^ 64 : xs + alo; PIN(x0); }
        if (n == 27) { x1 = (k & 1) ? x1 ^ 64 : xs + ahi; PIN(x1); }
      }
      SB();
    };
#pragma nounroll
    for (int it = 0; it < 5; ++it) {
      unit(F0, F1, 0, false); unit(F1, F0, 1, false); unit(F0, F1, 2, false); unit(F1, F0, 3, it == 4);
      ca += 16;
    }
    mma_done();
    relane();
    u32x2 P[2][8], P2[2][8];
    {
      const u32 dd = lds0 + L_DONE + (lane & 3) * 4;
      const int dv = ldw(dd);
#pragma unroll
      for (int f = 2; f < 7; ++f) { P[1][f] = pk4(acc[1][f]); P2[1][f] = pk4(acc[3][f]); PIN(P[1][f]); PIN(P2[1][f]); }
      lds_await<0>(__all(dv >= 20), dd, 20);
      const u32 hl = lds0 + L_HO + wave * HO_STRIDE + lane * 8, yl = lds0 + L_YX + (par * 4 + 2 * hp) * 512 + lane * 8;
#pragma unroll
      for (int f = 0; f < 3; ++f) st64(hl + (5 + f) * 512, pk4(acch[f]));
      st64(yl, pk4(accy[0])); st64(yl + 512, pk4(accy[1]));
      fence_c(); stw(dn, 40);
#pragma unroll
      for (int f = 2; f < 7; ++f) st64(hl + (f - 2) * 512, hp ? P2[1][f] : P[1][f]);
#pragma unroll
      for (int f = 0; f < 7; ++f) { P[0][f] = pk4(acc[0][f]); PIN(P[0][f]); }
      P[1][0] = pk4(acc[1][0]); P[1][1] = pk4(acc[1][1]);
      lds_spin<0>(dd, 40);
    }
    attention(P, at);
#pragma unroll
    for (int f = 0; f < 7; ++f) P2[0][f] = pk4(acc[2][f]);
    P2[1][0] = pk4(acc[3][0]); P2[1][1] = pk4(acc[3][1]);
    attention(P2, at2);
    const u64 bv = __hip_atomic_load((GAS u64*)(ws + bflag()), RLX_AGENT);
    take_g0();
    bseen = __all(bv == tag);
    waitv<0>();
    if (lane < 2) __hip_atomic_store((GAS u64*)(ws + ((lane ? at2.fo : at.fo) & 0xFFFFFF)), tag, RLX_AGENT);
    fence_c(); lds_arrive(lds0 + L_PUB, lane);
    lds_spin<1>(lds0 + L_PUB, 4);
  } else {
    const int h = wave;
    u32 bK0 = lds0 + L_AW + rB0 + h * 40 + p4 * 8, bK1 = bK0 - rB0 + rB1;
    const bool zk = (p4 >> 1) == (h >> 1);
    u32 bZ0 = lds0 + L_AW + rB0 + (zk ? p4 : p4 ^ 2) * 40 + 32 + (zk ? 0 : SEG), bZ1 = bZ0 - rB0 + rB1;
    PIN(bK0); PIN(bK1); PIN(bZ0); PIN(bZ1); PIN(ca); PIN(dcount);
    f32x4 acc[8][2], accz[4];
#pragma unroll
    for (int r = 0; r < 8; ++r) { acc[r][0] = splat(1e-6f); acc[r][1] = splat(0.f); PIN(acc[r][0]); PIN(acc[r][1]); }
#pragma unroll
    for (int r = 0; r < 4; ++r) { accz[r] = splat((q4 >> 1) == (h >> 1) ? 1e-6f : 0.f); PIN(accz[r]); }
    struct Fr { u32x4 a[8], gw; u32x2 w[3][2]; };
    f32x4 accg[2] = {};
    PIN(accg[0]); PIN(accg[1]);
    const u32 bsw = ((h & 1) * 4 + (h >> 1) * 2) * 2048;
    u32 xs = lds0 + L_AX + AX_SLOT, xa = aA0 + bsw, xr4[4], ga = lds0 + L_GATEW + r0 * 128 + g * 16;
#pragma unroll
    for (int i = 0; i < 4; ++i) xr4[i] = lds0 + L_AX + (xa ^ (i * 4096));
    PIN(ga);
    u32 wa = 2 * AW_SLOT, wb = 4 * AW_SLOT, wc = 0, k0 = bK0, k1 = bK1, z0_ = bZ0, z1_ = bZ1;
    PIN(xa);
    auto rd1 = [&](Fr& F, int sl, int n) {
      if (n >= 6) { F.a[n - 6] = ld128(xr4[(n - 6) >> 1] + (n & 1) * 2048); return; }
      const int f = n >> 1, t = n & 1;
      F.w[f][t] = f < 2 ? ldtr((t ? k1 : k0) + sl * AW_SLOT + f * SEG) : ldtr((t ? z1_ : z0_) + sl * AW_SLOT);
    };
    Fr F0, F1;
    waitv<0>(); lds_arrive(lds0 + L_CNT, lane);
    lds_spin<0>(ca, 4);
#pragma unroll
    for (int n = 0; n < 14; ++n) rd1(F0, 0, n);
    F0.gw = ld128(ga);
    fence_c(); stw(dn, dcount);
    bool ok = false;
#pragma unroll
    for (int i = 0; i < 4; ++i) xr4[i] ^= 64;
    auto unit = [&](Fr& Fc, Fr& Fn, int k, bool last) {
      u32 cv;
#pragma unroll
      for (int n = 0; n < 22; ++n) {
        const int r = n < 12 ? n / 3 : 4 + (n - 12) / 2, f = n < 12 ? n % 3 : n & 1;
        SB();
        if (n < 20) mmai(f < 2 ? acc[r][f] : accz[r & 3], Fc.a[r], cat(Fc.w[f][0], Fc.w[f][1])); else mmai(accg[n - 20], Fc.gw, Fc.a[n - 20]);
        SB();
        if (n == 1 && !last) lds_await<0>(ok, ca + (k + 1) * 4, 4);
        if (n >= 1 && n < 13) rd1(Fn, ~k & 1, (n & 1) ? (n - 1) / 2 : 6 + (n - 2) / 2);
        if (n == 13) cv = ldw(ca + (k + 2) * 4);
        if (n == 14 || n == 16) rd1(Fn, 0, 12 + (n - 14) / 2);
        if (n == 15) Fn.gw = ld128(ga + (k + 1) * 64);
        if (n == 9) { ++dcount; PIN(dcount); }
        if (n == 17) { fence_c(); stw(dn, dcount); }
        if (k & 1) {
          if (n == 13 || n == 15 || n == 17 || n == 18) { const int i = n == 18 ? 3 : (n - 13) / 2; xr4[i] ^= 64; PIN(xr4[i]); }
          if (n == 7) { xs = xs == lds0 + L_AX + 3 * AX_SLOT ? lds0 + L_AX : xs + AX_SLOT; asm volatile("" : "+s"(xs)); }
          if (n == 19) { const u32 t_ = wa; wa = wb; wb = wc; wc = t_; asm volatile("" : "+s"(wa), "+s"(wb), "+s"(wc)); }
        } else {
          if (n == 12) { k0 = bK0 + wa; PIN(k0); }
          if (n == 14) { k1 = bK1 + wa; PIN(k1); }
          if (n == 16) { z0_ = bZ0 + wa; PIN(z0_); }
          if (n == 13 || n == 15 || n == 17 || n == 18) { const int i = n == 18 ? 3 : (n - 13) / 2; xr4[i] = (xa ^ (i * 4096)) + xs; PIN(xr4[i]); }
          if (n == 19) { z1_ = bZ1 + wa; PIN(z1_); }
        }
        if (n == 20) ok = __all(cv >= 4);
      }
      SB();
    };
#pragma nounroll
    for (int it = 0; it < 5; ++it) {
      unit(F0, F1, 0, false); unit(F1, F0, 1, false); unit(F0, F1, 2, false); unit(F1, F0, 3, it == 4);
      ca += 16; ga += 256;
    }
    mma_done();
    relane(); get_tag();
    __builtin_amdgcn_s_setprio(2);
    const bool eh = q4 == (h ^ 2), gh = g == h, zc = i16 == 4 * h;
    const u32x4 ones = {0x3F803F80u, 0x3F803F80u, 0x3F803F80u, 0x3F803F80u};
    u32x4 cz[4];
    {
      const bool kcol = (q4 >> 1) == (h >> 1);
      const u32 xi = lds0 + L_TAB + (g * 8 + (q4 >> 1) * 4 + p4) * 16;
#pragma unroll
      for (int k = 0; k < 2; ++k) {
        u32x4 pz = cat(pk4(phi4(accz[2 * k])), pk4(phi4(accz[2 * k + 1]))), zz = cat(pk4(accz[2 * k]), pk4(accz[2 * k + 1]));
        PIN(pz); PIN(zz);
        cz[k] = kcol ? pz : zz;
      }
      const u32 xw = (q4 & 1) != (h & 1) ? xi + (h ^ 1) * TAB_STRIDE : xi + 4 * TAB_STRIDE;
      *(LAS u32x4*)(size_t)xw = cz[0]; *(LAS u32x4*)(size_t)(xw + 512) = cz[1];
      *(LAS float*)(size_t)(lds0 + L_GL + ((h & 1) * 4 + (h >> 1) * 2) * 64 + (lane & 31) * 4) = (g & 1) ? accg[1][0] : accg[0][0];
      fence_c(); stw(dn, 40); SB();
    }
    f32x4 t00 = {}, t01 = {}, t10 = {}, t11 = {};
    u32x4 ek[4], rk[4], vv[4];
    auto zpart = [&](int k) {
      const u32x4 zb = eh ? cz[k] : ones;
      t01 = mma(ek[k], zb, mma(rk[k], zb, t01)); t10 = mma(cz[k], vv[k], t10); t11 = mma(cz[k], zb, t11);
    };
#pragma unroll
    for (int k = 0; k < 4; ++k) {
      ek[k] = cat(pk4(exp4(acc[2 * k][0])), pk4(exp4(acc[2 * k + 1][0])));
      rk[k] = relu8(cat(pk4(acc[2 * k][0]), pk4(acc[2 * k + 1][0])));
      vv[k] = cat(pk4(acc[2 * k][1]), pk4(acc[2 * k + 1][1]));
      t00 = mma(ek[k], vv[k], mma(rk[k], vv[k], t00));
      if (k < 2) zpart(k);
    }
    PIN(t00); PIN(t01); PIN(t10); PIN(t11);
    {
      lds_spin<0>(lds0 + L_DONE + (h ^ 1) * 4, 40);
      const u32 xi = lds0 + L_TAB + h * TAB_STRIDE + (g * 8 + (q4 >> 1) * 4 + p4) * 16;
      cz[2] = ld128(xi); cz[3] = ld128(xi + 512);
      zpart(2); zpart(3);
    }
    lds_arrive(lds0 + L_CNT + 21 * 4, lane);
    const u32 pb = WS_KVP + (hg * 4 + h) * 16 * KVP_STRIDE, tb = lds0 + L_TAB + h * TAB_STRIDE;
    st64(tb + lane * 8, pk4(t00));
    st64(tb + (gh ? 64 + i16 : 106) * 8, pk4(t10));
    st64(tb + (eh ? 80 + p4 + 4 * g : (zc ? 96 + g : 106)) * 8, pk4(t01));
    st64(tb + (gh && eh ? 100 + p4 : (gh && zc ? 104 : 105)) * 8, pk4(t11));
    const u32 po = lane < 53 ? pb + lane * 16 : OOB;
    gst128(wsr, (lane < 56 ? pb + lane * 16 : OOB) + rt * KVP_STRIDE, ld128(tb + lane * 16));
    publish(ws, WS_KVFLAG + (hg * 4 + h) * 128 + rt * 8, tag, lane);
    __builtin_amdgcn_s_setprio(3);
    take_g0();
    lds_spin<1>(lds0 + L_D2 + (lane & 3) * 4, 1);
    wo_issue(10 + h, 20, 4);
    await(ws, WS_KVFLAG + (hg * 4 + h) * 128 + i16 * 8, tag);
    {
      u32x4 vm[16];
#pragma unroll
      for (int r = 0; r < 16; ++r) vm[r] = __builtin_amdgcn_raw_buffer_load_b128(wsr, po, r * KVP_STRIDE, 16);
      float sm[8] = {};
      u32 sel0 = 0x00003F80u, sel1 = 0x3F800000u;
      PIN(sel0); PIN(sel1);
#pragma unroll
      for (int r = 0; r < 16; ++r) {
#pragma unroll
        for (int e = 0; e < 8; ++e)
          sm[e] = __builtin_amdgcn_fdot2_f32_bf16(__builtin_bit_cast(bf16x2, (u32)(vm[r][e >> 1] + 0u)), __builtin_bit_cast(bf16x2, (e & 1) ? sel1 : sel0), sm[e], false);
        SB();
      }
      const u32 keep = lane == 52 ? 0u : ~0u;
      *(LAS u32x4*)(size_t)(tb + lane * 16) = u32x4{pk2(sm[0], sm[1]), pk2(sm[2], sm[3]), pk2(sm[4], sm[5]) & keep, pk2(sm[6], sm[7]) & keep};
    }
    fence_c(); stw(lds0 + L_CNT + (28 + h) * 4, 4);
    __builtin_amdgcn_s_setprio(0);
    wo_post(10 + h, 20, 4);
    lds_spin<1>(lds0 + L_CNT + (24 + (lane & 3)) * 4, 4);
    wo_issue(20 + h, 40, 4);
    lds_spin<2>(lds0 + L_PUB, 4);
  }

  {
    const int rp = wave & 1, kh = wave >> 1;
    const int rt2 = rg >> 1, par2 = rg & 1;
    const u32 blk0 = rg * 4 + rp * 2;
    f32x4 acc[2][5];
#pragma unroll
    for (int r = 0; r < 2; ++r)
#pragma unroll
      for (int c = 0; c < 5; ++c) { acc[r][c] = splat(0.f); PIN(acc[r][c]); }
    u32x4 ofb[10][2], ofa[10][2];
    float gt[2];
    const u32 ob[2] = {WS_O + (blk0 + kh) * 40960 + kh * 10240, WS_O + (blk0 + (kh ^ 1)) * 40960 + kh * 10240};
    const u32 gb[2] = {WS_GATE + (kh * 4 * 128 + blk0 + kh) * 64, WS_GATE + (kh * 4 * 128 + blk0 + (kh ^ 1)) * 64};
    auto ldo = [&](u32 v, int r, int n) { return __builtin_amdgcn_raw_buffer_load_b128(wsr, v + (n & 3) * 1024, ob[r] + (n >> 2) * 4096, 16); };
    auto ldg = [&](u32 v, int r) { return asf(__builtin_amdgcn_raw_buffer_load_b32(wsr, v, gb[r], 16)); };
    const u32 l16 = lane * 16;
    auto load_a = [&]() {
#pragma unroll
      for (int r = 0; r < 2; ++r) gt[r] = ldg(i16 * 4, r);
#pragma unroll
      for (int i = 0; i < 10; ++i) ofa[i >> 1][i & 1] = ldo(l16, i & 1, 20 + (i >> 1));
    };
    const u32 fa = WS_AFLAG + ((rt2 * 4 + par2 * 2 + rp) * 2 + kh) * 128 + (lane & 3) * 8;
    if (!bseen) await(ws, bflag(), tag);
    fence_c();
    if (!isB) wo_post(20 + wave, 40, 4);
    u64 av;
#pragma unroll
    for (int n = 0; n < 5; ++n) {
#pragma unroll
      for (int r = 0; r < 2; ++r) ofb[n][r] = ldo(l16, r, n);
    }
    bool early = false;
    u32 oe = OOB, og = OOB, so[3], xw, xr_, tw, trd;
    const u32 xb = wreg(1), blk = blk0 + kh, obase = ((blk >> 3) * 128 + (blk & 3) * 32 + ((blk >> 2) & 1)) * 1280 + ctile * 160;
    {
      constexpr int D = 6;
      u32x4 w[D + 1];
      u32 am = 0;
      sfor<-D, 100>([&](auto T) {
        constexpr int t = T.v, n = t + D, gr = n / 25;
        if constexpr (n < 100 && n > 0 && n % 25 == 0) {
          if constexpr (n == 50) { fence_c(); stw(lds0 + L_D2 + wave * 4, 5); }
          lds_spin<1>(n == 75 && lane < 4 ? lds0 + L_D2 + lane * 4 : lds0 + L_WC + (2 * gr + kh) * 4, 5);
          am = wreg(gr) + kh * CHUNK + lane * 16; PIN(am);
        }
        if constexpr (t == 2) av = __hip_atomic_load((GAS u64*)(ws + fa), RLX_AGENT);
        if constexpr (t == 25) { early = __all(av == tag); fence_c(); oe = early ? l16 : OOB; og = early ? i16 * 4 : OOB; gt[0] = ldg(og, 0); }
        if constexpr (t == 27) gt[1] = ldg(og, 1);
        if constexpr (t >= 0 && (t % 5 == 1 || t % 5 == 3) && t / 5 + 5 < 20) {
          constexpr int m = t / 5 + 5, r = t % 5 >> 1;
          if constexpr (m < 10) ofb[m][r] = ldo(l16, r, m); else ofa[m - 10][r] = ldo(oe, r, 10 + m);
        }
        if constexpr (t >= 80 && t < 83) { const u32 id = (t - 80) * 64 + lane; so[t - 80] = obase + id * 16 + mul24(mul24(id, 6554u) >> 16, 2400u) + (t == 82 ? (lane & 32) << 26 : 0); PIN(so[t - 80]); }
        if constexpr (t == 84) { xw = xb + wave * 5120 + l16; xr_ = xb + (wave ^ 2) * 5120 + l16; PIN(xw); PIN(xr_); }
        if constexpr (t == 85) { trd = xb + 20480 + wave * 2560 + l16; tw = xb + 20480 + wave * 2560 + i16 * 160 + g * 8; PIN(trd); PIN(tw); }
        if constexpr (t == 50) {
          if (!early) { await(ws, fa, tag); load_a(); }
          oe = l16;
          mma_done();
#pragma unroll
          for (int r = 0; r < 2; ++r)
#pragma unroll
            for (int cc = 0; cc < 5; ++cc) { acc[r][cc] *= 1.f - gt[r]; PIN(acc[r][cc]); }
          asm volatile("s_nop 1");
        }
        SB();
        if constexpr (t >= 0) mmai(acc[0][t % 5], t < 25 ? g0[t % 25] : w[t % (D + 1)], (t < 50 ? ofb : ofa)[t / 5 % 10][0]);
        SB();
        if constexpr (n >= 25 && n < 100) w[n % (D + 1)] = ld128(am + (n % 25) * 1024);
        SB();
        if constexpr (t >= 0) mmai(acc[1][t % 5], t < 25 ? g0[t % 25] : w[t % (D + 1)], (t < 50 ? ofb : ofa)[t / 5 % 10][1]);
        SB();
      });
      mma_done();
    }
#pragma unroll
    for (int c = 0; c < 5; ++c) *(LAS f32x4*)(size_t)(xw + c * 1024) = acc[1][c];
    fence_c(); stw(lds0 + L_D2 + wave * 4, 6);
    const rsrc_t outr = __builtin_amdgcn_make_buffer_rsrc(out, 0, 2048 * 1280, 0x00020000);
    lds_spin<0>(lds0 + L_D2 + (wave ^ 2) * 4, 6);
    f32x4 pv[5];
#pragma unroll
    for (int c = 0; c < 5; ++c) pv[c] = *(LAS f32x4*)(size_t)(xr_ + c * 1024);
    SB();
#pragma unroll
    for (int c = 0; c < 5; ++c) st64(tw + c * 32, pk4(acc[0][c] + pv[c]));
#pragma unroll
    for (int i = 0; i < 3; ++i) gst128(outr, so[i], ld128(trd + i * 1024));
  }
}

extern "C" void kernel_launch(void* const* d_in, const int* in_sizes, int n_in, void* d_out, int out_size, void* d_ws, size_t ws_size,
                              hipStream_t stream) {
  (void)hipFuncSetAttribute((const void*)fused_kernel, hipFuncAttributeMaxDynamicSharedMemorySize, LDS_BYTES);
  fused_kernel<<<256, 512, LDS_BYTES, stream>>>((const char*)d_in[0], (const char*)d_in[1], (const char*)d_in[2], (const char*)d_in[4],
                                                (const char*)d_in[5], (const char*)d_in[6], (char*)d_out, (char*)d_ws);
}
```
